```python
import torch

def _softmax(x, axis=-1):
    return torch.softmax(x, dim=axis)
def _gelu(x):
    return torch.nn.functional.gelu(x, approximate="tanh")
def _silu(x):
    return torch.nn.functional.silu(x)
def _sigmoid(x):
    return torch.sigmoid(x)
def _layernorm(x, axis=-1, eps=1e-5):
    m = x.mean(dim=axis, keepdim=True)
    v = x.var(dim=axis, keepdim=True, unbiased=False)
    return (x - m) / torch.sqrt(v + eps)
def _scatter_add(tgt, idx, src, axis=0):
    return tgt.clone().index_add_(axis, idx, src)
def _segment_sum(src, seg, n):
    out = torch.zeros((n,) + src.shape[1:], dtype=src.dtype)
    return out.index_add_(0, seg, src)
def _pad(x, pw):
    flat = tuple(v for pair in reversed(pw) for v in pair)
    return torch.nn.functional.pad(x, flat)

kernel_name = 'parallel_sum__sliding_block__dense_activation_relu__prompt'

def setup_inputs():

    _w = (lambda a: (lambda t: t.to(torch.bfloat16) if t.is_floating_point() else t)(torch.as_tensor(a)))
    def _A_setup():
        B, S, D = 1, 2048, 576
        return {
            "x": _w((lambda *s: torch.randn(*s, dtype=torch.bfloat16) * 0.05)(B, S, D)),
            "wqkv": _w((lambda *s: torch.randn(*s, dtype=torch.bfloat16) * 0.1)(D, 3 * D)),
            "wo": _w((lambda *s: torch.randn(*s, dtype=torch.bfloat16) * 0.05)(D, D)),
        }
    _A_in = _A_setup()

    def _B_setup():
        B, S, D, Dff = 1, 2048, 576, 4608
        return {
            "x": _w((lambda *s: torch.randn(*s, dtype=torch.bfloat16) * 0.05)(B, S, D)),
            "w1": _w((lambda *s: torch.randn(*s, dtype=torch.bfloat16) * 0.05)(D, Dff)),
            "w2": _w((lambda *s: torch.randn(*s, dtype=torch.bfloat16) * 0.05)(Dff, D)),
        }
    _B_in = _B_setup()

    return {
        "A_x": _A_in["x"],
        "A_wqkv": _A_in["wqkv"],
        "A_wo": _A_in["wo"],
        "B_x": _B_in["x"],
        "B_w1": _B_in["w1"],
        "B_w2": _B_in["w2"],
    }

def reference(A_x, A_wqkv, A_wo, B_x, B_w1, B_w2):
    def _A_ref(x, wqkv, wo):
        B, S, D = x.shape
        H = 32; Dh = D // H; W = 256
        qkv = (lambda x, *d: x.permute(*d))((x @ wqkv).reshape(B, S, 3, H, Dh), 2, 0, 3, 1, 4)
        q, k, v = qkv[0], qkv[1], qkv[2]
        scores = torch.einsum("bhqd,bhkd->bhqk", q, k) * (Dh ** -0.5)
        idx = torch.arange(S)
        mask = (idx[:, None] - idx[None, :] >= 0) & (idx[:, None] - idx[None, :] < W)
        scores = torch.where(mask[None, None], scores, -1e9)
        p = _softmax(scores, axis=-1)
        o = torch.einsum("bhqk,bhkd->bhqd", p, v)
        return (lambda x, *d: x.permute(*d))(o, 0, 2, 1, 3).reshape(B, S, D) @ wo

    def _B_ref(x, w1, w2):
        h = torch.einsum("bsd,df->bsf", x, w1)
        zero = torch.zeros_like(h)
        h = torch.maximum(h, zero)
        return torch.einsum("bsf,fd->bsd", h, w2)

    x = A_x
    xn = _layernorm(x, axis=-1)
    return x + _A_ref(xn, A_wqkv, A_wo) + _B_ref(xn, B_w1, B_w2)
```

```cpp
#include <hip/hip_runtime.h>
#include <hip/hip_bf16.h>
typedef unsigned short u16;
static __device__ __forceinline__ float bf2f(u16 b){ return __uint_as_float(((unsigned)b)<<16); }
static __device__ __forceinline__ u16 f2bf(float f){ unsigned u=__float_as_uint(f); u += 0x7fffu + ((u>>16)&1u); return (u16)(u>>16); }
constexpr int S=2048, D=576, H=32, DH=18, W=256, F=4608;

__global__ void k_ln(const u16* x, u16* xn){
  int row=blockIdx.x; int t=threadIdx.x;
  float v[9]; float s=0;
  for(int i=0;i<9;i++){ v[i]=bf2f(x[row*D+t+64*i]); s+=v[i]; }
  for(int o=32;o;o>>=1) s+=__shfl_xor(s,o);
  float m=s/D; float q=0;
  for(int i=0;i<9;i++){ float d=v[i]-m; q+=d*d; }
  for(int o=32;o;o>>=1) q+=__shfl_xor(q,o);
  float r=rsqrtf(q/D+1e-5f);
  for(int i=0;i<9;i++) xn[row*D+t+64*i]=f2bf((v[i]-m)*r);
}
template<int MODE> __global__ void k_gemm(const u16* A,const u16* B,void* C,int M,int N,int K,const u16* x,const float* add){
  __shared__ float As[16][65], Bs[16][65];
  int tx=threadIdx.x&15, ty=threadIdx.x>>4; int bm=blockIdx.y*64,bn=blockIdx.x*64;
  float acc[4][4]={};
  for(int k0=0;k0<K;k0+=16){
    for(int i=threadIdx.x;i<64*16;i+=256){ int r=i>>4,c=i&15; As[c][r]=bf2f(A[(size_t)(bm+r)*K+k0+c]); }
    for(int i=threadIdx.x;i<64*16;i+=256){ int r=i>>6,c=i&63; Bs[r][c]=bf2f(B[(size_t)(k0+r)*N+bn+c]); }
    __syncthreads();
    for(int k=0;k<16;k++){ float a[4],b[4]; for(int i=0;i<4;i++){a[i]=As[k][ty*4+i]; b[i]=Bs[k][tx*4+i];}
      for(int i=0;i<4;i++)for(int j=0;j<4;j++) acc[i][j]+=a[i]*b[j]; }
    __syncthreads();
  }
  for(int i=0;i<4;i++)for(int j=0;j<4;j++){ size_t o=(size_t)(bm+ty*4+i)*N+bn+tx*4+j; float v=acc[i][j];
    if(MODE==0) ((u16*)C)[o]=f2bf(v); else if(MODE==1) ((u16*)C)[o]=f2bf(fmaxf(v,0.f)); else if(MODE==2) ((float*)C)[o]=v; else ((u16*)C)[o]=f2bf(bf2f(x[o])+add[o]+v); }
}
__global__ void k_attn(const u16* qkv,u16* o){
  int q=blockIdx.x*64+threadIdx.x, h=blockIdx.y;
  float qv[DH]; for(int d=0;d<DH;d++) qv[d]=bf2f(qkv[(size_t)q*3*D+h*DH+d])*0.23570226f;
  float m=-1e30f,l=0,acc[DH]={};
  int k0=q-W+1; if(k0<0)k0=0;
  for(int k=k0;k<=q;k++){ float s=0; for(int d=0;d<DH;d++) s+=qv[d]*bf2f(qkv[(size_t)k*3*D+D+h*DH+d]);
    float mn=fmaxf(m,s); float a=__expf(m-mn), p=__expf(s-mn); l=l*a+p; for(int d=0;d<DH;d++) acc[d]=acc[d]*a+p*bf2f(qkv[(size_t)k*3*D+2*D+h*DH+d]); m=mn; }
  for(int d=0;d<DH;d++) o[(size_t)q*D+h*DH+d]=f2bf(acc[d]/l);
}
extern "C" void kernel_launch(void* const* d_in, const int* in_sizes, int n_in, void* d_out, int out_size, void* d_ws, size_t ws_size, hipStream_t stream){
  const u16* x=(const u16*)d_in[0]; const u16* wqkv=(const u16*)d_in[1]; const u16* wo=(const u16*)d_in[2];
  const u16* w1=(const u16*)d_in[4]; const u16* w2=(const u16*)d_in[5];
  char* ws=(char*)d_ws; u16* xn=(u16*)ws; u16* qkv=(u16*)(ws+(4<<20)); u16* o=(u16*)(ws+(12<<20)); float* a=(float*)(ws+(16<<20)); u16* h=(u16*)(ws+(24<<20));
  k_ln<<<S,64,0,stream>>>(x,xn);
  k_gemm<0><<<dim3(3*D/64,S/64),256,0,stream>>>(xn,wqkv,qkv,S,3*D,D,nullptr,nullptr);
  k_attn<<<dim3(S/64,H),64,0,stream>>>(qkv,o);
  k_gemm<2><<<dim3(D/64,S/64),256,0,stream>>>(o,wo,a,S,D,D,nullptr,nullptr);
  k_gemm<1><<<dim3(F/64,S/64),256,0,stream>>>(xn,w1,h,S,F,D,nullptr,nullptr);
  k_gemm<3><<<dim3(D/64,S/64),256,0,stream>>>(h,w2,d_out,S,D,F,x,a);
}
```

```cpp
#include <hip/hip_runtime.h>
#include <hip/hip_bf16.h>

extern "C" __device__ __attribute__((used, aligned(128))) const unsigned fb_offsets[64 * 32 + 1024] = {
    128u,256u,384u,0u,4294860800u,0u,368640u,294912u,331776u,0u,41472u,512u,512u,4294934528u,3997696u,18432u,4538368u,4784128u,5029888u,4358144u,5275648u,4194304u,5242880u,4290246656u,4293655552u,2097152u,2621440u,2707456u,221184u,0u,4u,0u,
    128u,256u,384u,0u,4294860800u,9216u,377856u,304128u,340992u,9216u,50688u,512u,4294930944u,4294934528u,3997696u,21504u,4540416u,4786176u,5031936u,4360192u,5341184u,4196352u,5242880u,4290251776u,4293660672u,2102272u,2642944u,2728960u,221184u,110592u,36u,0u,
    128u,256u,384u,0u,4294860800u,18432u,387072u,313344u,350208u,23040u,64512u,512u,4294894080u,4294934528u,3997696u,59904u,4542464u,4788224u,5033984u,4362240u,5406720u,4198400u,5242880u,4290256896u,4293665792u,2107392u,2664448u,2750464u,589824u,512u,11u,0u,
    128u,256u,384u,0u,4294860800u,27648u,396288u,322560u,359424u,32256u,73728u,512u,4294857216u,4294934528u,3997696u,62976u,4544512u,4790272u,5036032u,4364288u,5472256u,4200448u,5242880u,4290262016u,4293670912u,2112512u,2685952u,2771968u,589824u,295424u,43u,0u,
    768u,896u,1024u,640u,4294860800u,368640u,0u,331776u,294912u,41472u,0u,512u,18304u,4294934528u,3997696u,59904u,4595712u,4841472u,5087232u,4415488u,5276672u,4251648u,5259264u,4290277376u,4293686272u,2127872u,2707456u,2621440u,221184u,144u,4u,131072u,
    768u,896u,1024u,640u,4294860800u,377856u,9216u,340992u,304128u,50688u,9216u,512u,4294948736u,4294934528u,3997696u,62976u,4597760u,4843520u,5089280u,4417536u,5342208u,4253696u,5259264u,4290282496u,4293691392u,2132992u,2728960u,2642944u,221184u,110736u,36u,131072u,
    768u,896u,1024u,640u,4294860800u,387072u,18432u,350208u,313344u,64512u,23040u,512u,4294911872u,4294934528u,3997696u,18432u,4599808u,4845568u,5091328u,4419584u,5407744u,4255744u,5259264u,4290287616u,4293696512u,2138112u,2750464u,2664448u,221184u,288u,11u,131072u,
    768u,896u,1024u,640u,4294860800u,396288u,27648u,359424u,322560u,73728u,32256u,512u,4294875008u,4294934528u,3997696u,21504u,4601856u,4847616u,5093376u,4421632u,5473280u,4257792u,5259264u,4290292736u,4293701632u,2143232u,2771968u,2685952u,221184u,110880u,43u,131072u,
    128u,256u,384u,0u,122880u,663552u,1032192u,958464u,995328u,82944u,124416u,512u,512u,32768u,4007936u,101376u,4423680u,4669440u,4915200u,4243456u,5244928u,4308992u,5275648u,4290308096u,4293716992u,2158592u,2793472u,2879488u,221184u,432u,6u,393217u,
    128u,256u,384u,0u,122880u,672768u,1041408u,967680u,1004544u,92160u,133632u,512u,4294930944u,32768u,4007936u,104448u,4425728u,4671488u,4917248u,4245504u,5310464u,4311040u,5275648u,4290313216u,4293722112u,2163712u,2814976u,2900992u,221184u,111024u,38u,393217u,
    128u,256u,384u,0u,122880u,681984u,1050624u,976896u,1013760u,105984u,147456u,512u,4294894080u,32768u,4007936u,142848u,4427776u,4673536u,4919296u,4247552u,5376000u,4313088u,5275648u,4290318336u,4293727232u,2168832u,2836480u,2922496u,589824u,1664u,11u,393217u,
    128u,256u,384u,0u,122880u,691200u,1059840u,986112u,1022976u,115200u,156672u,512u,4294857216u,32768u,4007936u,145920u,4429824u,4675584u,4921344u,4249600u,5441536u,4315136u,5275648u,4290323456u,4293732352u,2173952u,2857984u,2944000u,589824u,296576u,43u,393217u,
    768u,896u,1024u,640u,122880u,1032192u,663552u,995328u,958464u,124416u,82944u,512u,18304u,32768u,4007936u,142848u,4481024u,4726784u,4972544u,4300800u,5245952u,4366336u,5292032u,4290338816u,4293747712u,2189312u,2879488u,2793472u,221184u,576u,6u,524289u,
    768u,896u,1024u,640u,122880u,1041408u,672768u,1004544u,967680u,133632u,92160u,512u,4294948736u,32768u,4007936u,145920u,4483072u,4728832u,4974592u,4302848u,5311488u,4368384u,5292032u,4290343936u,4293752832u,2194432u,2900992u,2814976u,221184u,111168u,38u,524289u,
    768u,896u,1024u,640u,122880u,1050624u,681984u,1013760u,976896u,147456u,105984u,512u,4294911872u,32768u,4007936u,101376u,4485120u,4730880u,4976640u,4304896u,5377024u,4370432u,5292032u,4290349056u,4293757952u,2199552u,2922496u,2836480u,221184u,720u,11u,524289u,
    768u,896u,1024u,640u,122880u,1059840u,691200u,1022976u,986112u,156672u,115200u,512u,4294875008u,32768u,4007936u,104448u,4487168u,4732928u,4978688u,4306944u,5442560u,4372480u,5292032u,4290354176u,4293763072u,2204672u,2944000u,2857984u,221184u,111312u,43u,524289u,
    256u,384u,0u,128u,4294860800u,1327104u,1695744u,1622016u,1658880u,165888u,207360u,512u,37248u,4294934528u,4018176u,184320u,4767744u,5013504u,4341760u,4587520u,5279744u,4423680u,5308416u,4290369536u,4293778432u,2220032u,2965504u,3051520u,221184u,1152u,7u,1048577u,
    256u,384u,0u,128u,4294860800u,1336320u,1704960u,1631232u,1668096u,175104u,216576u,512u,384u,4294934528u,4018176u,187392u,4769792u,5015552u,4343808u,4589568u,5345280u,4425728u,5308416u,4290374656u,4293783552u,2225152u,2987008u,3073024u,221184u,111744u,39u,1048577u,
    256u,384u,0u,128u,4294860800u,1345536u,1714176u,1640448u,1677312u,188928u,230400u,512u,4294930816u,4294934528u,4018176u,225792u,4771840u,5017600u,4345856u,4591616u,5410816u,4427776u,5308416u,4290379776u,4293788672u,2230272u,3008512u,3094528u,589824u,2816u,12u,1048577u,
    256u,384u,0u,128u,4294860800u,1354752u,1723392u,1649664u,1686528u,198144u,239616u,512u,4294893952u,4294934528u,4018176u,228864u,4773888u,5019648u,4347904u,4593664u,5476352u,4429824u,5308416u,4290384896u,4293793792u,2235392u,3030016u,3116032u,589824u,297728u,44u,1048577u,
    896u,1024u,640u,768u,4294860800u,1695744u,1327104u,1658880u,1622016u,207360u,165888u,512u,55040u,4294934528u,4018176u,225792u,4825088u,5070848u,4399104u,4644864u,5280768u,4481024u,5324800u,4290400256u,4293809152u,2250752u,3051520u,2965504u,221184u,864u,7u,786433u,
    896u,1024u,640u,768u,4294860800u,1704960u,1336320u,1668096u,1631232u,216576u,175104u,512u,18176u,4294934528u,4018176u,228864u,4827136u,5072896u,4401152u,4646912u,5346304u,4483072u,5324800u,4290405376u,4293814272u,2255872u,3073024u,2987008u,221184u,111456u,39u,786433u,
    896u,1024u,640u,768u,4294860800u,1714176u,1345536u,1677312u,1640448u,230400u,188928u,512u,4294948608u,4294934528u,4018176u,184320u,4829184u,5074944u,4403200u,4648960u,5411840u,4485120u,5324800u,4290410496u,4293819392u,2260992u,3094528u,3008512u,221184u,1008u,12u,786433u,
    896u,1024u,640u,768u,4294860800u,1723392u,1354752u,1686528u,1649664u,239616u,198144u,512u,4294911744u,4294934528u,4018176u,187392u,4831232u,5076992u,4405248u,4651008u,5477376u,4487168u,5324800u,4290415616u,4293824512u,2266112u,3116032u,3030016u,221184u,111600u,44u,786433u,
    256u,384u,0u,128u,122880u,1990656u,2359296u,2285568u,2322432u,248832u,290304u,512u,37248u,32768u,4059136u,267264u,4653056u,4898816u,4227072u,4472832u,5249024u,4538368u,5341184u,4290430976u,4293839872u,2281472u,3137536u,3223552u,221184u,1296u,7u,1179650u,
    256u,384u,0u,128u,122880u,1999872u,2368512u,2294784u,2331648u,258048u,299520u,512u,384u,32768u,4059136u,270336u,4655104u,4900864u,4229120u,4474880u,5314560u,4540416u,5341184u,4290436096u,4293844992u,2286592u,3159040u,3245056u,221184u,111888u,39u,1179650u,
    256u,384u,0u,128u,122880u,2009088u,2377728u,2304000u,2340864u,271872u,313344u,512u,4294930816u,32768u,4059136u,308736u,4657152u,4902912u,4231168u,4476928u,5380096u,4542464u,5341184u,4290441216u,4293850112u,2291712u,3180544u,3266560u,589824u,3968u,13u,1179650u,
    256u,384u,0u,128u,122880u,2018304u,2386944u,2313216u,2350080u,281088u,322560u,512u,4294893952u,32768u,4059136u,311808u,4659200u,4904960u,4233216u,4478976u,5445632u,4544512u,5341184u,4290446336u,4293855232u,2296832u,3202048u,3288064u,589824u,298880u,45u,1179650u,
    896u,1024u,640u,768u,122880u,2359296u,1990656u,2322432u,2285568u,290304u,248832u,512u,55040u,32768u,4059136u,308736u,4710400u,4956160u,4284416u,4530176u,5250048u,4595712u,5357568u,4290461696u,4293870592u,2312192u,3223552u,3137536u,221184u,1440u,7u,1310722u,
    896u,1024u,640u,768u,122880u,2368512u,1999872u,2331648u,2294784u,299520u,258048u,512u,18176u,32768u,4059136u,311808u,4712448u,4958208u,4286464u,4532224u,5315584u,4597760u,5357568u,4290466816u,4293875712u,2317312u,3245056u,3159040u,221184u,112032u,39u,1310722u,
    896u,1024u,640u,768u,122880u,2377728u,2009088u,2340864u,2304000u,313344u,271872u,512u,4294948608u,32768u,4059136u,267264u,4714496u,4960256u,4288512u,4534272u,5381120u,4599808u,5357568u,4290471936u,4293880832u,2322432u,3266560u,3180544u,221184u,1584u,13u,1310722u,
    896u,1024u,640u,768u,122880u,2386944u,2018304u,2350080u,2313216u,322560u,281088u,512u,4294911744u,32768u,4059136u,270336u,4716544u,4962304u,4290560u,4536320u,5446656u,4601856u,5357568u,4290477056u,4293885952u,2327552u,3288064u,3202048u,221184u,112176u,45u,1310722u,
    384u,0u,128u,256u,4294860800u,2654208u,3022848u,2949120u,2985984u,331776u,373248u,512u,73984u,4294934528u,4069376u,350208u,4997120u,4325376u,4571136u,4816896u,5283840u,4653056u,5373952u,4290492416u,4293901312u,2342912u,3309568u,3395584u,221184u,1728u,8u,1572867u,
    384u,0u,128u,256u,4294860800u,2663424u,3032064u,2958336u,2995200u,340992u,382464u,512u,37120u,4294934528u,4069376u,353280u,4999168u,4327424u,4573184u,4818944u,5349376u,4655104u,5373952u,4290497536u,4293906432u,2348032u,3331072u,3417088u,221184u,112320u,40u,1572867u,
    384u,0u,128u,256u,4294860800u,2672640u,3041280u,2967552u,3004416u,354816u,396288u,512u,256u,4294934528u,4069376u,391680u,5001216u,4329472u,4575232u,4820992u,5414912u,4657152u,5373952u,4290502656u,4293911552u,2353152u,3352576u,3438592u,589824u,5120u,13u,1572867u,
    384u,0u,128u,256u,4294860800u,2681856u,3050496u,2976768u,3013632u,364032u,405504u,512u,4294930688u,4294934528u,4069376u,394752u,5003264u,4331520u,4577280u,4823040u,5480448u,4659200u,5373952u,4290507776u,4293916672u,2358272u,3374080u,3460096u,589824u,300032u,45u,1572867u,
    1024u,640u,768u,896u,4294860800u,3022848u,2654208u,2985984u,2949120u,373248u,331776u,512u,91776u,4294934528u,4069376u,391680u,5054464u,4382720u,4628480u,4874240u,5284864u,4710400u,5390336u,4290523136u,4293932032u,2373632u,3395584u,3309568u,221184u,1872u,8u,1703939u,
    1024u,640u,768u,896u,4294860800u,3032064u,2663424u,2995200u,2958336u,382464u,340992u,512u,54912u,4294934528u,4069376u,394752u,5056512u,4384768u,4630528u,4876288u,5350400u,4712448u,5390336u,4290528256u,4293937152u,2378752u,3417088u,3331072u,221184u,112464u,40u,1703939u,
    1024u,640u,768u,896u,4294860800u,3041280u,2672640u,3004416u,2967552u,396288u,354816u,512u,18048u,4294934528u,4069376u,350208u,5058560u,4386816u,4632576u,4878336u,5415936u,4714496u,5390336u,4290533376u,4293942272u,2383872u,3438592u,3352576u,221184u,2016u,13u,1703939u,
    1024u,640u,768u,896u,4294860800u,3050496u,2681856u,3013632u,2976768u,405504u,364032u,512u,4294948480u,4294934528u,4069376u,353280u,5060608u,4388864u,4634624u,4880384u,5481472u,4716544u,5390336u,4290538496u,4293947392u,2388992u,3460096u,3374080u,221184u,112608u,45u,1703939u,
    384u,0u,128u,256u,122880u,3317760u,3686400u,3612672u,3649536u,414720u,456192u,512u,73984u,32768u,4079616u,433152u,4882432u,4210688u,4456448u,4702208u,5253120u,4767744u,5406720u,4290553856u,4293962752u,2404352u,3481600u,3567616u,221184u,2160u,9u,1966083u,
    384u,0u,128u,256u,122880u,3326976u,3695616u,3621888u,3658752u,423936u,465408u,512u,37120u,32768u,4079616u,436224u,4884480u,4212736u,4458496u,4704256u,5318656u,4769792u,5406720u,4290558976u,4293967872u,2409472u,3503104u,3589120u,221184u,112752u,41u,1966083u,
    384u,0u,128u,256u,122880u,3336192u,3704832u,3631104u,3667968u,437760u,479232u,512u,256u,32768u,4079616u,474624u,4886528u,4214784u,4460544u,4706304u,5384192u,4771840u,5406720u,4290564096u,4293972992u,2414592u,3524608u,3610624u,589824u,6272u,14u,1966083u,
    384u,0u,128u,256u,122880u,3345408u,3714048u,3640320u,3677184u,446976u,488448u,512u,4294930688u,32768u,4079616u,477696u,4888576u,4216832u,4462592u,4708352u,5449728u,4773888u,5406720u,4290569216u,4293978112u,2419712u,3546112u,3632128u,589824u,301184u,46u,1966083u,
    1024u,640u,768u,896u,122880u,3686400u,3317760u,3649536u,3612672u,456192u,414720u,512u,91776u,32768u,4079616u,474624u,4939776u,4268032u,4513792u,4759552u,5254144u,4825088u,5423104u,4290584576u,4293993472u,2435072u,3567616u,3481600u,221184u,2304u,9u,2097155u,
    1024u,640u,768u,896u,122880u,3695616u,3326976u,3658752u,3621888u,465408u,423936u,512u,54912u,32768u,4079616u,477696u,4941824u,4270080u,4515840u,4761600u,5319680u,4827136u,5423104u,4290589696u,4293998592u,2440192u,3589120u,3503104u,221184u,112896u,41u,2097155u,
    1024u,640u,768u,896u,122880u,3704832u,3336192u,3667968u,3631104u,479232u,437760u,512u,18048u,32768u,4079616u,433152u,4943872u,4272128u,4517888u,4763648u,5385216u,4829184u,5423104u,4290594816u,4294003712u,2445312u,3610624u,3524608u,221184u,2448u,14u,2097155u,
    1024u,640u,768u,896u,122880u,3714048u,3345408u,3677184u,3640320u,488448u,446976u,512u,4294948480u,32768u,4079616u,436224u,4945920u,4274176u,4519936u,4765696u,5450752u,4831232u,5423104u,4290599936u,4294008832u,2450432u,3632128u,3546112u,221184u,113040u,46u,2097155u,
    0u,128u,256u,384u,4294860800u,3981312u,4349952u,4276224u,4313088u,497664u,539136u,512u,110720u,4294934528u,4120576u,516096u,4308992u,4554752u,4800512u,5046272u,5287936u,4882432u,5439488u,4290615296u,4294024192u,2465792u,3653632u,3739648u,221184u,2592u,9u,2359301u,
    0u,128u,256u,384u,4294860800u,3990528u,4359168u,4285440u,4322304u,506880u,548352u,512u,73856u,4294934528u,4120576u,519168u,4311040u,4556800u,4802560u,5048320u,5353472u,4884480u,5439488u,4290620416u,4294029312u,2470912u,3675136u,3761152u,221184u,113184u,41u,2359301u,
    0u,128u,256u,384u,4294860800u,3999744u,4368384u,4294656u,4331520u,520704u,562176u,512u,36992u,4294934528u,4120576u,557568u,4313088u,4558848u,4804608u,5050368u,5419008u,4886528u,5439488u,4290625536u,4294034432u,2476032u,3696640u,3782656u,589824u,7424u,15u,2359301u,
    0u,128u,256u,384u,4294860800u,4008960u,4377600u,4303872u,4340736u,529920u,571392u,512u,128u,4294934528u,4120576u,560640u,4315136u,4560896u,4806656u,5052416u,5484544u,4888576u,5439488u,4290630656u,4294039552u,2481152u,3718144u,3804160u,589824u,302336u,47u,2359301u,
    640u,768u,896u,1024u,4294860800u,4349952u,3981312u,4313088u,4276224u,539136u,497664u,512u,128512u,4294934528u,4120576u,557568u,4366336u,4612096u,4857856u,5103616u,5288960u,4939776u,5455872u,4290646016u,4294054912u,2496512u,3739648u,3653632u,221184u,2736u,9u,2490373u,
    640u,768u,896u,1024u,4294860800u,4359168u,3990528u,4322304u,4285440u,548352u,506880u,512u,91648u,4294934528u,4120576u,560640u,4368384u,4614144u,4859904u,5105664u,5354496u,4941824u,5455872u,4290651136u,4294060032u,2501632u,3761152u,3675136u,221184u,113328u,41u,2490373u,
    640u,768u,896u,1024u,4294860800u,4368384u,3999744u,4331520u,4294656u,562176u,520704u,512u,54784u,4294934528u,4120576u,516096u,4370432u,4616192u,4861952u,5107712u,5420032u,4943872u,5455872u,4290656256u,4294065152u,2506752u,3782656u,3696640u,221184u,2880u,15u,2490373u,
    640u,768u,896u,1024u,4294860800u,4377600u,4008960u,4340736u,4303872u,571392u,529920u,512u,17920u,4294934528u,4120576u,519168u,4372480u,4618240u,4864000u,5109760u,5485568u,4945920u,5455872u,4290661376u,4294070272u,2511872u,3804160u,3718144u,221184u,113472u,47u,2490373u,
    0u,128u,256u,384u,122880u,4644864u,5013504u,4939776u,4976640u,580608u,622080u,512u,110720u,32768u,4130816u,599040u,4194304u,4440064u,4685824u,4931584u,5257216u,4997120u,5472256u,4290676736u,4294085632u,2527232u,3825664u,3911680u,221184u,3024u,10u,2752517u,
    0u,128u,256u,384u,122880u,4654080u,5022720u,4948992u,4985856u,589824u,631296u,512u,73856u,32768u,4130816u,602112u,4196352u,4442112u,4687872u,4933632u,5322752u,4999168u,5472256u,4290681856u,4294090752u,2532352u,3847168u,3933184u,221184u,113616u,42u,2752517u,
    0u,128u,256u,384u,122880u,4663296u,5031936u,4958208u,4995072u,603648u,645120u,512u,36992u,32768u,4130816u,640512u,4198400u,4444160u,4689920u,4935680u,5388288u,5001216u,5472256u,4290686976u,4294095872u,2537472u,3868672u,3954688u,589824u,8576u,15u,2752517u,
    0u,128u,256u,384u,122880u,4672512u,5041152u,4967424u,5004288u,612864u,654336u,512u,128u,32768u,4130816u,643584u,4200448u,4446208u,4691968u,4937728u,5453824u,5003264u,5472256u,4290692096u,4294100992u,2542592u,3890176u,3976192u,589824u,303488u,47u,2752517u,
    640u,768u,896u,1024u,122880u,5013504u,4644864u,4976640u,4939776u,622080u,580608u,512u,128512u,32768u,4130816u,640512u,4251648u,4497408u,4743168u,4988928u,5258240u,5054464u,5488640u,4290707456u,4294116352u,2557952u,3911680u,3825664u,221184u,3168u,10u,2883589u,
    640u,768u,896u,1024u,122880u,5022720u,4654080u,4985856u,4948992u,631296u,589824u,512u,91648u,32768u,4130816u,643584u,4253696u,4499456u,4745216u,4990976u,5323776u,5056512u,5488640u,4290712576u,4294121472u,2563072u,3933184u,3847168u,221184u,113760u,42u,2883589u,
    640u,768u,896u,1024u,122880u,5031936u,4663296u,4995072u,4958208u,645120u,603648u,512u,54784u,32768u,4130816u,599040u,4255744u,4501504u,4747264u,4993024u,5389312u,5058560u,5488640u,4290717696u,4294126592u,2568192u,3954688u,3868672u,221184u,3312u,15u,2883589u,
    640u,768u,896u,1024u,122880u,5041152u,4672512u,5004288u,4967424u,654336u,612864u,512u,17920u,32768u,4130816u,602112u,4257792u,4503552u,4749312u,4995072u,5454848u,5060608u,5488640u,4290722816u,4294131712u,2573312u,3976192u,3890176u,221184u,113904u,47u,2883589u,
    0u,19776u,39552u,62624u,16u,19792u,39552u,62640u,32u,19808u,42880u,62656u,48u,19824u,42896u,62672u,64u,19840u,42912u,62688u,80u,19840u,42928u,62704u,96u,23168u,42944u,62720u,112u,23184u,42960u,62720u,
    128u,23200u,42976u,66048u,128u,23216u,42992u,66064u,3456u,23232u,43008u,66080u,3472u,23248u,43008u,66096u,3488u,23264u,46336u,66112u,3504u,23280u,46352u,66128u,3520u,23296u,46368u,66144u,3536u,23296u,46384u,66160u,
    3552u,26624u,46400u,66176u,3568u,26640u,46416u,66176u,3584u,26656u,46432u,69504u,3584u,26672u,46448u,69520u,6912u,26688u,46464u,69536u,6928u,26704u,46464u,69552u,6944u,26720u,49792u,69568u,6960u,26736u,49808u,69584u,
    6976u,26752u,49824u,69600u,6992u,26752u,49840u,69616u,7008u,30080u,49856u,69632u,7024u,30096u,49872u,69632u,7040u,30112u,49888u,72960u,7040u,30128u,49904u,72976u,10368u,30144u,49920u,72992u,10384u,30160u,49920u,73008u,
    10400u,30176u,53248u,73024u,10416u,30192u,53264u,73040u,10432u,30208u,53280u,73056u,10448u,30208u,53296u,73072u,10464u,33536u,53312u,73088u,10480u,33552u,53328u,73088u,10496u,33568u,53344u,76416u,10496u,33584u,53360u,76432u,
    13824u,33600u,53376u,76448u,13840u,33616u,53376u,76464u,13856u,33632u,56704u,76480u,13872u,33648u,56720u,76496u,13888u,33664u,56736u,76512u,13904u,33664u,56752u,76528u,13920u,36992u,56768u,76544u,13936u,37008u,56784u,76544u,
    13952u,37024u,56800u,79872u,13952u,37040u,56816u,79888u,17280u,37056u,56832u,79904u,17296u,37072u,56832u,79920u,17312u,37088u,60160u,79936u,17328u,37104u,60176u,79952u,17344u,37120u,60192u,79968u,17360u,37120u,60208u,79984u,
    17376u,40448u,60224u,80000u,17392u,40464u,60240u,80000u,17408u,40480u,60256u,83328u,17408u,40496u,60272u,83344u,20736u,40512u,60288u,83360u,20752u,40528u,60288u,83376u,20768u,40544u,63616u,83392u,20784u,40560u,63632u,83408u,
    86496u,86512u,86528u,86528u,89856u,89872u,89888u,89904u,89920u,89936u,89952u,89968u,89984u,89984u,93312u,93328u,93344u,93360u,93376u,93392u,93408u,93424u,93440u,93440u,96768u,96784u,96800u,96816u,96832u,96848u,96864u,96880u,
    96896u,96896u,100224u,100240u,100256u,100272u,100288u,100304u,100320u,100336u,100352u,100352u,103680u,103696u,103712u,103728u,103744u,103760u,103776u,103792u,103808u,103808u,107136u,107152u,107168u,107184u,107200u,107216u,107232u,107248u,107264u,107264u,
    0u,0u,0u,0u,0u,0u,0u,0u,0u,0u,0u,0u,0u,0u,0u,0u,0u,0u,0u,0u,0u,0u,0u,0u,0u,0u,0u,0u,0u,0u,0u,0u,
    0u,0u,0u,0u,0u,0u,0u,0u,0u,0u,0u,0u,0u,0u,0u,0u,0u,0u,0u,0u,0u,0u,0u,0u,0u,0u,0u,0u,0u,0u,0u,0u,
    0u,0u,0u,0u,0u,0u,0u,0u,0u,0u,0u,0u,0u,0u,0u,0u,0u,0u,0u,0u,0u,0u,0u,0u,0u,0u,0u,0u,0u,0u,0u,0u,
    0u,0u,0u,0u,0u,0u,0u,0u,0u,0u,0u,0u,0u,0u,0u,0u,0u,0u,0u,0u,0u,0u,0u,0u,0u,0u,0u,0u,0u,0u,0u,0u,
    0u,0u,0u,0u,0u,0u,0u,0u,0u,0u,0u,0u,0u,0u,0u,0u,0u,0u,0u,0u,0u,0u,0u,0u,0u,0u,0u,0u,0u,0u,0u,0u,
    0u,0u,0u,0u,0u,0u,0u,0u,0u,0u,0u,0u,0u,0u,0u,0u,0u,0u,0u,0u,0u,0u,0u,0u,0u,0u,0u,0u,0u,0u,0u,0u,
    0u,54328u,108656u,172056u,16u,54344u,108656u,172072u,24u,54360u,117760u,172088u,40u,54368u,117776u,172104u,56u,54384u,117784u,172120u,72u,54384u,117800u,172128u,88u,63488u,117816u,172144u,96u,63504u,117832u,172144u,
    112u,63512u,117848u,181248u,112u,63528u,117856u,181264u,9216u,63544u,117872u,181272u,9232u,63560u,117872u,181288u,9240u,63576u,126976u,181304u,9256u,63584u,126992u,181320u,9272u,63600u,127000u,181336u,9288u,63600u,127016u,181344u,
    9304u,72704u,127032u,181360u,9312u,72720u,127048u,181360u,9328u,72728u,127064u,190464u,9328u,72744u,127072u,190480u,18432u,72760u,127088u,190488u,18448u,72776u,127088u,190504u,18456u,72792u,136192u,190520u,18472u,72800u,136208u,190536u,
    18488u,72816u,136216u,190552u,18504u,72816u,136232u,190560u,18520u,81920u,136248u,190576u,18528u,81936u,136264u,190576u,18544u,81944u,136280u,199680u,18544u,81960u,136288u,199696u,27648u,81976u,136304u,199704u,27664u,81992u,136304u,199720u,
    27672u,82008u,145408u,199736u,27688u,82016u,145424u,199752u,27704u,82032u,145432u,199768u,27720u,82032u,145448u,199776u,27736u,91136u,145464u,199792u,27744u,91152u,145480u,199792u,27760u,91160u,145496u,208896u,27760u,91176u,145504u,208912u,
    36864u,91192u,145520u,208920u,36880u,91208u,145520u,208936u,36888u,91224u,154624u,208952u,36904u,91232u,154640u,208968u,36920u,91248u,154648u,208984u,36936u,91248u,154664u,208992u,36952u,100352u,154680u,209008u,36960u,100368u,154696u,209008u,
    36976u,100376u,154712u,218112u,36976u,100392u,154720u,218128u,46080u,100408u,154736u,218136u,46096u,100424u,154736u,218152u,46104u,100440u,163840u,218168u,46120u,100448u,163856u,218184u,46136u,100464u,163864u,218200u,46152u,100464u,163880u,218208u,
    46168u,109568u,163896u,218224u,46176u,109584u,163912u,218224u,46192u,109592u,163928u,227328u,46192u,109608u,163936u,227344u,55296u,109624u,163952u,227352u,55312u,109640u,163952u,227368u,55320u,109656u,173056u,227384u,55336u,109664u,173072u,227400u,
    230488u,230496u,230512u,230512u,239616u,239632u,239640u,239656u,239672u,239688u,239704u,239712u,239728u,239728u,248832u,248848u,248856u,248872u,248888u,248904u,248920u,248928u,248944u,248944u,258048u,258064u,258072u,258088u,258104u,258120u,258136u,258144u,
    258160u,258160u,267264u,267280u,267288u,267304u,267320u,267336u,267352u,267360u,267376u,267376u,276480u,276496u,276504u,276520u,276536u,276552u,276568u,276576u,276592u,276592u,285696u,285712u,285720u,285736u,285752u,285768u,285784u,285792u,285808u,285808u,
    0u,0u,0u,0u,0u,0u,0u,0u,0u,0u,0u,0u,0u,0u,0u,0u,0u,0u,0u,0u,0u,0u,0u,0u,0u,0u,0u,0u,0u,0u,0u,0u,
    0u,0u,0u,0u,0u,0u,0u,0u,0u,0u,0u,0u,0u,0u,0u,0u,0u,0u,0u,0u,0u,0u,0u,0u,0u,0u,0u,0u,0u,0u,0u,0u,
    0u,0u,0u,0u,0u,0u,0u,0u,0u,0u,0u,0u,0u,0u,0u,0u,0u,0u,0u,0u,0u,0u,0u,0u,0u,0u,0u,0u,0u,0u,0u,0u,
    0u,0u,0u,0u,0u,0u,0u,0u,0u,0u,0u,0u,0u,0u,0u,0u,0u,0u,0u,0u,0u,0u,0u,0u,0u,0u,0u,0u,0u,0u,0u,0u,
    0u,0u,0u,0u,0u,0u,0u,0u,0u,0u,0u,0u,0u,0u,0u,0u,0u,0u,0u,0u,0u,0u,0u,0u,0u,0u,0u,0u,0u,0u,0u,0u,
    0u,0u,0u,0u,0u,0u,0u,0u,0u,0u,0u,0u,0u,0u,0u,0u,0u,0u,0u,0u,0u,0u,0u,0u,0u,0u,0u,0u,0u,0u,0u,0u};

__global__ __launch_bounds__(256, 1) void parallel_sum__sliding_block__dense_activation_relu__prompt_kernel(
        const void* x, const void* wqkv, const void* wo, const void* w1, const void* w2, void* out, void* ws) {
    __shared__ uint4 lds[163840 / 16];
    asm volatile(
    "L_entry_%=:\n"
    "s_load_dwordx2 s[4:5], %0, 0\n"
    "s_load_dwordx2 s[8:9], %0, 8\n"
    "s_load_dwordx2 s[12:13], %0, 16\n"
    "s_load_dwordx2 s[16:17], %0, 24\n"
    "s_load_dwordx2 s[20:21], %0, 32\n"
    "s_load_dwordx2 s[24:25], %0, 40\n"
    "s_load_dwordx2 s[28:29], %0, 48\n"
    "v_lshrrev_b32 v16, 6, v0\n"
    "v_and_b32 v1, 63, v0\n"
    "v_readfirstlane_b32 s78, v16\n"
    "v_and_b32 v15, 7, v0\n"
    "s_and_b32 s80, %1, 7\n"
    "s_lshr_b32 s85, %1, 3\n"
    "v_lshl_add_u32 v16, s85, 2, v16\n"
    "v_min_u32 v17, 3, v1\n"
    "v_lshl_add_u32 v16, v16, 2, v17\n"
    "v_lshlrev_b32 v16, 7, v16\n"
    "v_min_u32 v16, L_end_%=-L_pc_%=-4, v16\n"
    "s_getpc_b64 s[68:69]\n"
    "L_pc_%=:\n"
    "global_load_dword a0, v16, s[68:69]\n"
    "s_and_b32 s77, s80, 1\n"
    "s_lshl_b32 s77, s77, 2\n"
    "s_lshr_b32 s80, s80, 1\n"
    "s_or_b32 s80, s80, s77\n"
    "s_and_b32 s77, s80, 3\n"
    "s_lshl_b32 s77, s77, 2\n"
    "s_and_b32 s82, s85, 3\n"
    "s_add_u32 s77, s77, s82\n"
    "s_lshr_b32 s76, s80, 2\n"
    "s_mul_i32 s76, s76, 8\n"
    "s_lshr_b32 s83, s85, 2\n"
    "s_add_u32 s76, s76, s83\n"
    "s_mov_b32 s6, 2359296\n"
    "s_mov_b32 s7, 0x20000\n"
    "s_lshl_b32 s84, s76, 2\n"
    "s_add_u32 s84, s84, s78\n"
    "s_mul_i32 s84, s84, 36864\n"
    "s_cmp_lt_u32 s78, 2\n"
    "s_movk_i32 s82, 8192\n"
    "s_cselect_b32 s82, 122880, s82\n"
    "s_lshl_b32 s88, s78, 14\n"
    "s_add_u32 s88, s88, s82\n"
    "v_lshrrev_b32 v6, 3, v1\n"
    "v_lshrrev_b32 v13, 4, v1\n"
    "v_and_b32 v14, 15, v1\n"
    "v_mul_u32_u24 v16, 1152, v6\n"
    "v_add_u32 v16, s84, v16\n"
    "v_xor_b32 v17, v15, v13\n"
    "v_lshlrev_b32 v17, 4, v17\n"
    "v_add_u32 v2, v16, v17\n"
    "v_xor_b32 v17, 64, v17\n"
    "v_add_u32 v3, v16, v17\n"
    "v_add_u32 v3, 8192, v3\n"
    "v_add_u32 v4, 16384, v2\n"
    "v_add_u32 v5, 16384, v3\n"
    "s_mul_i32 s82, s77, 2304\n"
    "s_add_u32 s82, s82, s84\n"
    "v_min_u32 v16, 16, v1\n"
    "v_lshlrev_b32 v18, 7, v16\n"
    "s_getpc_b64 s[70:71]\n"
    "s_add_u32 s70, s70, fb_offsets@rel32@lo+4\n"
    "s_addc_u32 s71, s71, fb_offsets@rel32@hi+12\n"
    "s_lshr_b32 s80, s78, 1\n"
    "s_andn2_b32 s80, s80, s77\n"
    "s_lshl_b32 s81, s80, 11\n"
    "s_add_u32 s81, s81, 8192\n"
    "v_lshl_add_u32 v16, v1, 4, s81\n"
    "v_lshl_add_u32 v17, v1, 2, s81\n"
    "global_load_dwordx4 v[20:23], v16, s[70:71]\n"
    "global_load_dword v24, v17, s[70:71] offset:1024\n"
    "s_waitcnt lgkmcnt(0)\n"
    "s_add_u32 m0, s88, 0\n"
    "s_nop 0\n"
    "buffer_load_dwordx4 v2, s[4:7], 0 offen offset:0 lds\n"
    "buffer_load_dwordx4 v3, s[4:7], 0 offen offset:1024 lds\n"
    "buffer_load_dwordx4 v4, s[4:7], 0 offen offset:2048 lds\n"
    "buffer_load_dwordx4 v5, s[4:7], 0 offen offset:3072 lds\n"
    "buffer_load_dword a0, v18, s[4:7], s82 offen offset:128\n"
    "s_cmp_eq_u32 s80, 0\n"
    "s_cselect_b64 s[60:61], s[8:9], s[16:17]\n"
    "s_mov_b32 s81, 1990656\n"
    "s_cselect_b32 s62, s81, 5308416\n"
    "s_mov_b32 s63, 0x20000\n"
    "s_lshl_b32 s80, s77, 2\n"
    "s_add_u32 s80, s80, s78\n"
    "s_lshl_b32 s80, s80, 7\n"
    "s_add_u32 s70, s70, s80\n"
    "s_addc_u32 s71, s71, 0\n"
    "s_load_dwordx16 s[32:47], s[70:71], 0\n"
    "s_load_dwordx8 s[48:55], s[70:71], 64\n"
    "s_load_dwordx4 s[56:59], s[70:71], 96\n"
    "s_load_dwordx4 s[64:67], s[70:71], 112\n"
    "s_lshl_b32 s3, s76, 4\n"
    "s_add_u32 s3, s3, s77\n"
    "v_mov_b32 v12, 0x7ff00000\n"
    "s_lshl_b32 s85, s85, 2\n"
    "s_add_u32 s85, s85, s78\n"
    "s_mov_b32 s30, 134217728\n"
    "s_mov_b32 s31, 0x20000\n"
    "v_cmp_eq_u32 vcc, 0, v1\n"
    "v_mov_b32 v25, 0\n"
    "s_lshl_b32 s80, s76, 2\n"
    "s_add_u32 s80, s80, s78\n"
    "s_lshl_b32 s80, s80, 4\n"
    "s_add_u32 s80, s80, s77\n"
    "s_lshl_b32 s90, s80, 10\n"
    "s_add_u32 s90, s90, 1048576\n"
    "v_cndmask_b32 v19, v12, v25, vcc\n"
    "s_lshl_b32 s91, s78, 8\n"
    "s_add_u32 s91, s91, s3\n"
    "s_lshl_b32 s91, s91, 2\n"
    "s_add_u32 s91, s91, 4096\n"
    "v_lshlrev_b32 v28, 4, v1\n"
    "s_mov_b32 s2, 18432\n"
    "s_mov_b32 s1, 0\n"
    "s_mov_b32 s0, 0\n"
    "s_cmp_eq_u32 s3, 0\n"
    "s_cbranch_scc1 L_ld0_%=\n"
    "L_ld0_ret_%=:\n"
    "v_lshrrev_b32 v16, 1, v14\n"
    "v_xor_b32 v16, v16, v13\n"
    "v_lshlrev_b32 v16, 4, v16\n"
    "v_lshl_add_u32 v17, v14, 7, s88\n"
    "v_add_u32 v8, v17, v16\n"
    "v_xor_b32 v9, 64, v8\n"
    "v_mov_b32 v42, 0x3f803f80\n"
    "v_mov_b32 v43, 0x3f803f80\n"
    "v_mov_b32 v44, 0x3f803f80\n"
    "v_mov_b32 v45, 0x3f803f80\n"
    "s_and_b32 s92, s77, 1\n"
    "s_mul_i32 s92, s92, 5\n"
    "s_lshr_b32 s80, s77, 2\n"
    "s_add_u32 s92, s92, s80\n"
    "s_and_b32 s80, s77, 2\n"
    "s_lshl_b32 s80, s80, 1\n"
    "v_lshrrev_b32 v16, 1, v13\n"
    "v_or_b32 v16, s80, v16\n"
    "v_lshrrev_b32 v18, 1, v14\n"
    "v_xor_b32 v16, v16, v18\n"
    "v_lshl_add_u32 v16, v16, 4, v17\n"
    "v_and_b32 v18, 1, v13\n"
    "v_lshl_add_u32 v54, v18, 3, v16\n"
    "v_xor_b32 v55, 32, v54\n"
    "s_mov_b32 s10, 1990656\n"
    "s_mov_b32 s11, 0x20000\n"
    "s_mov_b32 s18, 5308416\n"
    "s_mov_b32 s19, 0x20000\n"
    "s_and_b32 s79, s77, 1\n"
    "s_mul_i32 s86, s78, 5120\n"
    "s_lshl_b32 s87, s78, 11\n"
    "s_lshl_b32 s81, s3, 2\n"
    "s_add_u32 s81, s81, s78\n"
    "s_mul_i32 s82, s81, 432\n"
    "v_min_u32 v17, 3, v1\n"
    "v_lshl_add_u32 v17, v17, 7, s82\n"
    "s_mul_i32 s81, s81, 1152\n"
    "buffer_load_dword a0, v17, s[8:11], 0 offen offset:0\n"
    "buffer_load_dword a0, v25, s[16:19], s81 offen offset:512\n"
    "v_lshrrev_b32 v10, 2, v1\n"
    "v_and_b32 v11, 3, v1\n"
    "v_lshlrev_b32 v11, 3, v11\n"
    "v_lshlrev_b32 v18, 1, v13\n"
    "v_xor_b32 v16, v15, v18\n"
    "v_lshlrev_b32 v7, 4, v16\n"
    "s_mul_i32 s80, s77, 576\n"
    "s_mul_i32 s81, s78, 147456\n"
    "s_add_u32 s80, s80, s81\n"
    "s_lshl_b32 s93, s3, 2\n"
    "s_add_u32 s93, s93, s78\n"
    "s_mul_i32 s81, s93, 1536\n"
    "s_add_u32 s81, s81, 442368\n"
    "v_min_u32 v17, 11, v1\n"
    "v_lshl_add_u32 v56, v17, 7, s81\n"
    "s_add_u32 m0, s88, 4096\n"
    "s_movk_i32 s89, 128\n"
    "buffer_load_dwordx4 v2, s[4:7], s89 offen offset:0 lds\n"
    "buffer_load_dwordx4 v3, s[4:7], s89 offen offset:1024 lds\n"
    "buffer_load_dwordx4 v4, s[4:7], s89 offen offset:2048 lds\n"
    "buffer_load_dwordx4 v5, s[4:7], s89 offen offset:3072 lds\n"
    "s_mul_i32 s81, s93, 4608\n"
    "s_add_u32 s81, s81, 1180160\n"
    "v_min_u32 v17, 3, v1\n"
    "v_mul_u32_u24 v17, 1152, v17\n"
    "v_add_u32 v57, s81, v17\n"
    "s_mul_i32 s81, s93, 5184\n"
    "v_min_u32 v17, 40, v1\n"
    "v_lshl_add_u32 v58, v17, 7, s81\n"
    "s_mul_i32 s81, s93, 5184\n"
    "v_lshl_add_u32 v59, v17, 7, s81\n"
    "s_mul_i32 s81, s93, 648\n"
    "v_min_u32 v17, 5, v1\n"
    "v_lshl_add_u32 v60, v17, 7, s81\n"
    "s_mul_i32 s81, s85, 81\n"
    "s_lshr_b32 s81, s81, 1\n"
    "v_min_u32 v17, 40, v1\n"
    "v_add_u32 v58, s81, v17\n"
    "v_mul_u32_u24 v17, 58255, v58\n"
    "v_lshrrev_b32 v17, 20, v17\n"
    "s_lshr_b32 s81, s76, 3\n"
    "s_mul_i32 s81, s81, 2654208\n"
    "s_and_b32 s82, s77, 12\n"
    "s_mul_i32 s82, s82, 576\n"
    "s_add_u32 s81, s81, s82\n"
    "v_mul_u32_u24 v17, 6912, v17\n"
    "v_lshl_add_u32 v58, v58, 7, v17\n"
    "v_add_u32 v58, s81, v58\n"
    "s_and_b32 s81, s77, 12\n"
    "s_lshr_b32 s82, s76, 3\n"
    "s_lshl_b32 s82, s82, 1\n"
    "s_add_u32 s81, s81, s82\n"
    "s_lshl_b32 s81, s81, 6\n"
    "s_add_u32 s81, s81, s85\n"
    "s_sub_u32 s81, s81, s93\n"
    "s_mul_i32 s82, s81, 5184\n"
    "v_add_u32 v59, s82, v59\n"
    "s_mul_i32 s82, s81, 648\n"
    "v_add_u32 v60, s82, v60\n"
    "s_and_b32 s93, s76, 3\n"
    "s_lshl_b32 s83, s79, 6\n"
    "s_add_u32 s83, s83, s80\n"
    "v_mul_u32_u24 v17, 9216, v6\n"
    "v_add3_u32 v61, v17, v7, s83\n"
    "v_add_u32 v62, 72704, v61\n"
    "v_mul_u32_u24 v17, 1152, v6\n"
    "v_add_u32 v17, v17, v7\n"
    "v_accvgpr_write_b32 a1, v17\n"
    "v_mul_u32_u24 v16, 160, v10\n"
    "v_add_u32 v63, v16, v11\n"
    "v_and_b32 v17, 8, v11\n"
    "s_getpc_b64 s[26:27]\n"
    "L_hpc_%=:\n"
    "s_lshl_b32 s68, s78, 12\n"
    "s_add_u32 s68, s68, L_pg1_%=-L_hpc_%=\n"
    "s_add_u32 s26, s26, s68\n"
    "s_addc_u32 s27, s27, 0\n"
    "s_branch L_hop0_%=\n"
    "L_tramp_%=:\n"
    "s_mov_b64 exec, -1\n"
    "s_setpc_b64 s[68:69]\n"
    "L_hop0_%=:\n"
    "s_mov_b64 exec, 0\n"
    "s_swappc_b64 s[68:69], s[26:27]\n"
    "s_add_u32 s26, s26, 16384\n"
    "s_addc_u32 s27, s27, 0\n"
    "s_mov_b64 exec, 0\n"
    "s_swappc_b64 s[68:69], s[26:27]\n"
    "s_add_u32 m0, s88, 8192\n"
    "s_movk_i32 s89, 256\n"
    "buffer_load_dwordx4 v2, s[4:7], s89 offen offset:0 lds\n"
    "buffer_load_dwordx4 v3, s[4:7], s89 offen offset:1024 lds\n"
    "buffer_load_dwordx4 v4, s[4:7], s89 offen offset:2048 lds\n"
    "buffer_load_dwordx4 v5, s[4:7], s89 offen offset:3072 lds\n"
    "s_waitcnt lgkmcnt(0)\n"
    "s_waitcnt vmcnt(11)\n"
    "s_add_u32 m0, s88, 12288\n"
    "s_movk_i32 s89, 384\n"
    "buffer_load_dwordx4 v2, s[4:7], s89 offen offset:0 lds\n"
    "buffer_load_dwordx4 v3, s[4:7], s89 offen offset:1024 lds\n"
    "buffer_load_dwordx4 v4, s[4:7], s89 offen offset:2048 lds\n"
    "buffer_load_dwordx4 v5, s[4:7], s89 offen offset:3072 lds\n"
    "s_cmp_eq_u32 s92, 0\n"
    "s_cselect_b64 exec, -1, 0\n"
    "ds_read_b64 a[6:7], v54 offset:0\n"
    "ds_read_b64 a[8:9], v55 offset:0\n"
    "ds_read_b64 a[10:11], v54 offset:2048\n"
    "ds_read_b64 a[12:13], v55 offset:2048\n"
    "s_mov_b64 exec, -1\n"
    "ds_read_b128 a[46:49], v8 offset:0\n"
    "ds_read_b128 a[50:53], v9 offset:0\n"
    "ds_read_b128 a[118:121], v8 offset:2048\n"
    "ds_read_b128 a[122:125], v9 offset:2048\n"
    "v_mad_u32_u24 v16, v17, 9, v16\n"
    "v_lshrrev_b32 v17, 4, v11\n"
    "v_mul_u32_u24 v17, 10240, v17\n"
    "v_add_u32 v65, v16, v17\n"
    "v_add_u32 v64, 0x10000, v63\n"
    "v_add_u32 v66, 0x10000, v65\n"
    "v_mov_b32 v29, 0x3f80\n"
    "v_and_b32 v16, 1, v14\n"
    "v_lshlrev_b32 v16, 4, v16\n"
    "v_lshlrev_b32 v16, v16, v29\n"
    "v_lshlrev_b32 v17, 2, v13\n"
    "v_lshrrev_b32 v18, 1, v14\n"
    "v_sub_u32 v17, v18, v17\n"
    "v_cmp_eq_i32 s[70:71], 0, v17\n"
    "v_cmp_eq_i32 s[72:73], 1, v17\n"
    "v_cmp_eq_i32 s[74:75], 2, v17\n"
    "v_cmp_eq_i32 vcc, 3, v17\n"
    "v_cndmask_b32 v46, 0, v16, s[70:71]\n"
    "v_cndmask_b32 v47, 0, v16, s[72:73]\n"
    "v_cndmask_b32 v48, 0, v16, s[74:75]\n"
    "v_cndmask_b32 v49, 0, v16, vcc\n"
    "v_cmp_eq_i32 s[70:71], -8, v17\n"
    "v_cmp_eq_i32 s[72:73], -7, v17\n"
    "v_cmp_eq_i32 s[74:75], -6, v17\n"
    "s_waitcnt lgkmcnt(0)\n"
    "s_add_u32 s26, s26, 16384\n"
    "s_addc_u32 s27, s27, 0\n"
    "s_mov_b64 exec, 0\n"
    "s_swappc_b64 s[68:69], s[26:27]\n"
    "s_waitcnt vmcnt(8)\n"
    "s_add_u32 m0, s88, 0\n"
    "s_movk_i32 s89, 512\n"
    "buffer_load_dwordx4 v2, s[4:7], s89 offen offset:0 lds\n"
    "buffer_load_dwordx4 v3, s[4:7], s89 offen offset:1024 lds\n"
    "buffer_load_dwordx4 v4, s[4:7], s89 offen offset:2048 lds\n"
    "buffer_load_dwordx4 v5, s[4:7], s89 offen offset:3072 lds\n"
    "s_cmp_eq_u32 s92, 1\n"
    "s_cselect_b64 exec, -1, 0\n"
    "ds_read_b64 a[6:7], v54 offset:4096\n"
    "ds_read_b64 a[8:9], v55 offset:4096\n"
    "ds_read_b64 a[10:11], v54 offset:6144\n"
    "ds_read_b64 a[12:13], v55 offset:6144\n"
    "s_mov_b64 exec, -1\n"
    "ds_read_b128 a[54:57], v8 offset:4096\n"
    "ds_read_b128 a[58:61], v9 offset:4096\n"
    "ds_read_b128 a[126:129], v8 offset:6144\n"
    "ds_read_b128 a[130:133], v9 offset:6144\n"
    "v_mfma_f32_16x16x32_bf16 v[76:79], v[42:45], a[46:49], 0\n"
    "v_cmp_eq_i32 vcc, -5, v17\n"
    "v_cndmask_b32 v50, 0, v16, s[70:71]\n"
    "v_cndmask_b32 v51, 0, v16, s[72:73]\n"
    "v_mfma_f32_16x16x32_bf16 v[76:79], v[42:45], a[50:53], v[76:79]\n"
    "v_cndmask_b32 v52, 0, v16, s[74:75]\n"
    "v_cndmask_b32 v53, 0, v16, vcc\n"
    "s_mov_b32 s14, 663552\n"
    "v_mfma_f32_16x16x32_bf16 v[80:83], v[42:45], a[118:121], 0\n"
    "s_mov_b32 s15, 0x20000\n"
    "s_mov_b32 s22, 5308416\n"
    "s_mov_b32 s23, 0x20000\n"
    "v_mfma_f32_16x16x32_bf16 v[80:83], v[42:45], a[122:125], v[80:83]\n"
    "v_lshlrev_b32 v17, 1, v13\n"
    "v_sub_u32 v17, v18, v17\n"
    "v_cmp_eq_u32 s[70:71], 0, v17\n"
    "v_mfma_f32_16x16x32_bf16 v[84:87], a[46:49], a[46:49], 0\n"
    "v_cmp_eq_u32 s[72:73], 1, v17\n"
    "s_nop 0\n"
    "v_cndmask_b32 v17, 0, v16, s[70:71]\n"
    "v_mfma_f32_16x16x32_bf16 v[84:87], a[50:53], a[50:53], v[84:87]\n"
    "v_cndmask_b32 v18, 0, v16, s[72:73]\n"
    "v_accvgpr_write_b32 a14, 0\n"
    "v_accvgpr_write_b32 a15, 0\n"
    "v_mfma_f32_16x16x32_bf16 v[88:91], a[118:121], a[118:121], 0\n"
    "v_accvgpr_write_b32 a16, v17\n"
    "v_accvgpr_write_b32 a17, v18\n"
    "v_accvgpr_write_b32 a18, 0\n"
    "v_mfma_f32_16x16x32_bf16 v[88:91], a[122:125], a[122:125], v[88:91]\n"
    "v_accvgpr_write_b32 a19, 0\n"
    "v_mov_b32 v16, 0xf149f2ca\n"
    "v_lshlrev_b32 v17, 2, v13\n"
    "s_waitcnt lgkmcnt(0)\n"
    "s_waitcnt vmcnt(8)\n"
    "s_add_u32 m0, s88, 4096\n"
    "s_movk_i32 s89, 640\n"
    "buffer_load_dwordx4 v2, s[4:7], s89 offen offset:0 lds\n"
    "buffer_load_dwordx4 v3, s[4:7], s89 offen offset:1024 lds\n"
    "buffer_load_dwordx4 v4, s[4:7], s89 offen offset:2048 lds\n"
    "buffer_load_dwordx4 v5, s[4:7], s89 offen offset:3072 lds\n"
    "s_cmp_eq_u32 s92, 2\n"
    "s_cselect_b64 exec, -1, 0\n"
    "ds_read_b64 a[6:7], v54 offset:8192\n"
    "ds_read_b64 a[8:9], v55 offset:8192\n"
    "ds_read_b64 a[10:11], v54 offset:10240\n"
    "ds_read_b64 a[12:13], v55 offset:10240\n"
    "s_mov_b64 exec, -1\n"
    "ds_read_b128 a[62:65], v8 offset:8192\n"
    "ds_read_b128 a[66:69], v9 offset:8192\n"
    "ds_read_b128 a[134:137], v8 offset:10240\n"
    "ds_read_b128 a[138:141], v9 offset:10240\n"
    "v_mfma_f32_16x16x32_bf16 v[76:79], v[42:45], a[54:57], v[76:79]\n"
    "v_sub_u32 v17, v14, v17\n"
    "v_cmp_gt_i32 s[70:71], 0, v17\n"
    "v_cmp_gt_i32 s[72:73], 1, v17\n"
    "v_mfma_f32_16x16x32_bf16 v[76:79], v[42:45], a[58:61], v[76:79]\n"
    "v_cmp_gt_i32 s[74:75], 2, v17\n"
    "v_cmp_gt_i32 vcc, 3, v17\n"
    "v_cndmask_b32 v18, 0, v16, s[70:71]\n"
    "v_mfma_f32_16x16x32_bf16 v[80:83], v[42:45], a[126:129], v[80:83]\n"
    "v_accvgpr_write_b32 a20, v18\n"
    "v_cndmask_b32 v18, 0, v16, s[72:73]\n"
    "v_accvgpr_write_b32 a21, v18\n"
    "v_mfma_f32_16x16x32_bf16 v[80:83], v[42:45], a[130:133], v[80:83]\n"
    "v_cndmask_b32 v18, 0, v16, s[74:75]\n"
    "v_accvgpr_write_b32 a22, v18\n"
    "v_cndmask_b32 v18, 0, v16, vcc\n"
    "v_mfma_f32_16x16x32_bf16 v[84:87], a[54:57], a[54:57], v[84:87]\n"
    "v_accvgpr_write_b32 a23, v18\n"
    "v_bfe_u32 v16, v10, 1, 2\n"
    "v_lshl_add_u32 v17, v10, 7, v11\n"
    "v_mfma_f32_16x16x32_bf16 v[84:87], a[58:61], a[58:61], v[84:87]\n"
    "v_lshl_add_u32 v67, v16, 5, v17\n"
    "v_xor_b32 v69, 32, v67\n"
    "v_xor_b32 v71, 64, v67\n"
    "v_mfma_f32_16x16x32_bf16 v[88:91], a[126:129], a[126:129], v[88:91]\n"
    "v_xor_b32 v73, 96, v67\n"
    "v_add_u32 v68, 0x10000, v67\n"
    "v_add_u32 v70, 0x10000, v69\n"
    "v_mfma_f32_16x16x32_bf16 v[88:91], a[130:133], a[130:133], v[88:91]\n"
    "v_add_u32 v72, 0x10000, v71\n"
    "v_add_u32 v74, 0x10000, v73\n"
    "v_mul_u32_u24 v16, 1152, v14\n"
    "s_cmp_eq_u32 s3, 0\n"
    "s_cbranch_scc1 L_ld1_%=\n"
    "L_ld1_ret_%=:\n"
    "s_waitcnt lgkmcnt(0)\n"
    "s_waitcnt vmcnt(8)\n"
    "s_add_u32 m0, s88, 8192\n"
    "s_movk_i32 s89, 768\n"
    "buffer_load_dwordx4 v2, s[4:7], s89 offen offset:0 lds\n"
    "buffer_load_dwordx4 v3, s[4:7], s89 offen offset:1024 lds\n"
    "buffer_load_dwordx4 v4, s[4:7], s89 offen offset:2048 lds\n"
    "buffer_load_dwordx4 v5, s[4:7], s89 offen offset:3072 lds\n"
    "s_cmp_eq_u32 s92, 3\n"
    "s_cselect_b64 exec, -1, 0\n"
    "ds_read_b64 a[6:7], v54 offset:12288\n"
    "ds_read_b64 a[8:9], v55 offset:12288\n"
    "ds_read_b64 a[10:11], v54 offset:14336\n"
    "ds_read_b64 a[12:13], v55 offset:14336\n"
    "s_mov_b64 exec, -1\n"
    "ds_read_b128 a[70:73], v8 offset:12288\n"
    "ds_read_b128 a[74:77], v9 offset:12288\n"
    "ds_read_b128 a[142:145], v8 offset:14336\n"
    "ds_read_b128 a[146:149], v9 offset:14336\n"
    "v_mfma_f32_16x16x32_bf16 v[76:79], v[42:45], a[62:65], v[76:79]\n"
    "v_lshl_add_u32 v16, v13, 3, v16\n"
    "v_add_u32 v75, s84, v16\n"
    "v_mul_u32_u24 v16, 1152, v14\n"
    "v_mfma_f32_16x16x32_bf16 v[76:79], v[42:45], a[66:69], v[76:79]\n"
    "v_and_b32 v17, 1, v13\n"
    "v_lshl_add_u32 v16, v17, 5, v16\n"
    "v_lshrrev_b32 v17, 1, v13\n"
    "v_mfma_f32_16x16x32_bf16 v[80:83], v[42:45], a[134:137], v[80:83]\n"
    "v_lshl_add_u32 v16, v17, 4, v16\n"
    "v_add_u32 v18, s84, v16\n"
    "s_lshr_b32 s80, s77, 1\n"
    "v_mfma_f32_16x16x32_bf16 v[80:83], v[42:45], a[138:141], v[80:83]\n"
    "s_lshl_b32 s80, s80, 6\n"
    "s_mul_i32 s81, s79, 640\n"
    "s_add_u32 s82, s80, s81\n"
    "v_mfma_f32_16x16x32_bf16 v[84:87], a[62:65], a[62:65], v[84:87]\n"
    "v_add_u32 v75, s82, v75\n"
    "v_add_u32 v18, s82, v18\n"
    "v_accvgpr_write_b32 a24, v18\n"
    "v_mfma_f32_16x16x32_bf16 v[84:87], a[66:69], a[66:69], v[84:87]\n"
    "s_and_b32 s84, s80, 64\n"
    "s_xor_b32 s84, s84, 64\n"
    "v_lshrrev_b32 v16, 3, v10\n"
    "v_mfma_f32_16x16x32_bf16 v[88:91], a[134:137], a[134:137], v[88:91]\n"
    "v_lshl_add_u32 v16, v16, 12, v67\n"
    "v_xor_b32 v16, s84, v16\n"
    "v_accvgpr_write_b32 a25, v16\n"
    "v_mfma_f32_16x16x32_bf16 v[88:91], a[138:141], a[138:141], v[88:91]\n"
    "v_and_b32 v16, 3, v10\n"
    "v_mul_u32_u24 v16, 1152, v16\n"
    "v_add_u32 v16, v16, v11\n"
    "s_waitcnt lgkmcnt(0)\n"
    "s_waitcnt vmcnt(8)\n"
    "s_add_u32 m0, s88, 12288\n"
    "s_movk_i32 s89, 896\n"
    "buffer_load_dwordx4 v2, s[4:7], s89 offen offset:0 lds\n"
    "buffer_load_dwordx4 v3, s[4:7], s89 offen offset:1024 lds\n"
    "buffer_load_dwordx4 v4, s[4:7], s89 offen offset:2048 lds\n"
    "buffer_load_dwordx4 v5, s[4:7], s89 offen offset:3072 lds\n"
    "ds_read_b128 a[78:81], v8 offset:0\n"
    "ds_read_b128 a[82:85], v9 offset:0\n"
    "ds_read_b128 a[150:153], v8 offset:2048\n"
    "ds_read_b128 a[154:157], v9 offset:2048\n"
    "v_mfma_f32_16x16x32_bf16 v[76:79], v[42:45], a[70:73], v[76:79]\n"
    "v_add_u32 v16, 122880, v16\n"
    "v_lshlrev_b32 v17, 5, v13\n"
    "v_xor_b32 v18, s84, v17\n"
    "v_mfma_f32_16x16x32_bf16 v[76:79], v[42:45], a[74:77], v[76:79]\n"
    "v_add_u32 v18, v18, v16\n"
    "v_accvgpr_write_b32 a26, v18\n"
    "v_and_b32 v16, 3, v14\n"
    "v_mfma_f32_16x16x32_bf16 v[80:83], v[42:45], a[142:145], v[80:83]\n"
    "v_lshlrev_b32 v17, 6, v13\n"
    "v_lshl_add_u32 v17, v16, 4, v17\n"
    "v_lshrrev_b32 v18, 2, v14\n"
    "v_mfma_f32_16x16x32_bf16 v[80:83], v[42:45], a[146:149], v[80:83]\n"
    "v_lshl_add_u32 v17, v18, 8, v17\n"
    "v_accvgpr_write_b32 a27, v17\n"
    "v_mul_u32_u24 v18, 768, v18\n"
    "v_mfma_f32_16x16x32_bf16 v[84:87], a[70:73], a[70:73], v[84:87]\n"
    "s_lshl_b32 s80, s78, 8\n"
    "v_add3_u32 v17, v17, v18, s80\n"
    "v_accvgpr_write_b32 a28, v17\n"
    "v_mfma_f32_16x16x32_bf16 v[84:87], a[74:77], a[74:77], v[84:87]\n"
    "s_mul_i32 s80, s78, 4608\n"
    "v_cmp_gt_u32 vcc, 4, v14\n"
    "v_mov_b32 v16, s80\n"
    "v_mfma_f32_16x16x32_bf16 v[88:91], a[142:145], a[142:145], v[88:91]\n"
    "s_nop 0\n"
    "v_cndmask_b32 v16, v12, v16, vcc\n"
    "v_add_u32 v75, v75, v16\n"
    "v_mfma_f32_16x16x32_bf16 v[88:91], a[146:149], a[146:149], v[88:91]\n"
    "v_accvgpr_write_b32 a29, v16\n"
    "s_mul_i32 s83, s76, 3408896\n"
    "s_lshr_b32 s80, s67, 16\n"
    "s_waitcnt lgkmcnt(0)\n"
    "s_waitcnt vmcnt(8)\n"
    "s_add_u32 m0, s88, 0\n"
    "s_movk_i32 s89, 1024\n"
    "buffer_load_dwordx4 v2, s[4:7], s89 offen offset:0 lds\n"
    "buffer_load_dwordx4 v3, s[4:7], s89 offen offset:1024 lds\n"
    "buffer_load_dwordx4 v4, s[4:7], s89 offen offset:2048 lds\n"
    "buffer_load_dwordx4 v5, s[4:7], s89 offen offset:3072 lds\n"
    "s_cmp_eq_u32 s92, 5\n"
    "s_cselect_b64 exec, -1, 0\n"
    "ds_read_b64 a[6:7], v54 offset:4096\n"
    "ds_read_b64 a[8:9], v55 offset:4096\n"
    "ds_read_b64 a[10:11], v54 offset:6144\n"
    "ds_read_b64 a[12:13], v55 offset:6144\n"
    "s_mov_b64 exec, -1\n"
    "ds_read_b128 a[86:89], v8 offset:4096\n"
    "ds_read_b128 a[90:93], v9 offset:4096\n"
    "ds_read_b128 a[158:161], v8 offset:6144\n"
    "ds_read_b128 a[162:165], v9 offset:6144\n"
    "v_mfma_f32_16x16x32_bf16 v[76:79], v[42:45], a[78:81], v[76:79]\n"
    "v_add_u32 v16, s80, v13\n"
    "v_lshrrev_b32 v17, 4, v16\n"
    "v_and_b32 v16, 15, v16\n"
    "v_mfma_f32_16x16x32_bf16 v[76:79], v[42:45], a[82:85], v[76:79]\n"
    "v_cmp_eq_u32 s[74:75], 2, v17\n"
    "v_cmp_eq_u32 vcc, 0, v17\n"
    "v_mul_u32_u24 v16, 30720, v16\n"
    "v_mfma_f32_16x16x32_bf16 v[80:83], v[42:45], a[150:153], v[80:83]\n"
    "v_add_u32 v16, s83, v16\n"
    "v_cndmask_b32 v17, v17, 3, vcc\n"
    "v_mul_u32_u24 v17, 10240, v17\n"
    "v_mfma_f32_16x16x32_bf16 v[80:83], v[42:45], a[154:157], v[80:83]\n"
    "v_add_u32 v16, 2086912, v16\n"
    "v_add_u32 v16, v16, v17\n"
    "v_mov_b32 v18, 0x3eae1a93\n"
    "v_mfma_f32_16x16x32_bf16 v[84:87], a[78:81], a[78:81], v[84:87]\n"
    "v_cndmask_b32 v18, 1.0, v18, vcc\n"
    "v_accvgpr_write_b32 a30, v18\n"
    "v_readlane_b32 s94, v16, 0\n"
    "v_mfma_f32_16x16x32_bf16 v[84:87], a[82:85], a[82:85], v[84:87]\n"
    "v_readlane_b32 s95, v16, 32\n"
    "s_mul_i32 s81, s78, 2560\n"
    "v_lshl_add_u32 v17, v14, 4, s81\n"
    "v_mfma_f32_16x16x32_bf16 v[88:91], a[150:153], a[150:153], v[88:91]\n"
    "v_add_u32 v17, 1024, v17\n"
    "s_lshl_b32 s80, 2, s79\n"
    "v_cmp_gt_u32 vcc, s80, v13\n"
    "v_mfma_f32_16x16x32_bf16 v[88:91], a[154:157], a[154:157], v[88:91]\n"
    "v_add_u32 v16, v16, v17\n"
    "v_cmp_eq_u32 s[70:71], 0, v13\n"
    "v_cndmask_b32 v16, v12, v16, vcc\n"
    "s_waitcnt vmcnt(8) lgkmcnt(0)\n"
    "s_cmp_eq_u32 s92, 6\n"
    "s_cselect_b64 exec, -1, 0\n"
    "ds_read_b64 a[6:7], v54 offset:8192\n"
    "ds_read_b64 a[8:9], v55 offset:8192\n"
    "ds_read_b64 a[10:11], v54 offset:10240\n"
    "ds_read_b64 a[12:13], v55 offset:10240\n"
    "s_mov_b64 exec, -1\n"
    "ds_read_b128 a[94:97], v8 offset:8192\n"
    "ds_read_b128 a[98:101], v9 offset:8192\n"
    "ds_read_b128 a[166:169], v8 offset:10240\n"
    "ds_read_b128 a[170:173], v9 offset:10240\n"
    "v_mfma_f32_16x16x32_bf16 v[76:79], v[42:45], a[86:89], v[76:79]\n"
    "v_accvgpr_write_b32 a31, v16\n"
    "v_cndmask_b32 v16, v12, v17, s[70:71]\n"
    "v_accvgpr_write_b32 a32, v16\n"
    "v_mfma_f32_16x16x32_bf16 v[76:79], v[42:45], a[90:93], v[76:79]\n"
    "v_cndmask_b32 v16, v12, v28, s[70:71]\n"
    "v_accvgpr_write_b32 a33, v16\n"
    "v_cndmask_b32 v16, 8, 0, s[70:71]\n"
    "v_mfma_f32_16x16x32_bf16 v[80:83], v[42:45], a[158:161], v[80:83]\n"
    "v_add_u32 v16, v16, v17\n"
    "v_add_u32 v16, 102400, v16\n"
    "v_accvgpr_write_b32 a34, v16\n"
    "v_mfma_f32_16x16x32_bf16 v[80:83], v[42:45], a[162:165], v[80:83]\n"
    "v_and_b32 v16, 1, v13\n"
    "v_lshlrev_b32 v16, 7, v16\n"
    "v_xor_b32 v16, v16, v28\n"
    "v_mfma_f32_16x16x32_bf16 v[84:87], a[86:89], a[86:89], v[84:87]\n"
    "v_add_u32 v16, s81, v16\n"
    "v_accvgpr_write_b32 a35, v16\n"
    "s_add_u32 s81, s81, 133120\n"
    "v_mfma_f32_16x16x32_bf16 v[84:87], a[90:93], a[90:93], v[84:87]\n"
    "v_lshl_add_u32 v16, v10, 4, s81\n"
    "v_add_u32 v16, 1024, v16\n"
    "v_accvgpr_write_b32 a36, v16\n"
    "v_mfma_f32_16x16x32_bf16 v[88:91], a[158:161], a[158:161], v[88:91]\n"
    "v_and_b32 v16, 8, v11\n"
    "v_xor_b32 v16, v16, v10\n"
    "v_lshl_add_u32 v16, v11, 1, v16\n"
    "v_mfma_f32_16x16x32_bf16 v[88:91], a[162:165], a[162:165], v[88:91]\n"
    "v_lshl_add_u32 v16, v16, 4, s81\n"
    "v_accvgpr_write_b32 a37, v16\n"
    "s_lshr_b32 s82, s78, 1\n"
    "s_add_u32 m0, s86, 73728\n"
    "s_nop 0\n"
    "buffer_load_dwordx4 v20, s[60:63], s65 offen offset:0 lds\n"
    "buffer_load_dwordx4 v21, s[60:63], s65 offen offset:1024 lds\n"
    "buffer_load_dwordx4 v22, s[60:63], s65 offen offset:2048 lds\n"
    "buffer_load_dwordx4 v23, s[60:63], s65 offen offset:3072 lds\n"
    "s_add_u32 m0, s86, 77824\n"
    "s_nop 0\n"
    "buffer_load_dwordx4 v24, s[60:63], s65 offen offset:0 lds\n"
    "s_waitcnt vmcnt(9) lgkmcnt(0)\n"
    "s_cmp_eq_u32 s92, 7\n"
    "s_cselect_b64 exec, -1, 0\n"
    "ds_read_b64 a[6:7], v54 offset:12288\n"
    "ds_read_b64 a[8:9], v55 offset:12288\n"
    "ds_read_b64 a[10:11], v54 offset:14336\n"
    "ds_read_b64 a[12:13], v55 offset:14336\n"
    "s_mov_b64 exec, -1\n"
    "ds_read_b128 a[102:105], v8 offset:12288\n"
    "ds_read_b128 a[106:109], v9 offset:12288\n"
    "ds_read_b128 a[174:177], v8 offset:14336\n"
    "ds_read_b128 a[178:181], v9 offset:14336\n"
    "v_mfma_f32_16x16x32_bf16 v[76:79], v[42:45], a[94:97], v[76:79]\n"
    "s_mul_i32 s96, s82, 20480\n"
    "s_add_u32 s96, s96, s86\n"
    "s_add_u32 s96, s96, 102400\n"
    "v_mfma_f32_16x16x32_bf16 v[76:79], v[42:45], a[98:101], v[76:79]\n"
    "v_mul_u32_u24 v16, 1024, v13\n"
    "v_add3_u32 v16, v16, v28, s96\n"
    "v_accvgpr_write_b32 a38, v16\n"
    "v_mfma_f32_16x16x32_bf16 v[80:83], v[42:45], a[166:169], v[80:83]\n"
    "s_lshl_b32 s81, s76, 6\n"
    "s_add_u32 s82, s66, s81\n"
    "v_and_b32 v16, 1, v1\n"
    "v_mfma_f32_16x16x32_bf16 v[80:83], v[42:45], a[170:173], v[80:83]\n"
    "v_lshl_add_u32 v16, v16, 4, s82\n"
    "v_lshrrev_b32 v17, 1, v1\n"
    "v_lshlrev_b32 v18, 6, v17\n"
    "v_mfma_f32_16x16x32_bf16 v[84:87], a[94:97], a[94:97], v[84:87]\n"
    "v_sub_u32 v16, v16, v18\n"
    "s_min_u32 s80, s76, 2\n"
    "v_cmp_ge_u32 vcc, s80, v17\n"
    "v_mfma_f32_16x16x32_bf16 v[84:87], a[98:101], a[98:101], v[84:87]\n"
    "s_and_b32 s80, s67, 15\n"
    "s_lshl_b32 s82, s78, 4\n"
    "s_add_u32 s81, s81, s82\n"
    "v_mfma_f32_16x16x32_bf16 v[88:91], a[166:169], a[166:169], v[88:91]\n"
    "s_add_u32 s80, s80, s81\n"
    "v_mov_b32 v17, s80\n"
    "v_cndmask_b32 v16, v17, v16, vcc\n"
    "v_mfma_f32_16x16x32_bf16 v[88:91], a[170:173], a[170:173], v[88:91]\n"
    "v_lshlrev_b32 v16, 10, v16\n"
    "v_add_u32 v16, 1048576, v16\n"
    "v_accvgpr_write_b32 a39, v16\n"
    "s_add_u32 s65, s65, s64\n"
    "s_add_u32 m0, s86, 98304\n"
    "s_nop 0\n"
    "buffer_load_dwordx4 v20, s[60:63], s65 offen offset:0 lds\n"
    "buffer_load_dwordx4 v21, s[60:63], s65 offen offset:1024 lds\n"
    "buffer_load_dwordx4 v22, s[60:63], s65 offen offset:2048 lds\n"
    "buffer_load_dwordx4 v23, s[60:63], s65 offen offset:3072 lds\n"
    "s_add_u32 m0, s86, 102400\n"
    "s_nop 0\n"
    "buffer_load_dwordx4 v24, s[60:63], s65 offen offset:0 lds\n"
    "s_waitcnt vmcnt(10) lgkmcnt(0)\n"
    ".p2align 12\n"
    "L_pg1_%=:\n"
    "s_cbranch_execz L_tramp_%=\n"
    "s_cmp_eq_u32 s92, 8\n"
    "s_cselect_b64 exec, -1, 0\n"
    "ds_read_b64 a[6:7], v54 offset:0\n"
    "ds_read_b64 a[8:9], v55 offset:0\n"
    "ds_read_b64 a[10:11], v54 offset:2048\n"
    "ds_read_b64 a[12:13], v55 offset:2048\n"
    "s_mov_b64 exec, -1\n"
    "ds_read_b128 a[110:113], v8 offset:0\n"
    "ds_read_b128 a[114:117], v9 offset:0\n"
    "ds_read_b128 a[182:185], v8 offset:2048\n"
    "ds_read_b128 a[186:189], v9 offset:2048\n"
    "v_mfma_f32_16x16x32_bf16 v[76:79], v[42:45], a[102:105], v[76:79]\n"
    "s_add_u32 s81, s81, s79\n"
    "v_min_u32 v16, 7, v1\n"
    "v_lshl_add_u32 v16, v16, 1, s81\n"
    "v_mfma_f32_16x16x32_bf16 v[76:79], v[42:45], a[106:109], v[76:79]\n"
    "v_lshlrev_b32 v16, 10, v16\n"
    "v_add_u32 v16, 1048576, v16\n"
    "v_accvgpr_write_b32 a40, v16\n"
    "v_mfma_f32_16x16x32_bf16 v[80:83], v[42:45], a[174:177], v[80:83]\n"
    "s_sub_u32 s81, s81, s79\n"
    "v_min_u32 v16, 15, v1\n"
    "v_add_u32 v16, s81, v16\n"
    "v_mfma_f32_16x16x32_bf16 v[80:83], v[42:45], a[178:181], v[80:83]\n"
    "v_lshlrev_b32 v16, 10, v16\n"
    "s_lshr_b32 s80, s77, 2\n"
    "s_sub_u32 s80, s80, s78\n"
    "v_mfma_f32_16x16x32_bf16 v[84:87], a[102:105], a[102:105], v[84:87]\n"
    "s_lshl_b32 s80, s80, 14\n"
    "s_add_u32 s80, s80, 1048576\n"
    "v_add_u32 v16, s80, v16\n"
    "v_mfma_f32_16x16x32_bf16 v[84:87], a[106:109], a[106:109], v[84:87]\n"
    "v_accvgpr_write_b32 a41, v16\n"
    "s_mov_b32 s72, 0xffff0000\n"
    "s_mov_b32 s73, 0\n"
    "v_mfma_f32_16x16x32_bf16 v[88:91], a[174:177], a[174:177], v[88:91]\n"
    "v_mov_b32 v16, 0x7fa57fa5\n"
    "v_accvgpr_write_b32 a42, v16\n"
    "v_accvgpr_write_b32 a43, v16\n"
    "v_mfma_f32_16x16x32_bf16 v[88:91], a[178:181], a[178:181], v[88:91]\n"
    "v_accvgpr_write_b32 a44, v16\n"
    "v_accvgpr_write_b32 a45, v16\n"
    "s_waitcnt lgkmcnt(0)\n"
    "v_mfma_f32_16x16x32_bf16 v[76:79], v[42:45], a[110:113], v[76:79]\n"
    "v_mfma_f32_16x16x32_bf16 v[76:79], v[42:45], a[114:117], v[76:79]\n"
    "v_mfma_f32_16x16x32_bf16 v[80:83], v[42:45], a[182:185], v[80:83]\n"
    "v_mfma_f32_16x16x32_bf16 v[80:83], v[42:45], a[186:189], v[80:83]\n"
    "s_nop 1\n"
    "v_mov_b32 v188, 0\n"
    "v_mfma_f32_16x16x32_bf16 v[84:87], a[110:113], a[110:113], v[84:87]\n"
    "v_mov_b32 v189, 0\n"
    "v_mov_b32 v190, 0\n"
    "v_mfma_f32_16x16x32_bf16 v[84:87], a[114:117], a[114:117], v[84:87]\n"
    "v_mul_f32 v76, 0xbae38e39, v76\n"
    "v_mul_f32 v77, 0xbae38e39, v77\n"
    "v_mfma_f32_16x16x32_bf16 v[88:91], a[182:185], a[182:185], v[88:91]\n"
    "v_mul_f32 v78, 0xbae38e39, v78\n"
    "v_mul_f32 v79, 0xbae38e39, v79\n"
    "v_mfma_f32_16x16x32_bf16 v[88:91], a[186:189], a[186:189], v[88:91]\n"
    "v_mul_f32 v80, 0xbae38e39, v80\n"
    "v_mul_f32 v81, 0xbae38e39, v81\n"
    "v_mfma_f32_16x16x32_bf16 v[164:167], v[46:49], a[46:49], v[76:79]\n"
    "v_mul_f32 v82, 0xbae38e39, v82\n"
    "v_mul_f32 v83, 0xbae38e39, v83\n"
    "v_mfma_f32_16x16x32_bf16 v[168:171], v[50:53], a[46:49], v[76:79]\n"
    "v_mov_b32 v191, 0\n"
    "s_add_u32 s97, s91, 4096\n"
    "v_mfma_f32_16x16x32_bf16 v[172:175], v[46:49], a[118:121], v[80:83]\n"
    "buffer_load_dword a0, v56, s[8:11], 0 offen offset:0\n"
    "s_add_u32 s65, s65, s64\n"
    "v_mfma_f32_16x16x32_bf16 v[176:179], v[50:53], a[118:121], v[80:83]\n"
    "v_cvt_pk_bf16_f32 v92, v164, v165\n"
    "v_cvt_pk_bf16_f32 v93, v166, v167\n"
    "v_mfma_f32_16x16x32_bf16 v[180:183], v[46:49], a[50:53], v[76:79]\n"
    "v_cvt_pk_bf16_f32 v94, v168, v169\n"
    "v_cvt_pk_bf16_f32 v95, v170, v171\n"
    "v_mfma_f32_16x16x32_bf16 v[184:187], v[50:53], a[50:53], v[76:79]\n"
    "buffer_load_dword a0, v57, s[16:19], 0 offen offset:0\n"
    "v_cvt_pk_bf16_f32 v172, v172, v173\n"
    "v_mfma_f32_16x16x32_bf16 v[164:167], v[46:49], a[122:125], v[80:83]\n"
    "buffer_load_dword v27, v25, s[28:31], s97 offen offset:0 sc1\n"
    "v_cvt_pk_bf16_f32 v174, v174, v175\n"
    "v_mfma_f32_16x16x32_bf16 v[168:171], v[50:53], a[122:125], v[80:83]\n"
    "v_accvgpr_write_b32 a118, v172\n"
    "v_accvgpr_write_b32 a119, v174\n"
    "v_mfma_f32_16x16x32_bf16 v[172:175], v[46:49], a[54:57], v[76:79]\n"
    "buffer_store_dwordx4 v[188:191], v28, s[28:31], s90 offen offset:0 sc1\n"
    "s_waitcnt vmcnt(9)\n"
    "s_barrier\n"
    "ds_read_b64_tr_b16 a[190:191], v64 offset:8192\n"
    "v_cvt_pk_bf16_f32 v176, v176, v177\n"
    "v_cvt_pk_bf16_f32 v178, v178, v179\n"
    "v_accvgpr_write_b32 a120, v176\n"
    "v_accvgpr_write_b32 a121, v178\n"
    "v_mfma_f32_16x16x32_bf16 v[176:179], v[50:53], a[54:57], v[76:79]\n"
    "ds_read_b64_tr_b16 a[192:193], v64 offset:10752\n"
    "v_cvt_pk_bf16_f32 v96, v180, v181\n"
    "v_cvt_pk_bf16_f32 v97, v182, v183\n"
    "v_mfma_f32_16x16x32_bf16 v[180:183], v[46:49], a[126:129], v[80:83]\n"
    "ds_read_b64_tr_b16 a[194:195], v64 offset:13312\n"
    "v_cvt_pk_bf16_f32 v98, v184, v185\n"
    "v_cvt_pk_bf16_f32 v99, v186, v187\n"
    "v_mfma_f32_16x16x32_bf16 v[184:187], v[50:53], a[126:129], v[80:83]\n"
    "ds_read_b64_tr_b16 a[196:197], v64 offset:15872\n"
    "v_cvt_pk_bf16_f32 v164, v164, v165\n"
    "v_cvt_pk_bf16_f32 v166, v166, v167\n"
    "v_accvgpr_write_b32 a122, v164\n"
    "v_accvgpr_write_b32 a123, v166\n"
    "v_mfma_f32_16x16x32_bf16 v[164:167], v[46:49], a[58:61], v[76:79]\n"
    "ds_read_b64_tr_b16 a[198:199], v64 offset:8232\n"
    "v_cvt_pk_bf16_f32 v168, v168, v169\n"
    "v_cvt_pk_bf16_f32 v170, v170, v171\n"
    "v_accvgpr_write_b32 a124, v168\n"
    "v_accvgpr_write_b32 a125, v170\n"
    "v_mfma_f32_16x16x32_bf16 v[168:171], v[50:53], a[58:61], v[76:79]\n"
    "ds_read_b64_tr_b16 a[200:201], v64 offset:10792\n"
    "v_cvt_pk_bf16_f32 v100, v172, v173\n"
    "v_cvt_pk_bf16_f32 v101, v174, v175\n"
    "v_mfma_f32_16x16x32_bf16 v[172:175], v[46:49], a[130:133], v[80:83]\n"
    "ds_read_b64_tr_b16 a[202:203], v64 offset:13352\n"
    "v_cvt_pk_bf16_f32 v102, v176, v177\n"
    "v_cvt_pk_bf16_f32 v103, v178, v179\n"
    "s_add_u32 m0, s86, 0\n"
    "v_mfma_f32_16x16x32_bf16 v[176:179], v[50:53], a[130:133], v[80:83]\n"
    "ds_read_b64_tr_b16 a[204:205], v64 offset:15912\n"
    "buffer_load_dwordx4 v20, s[60:63], s65 offen offset:0 lds\n"
    "s_waitcnt lgkmcnt(5)\n"
    "v_mfma_f32_16x16x32_bf16 v[192:195], a[190:193], v[92:95], 0\n"
    "ds_read_b64_tr_b16 a[206:207], v64 offset:8264\n"
    "v_cvt_pk_bf16_f32 v180, v180, v181\n"
    "v_mfma_f32_16x16x32_bf16 v[196:199], a[190:193], a[118:121], 0\n"
    "ds_read_b64_tr_b16 a[208:209], v64 offset:10824\n"
    "s_waitcnt lgkmcnt(5)\n"
    "v_mfma_f32_16x16x32_bf16 v[196:199], a[194:197], a[122:125], v[196:199]\n"
    "ds_read_b64_tr_b16 a[210:211], v64 offset:13384\n"
    "buffer_load_dwordx4 v21, s[60:63], s65 offen offset:1024 lds\n"
    "v_mfma_f32_16x16x32_bf16 v[192:195], a[194:197], v[96:99], v[192:195]\n"
    "ds_read_b64_tr_b16 a[212:213], v64 offset:15944\n"
    "v_cvt_pk_bf16_f32 v182, v182, v183\n"
    "v_accvgpr_write_b32 a126, v180\n"
    "v_accvgpr_write_b32 a127, v182\n"
    "v_mfma_f32_16x16x32_bf16 v[180:183], v[46:49], a[62:65], v[76:79]\n"
    "ds_read_b64_tr_b16 a[214:215], v64 offset:8304\n"
    "buffer_load_dwordx4 v22, s[60:63], s65 offen offset:2048 lds\n"
    "v_cvt_pk_bf16_f32 v184, v184, v185\n"
    "v_cvt_pk_bf16_f32 v186, v186, v187\n"
    "v_accvgpr_write_b32 a128, v184\n"
    "v_accvgpr_write_b32 a129, v186\n"
    "v_mfma_f32_16x16x32_bf16 v[184:187], v[50:53], a[62:65], v[76:79]\n"
    "ds_read_b64_tr_b16 a[216:217], v64 offset:10864\n"
    "s_waitcnt lgkmcnt(6)\n"
    "v_mfma_f32_16x16x32_bf16 v[200:203], a[198:201], v[92:95], 0\n"
    "ds_read_b64_tr_b16 a[218:219], v64 offset:13424\n"
    "v_cvt_pk_bf16_f32 v104, v164, v165\n"
    "v_mfma_f32_16x16x32_bf16 v[204:207], a[198:201], a[118:121], 0\n"
    "ds_read_b64_tr_b16 a[220:221], v64 offset:15984\n"
    "buffer_load_dwordx4 v23, s[60:63], s65 offen offset:3072 lds\n"
    "s_add_u32 m0, s86, 4096\n"
    "v_mfma_f32_16x16x32_bf16 v[204:207], a[202:205], a[122:125], v[204:207]\n"
    "ds_read_b64_tr_b16 a[222:223], v64 offset:18432\n"
    "v_cvt_pk_bf16_f32 v105, v166, v167\n"
    "v_mfma_f32_16x16x32_bf16 v[200:203], a[202:205], v[96:99], v[200:203]\n"
    "ds_read_b64_tr_b16 a[224:225], v64 offset:20992\n"
    "s_waitcnt lgkmcnt(6)\n"
    "v_mfma_f32_16x16x32_bf16 v[208:211], a[206:209], v[92:95], 0\n"
    "ds_read_b64_tr_b16 a[226:227], v64 offset:23552\n"
    "buffer_load_dwordx4 v24, s[60:63], s65 offen offset:0 lds\n"
    "v_mfma_f32_16x16x32_bf16 v[212:215], a[206:209], a[118:121], 0\n"
    "ds_read_b64_tr_b16 a[228:229], v64 offset:26112\n"
    "v_cvt_pk_bf16_f32 v106, v168, v169\n"
    "v_mfma_f32_16x16x32_bf16 v[212:215], a[210:213], a[122:125], v[212:215]\n"
    "ds_read_b64_tr_b16 a[230:231], v64 offset:18472\n"
    "v_cvt_pk_bf16_f32 v107, v170, v171\n"
    "v_mfma_f32_16x16x32_bf16 v[208:211], a[210:213], v[96:99], v[208:211]\n"
    "ds_read_b64_tr_b16 a[232:233], v64 offset:21032\n"
    "v_cvt_pk_bf16_f32 v172, v172, v173\n"
    "v_mfma_f32_16x16x32_bf16 v[164:167], v[46:49], a[134:137], v[80:83]\n"
    "ds_read_b64_tr_b16 a[234:235], v64 offset:23592\n"
    "v_cvt_pk_bf16_f32 v174, v174, v175\n"
    "v_mfma_f32_16x16x32_bf16 v[168:171], v[50:53], a[134:137], v[80:83]\n"
    "ds_read_b64_tr_b16 a[236:237], v64 offset:26152\n"
    "s_waitcnt lgkmcnt(7)\n"
    "v_mfma_f32_16x16x32_bf16 v[216:219], a[214:217], v[92:95], 0\n"
    "ds_read_b64_tr_b16 a[238:239], v64 offset:18504\n"
    "v_accvgpr_write_b32 a130, v172\n"
    "v_mfma_f32_16x16x32_bf16 v[220:223], a[214:217], a[118:121], 0\n"
    "ds_read_b64_tr_b16 a[240:241], v64 offset:21064\n"
    "v_accvgpr_write_b32 a131, v174\n"
    "v_mfma_f32_16x16x32_bf16 v[220:223], a[218:221], a[122:125], v[220:223]\n"
    "ds_read_b64_tr_b16 a[242:243], v64 offset:23624\n"
    "v_cvt_pk_bf16_f32 v176, v176, v177\n"
    "v_mfma_f32_16x16x32_bf16 v[216:219], a[218:221], v[96:99], v[216:219]\n"
    "ds_read_b64_tr_b16 a[244:245], v64 offset:26184\n"
    "s_waitcnt lgkmcnt(7)\n"
    "v_mfma_f32_16x16x32_bf16 v[224:227], a[222:225], v[92:95], 0\n"
    "ds_read_b64_tr_b16 a[246:247], v64 offset:18544\n"
    "v_cvt_pk_bf16_f32 v178, v178, v179\n"
    "v_mfma_f32_16x16x32_bf16 v[228:231], a[222:225], a[118:121], 0\n"
    "ds_read_b64_tr_b16 a[248:249], v64 offset:21104\n"
    "v_accvgpr_write_b32 a132, v176\n"
    "v_mfma_f32_16x16x32_bf16 v[228:231], a[226:229], a[122:125], v[228:231]\n"
    "ds_read_b64_tr_b16 a[250:251], v64 offset:23664\n"
    "s_waitcnt vmcnt(9) lgkmcnt(7)\n"
    "v_mfma_f32_16x16x32_bf16 v[224:227], a[226:229], v[96:99], v[224:227]\n"
    "s_barrier\n"
    "ds_read_b64_tr_b16 a[252:253], v64 offset:26224\n"
    "v_mfma_f32_16x16x32_bf16 v[172:175], v[46:49], a[66:69], v[76:79]\n"
    "ds_read_b64_tr_b16 a[190:191], v66 offset:8224\n"
    "v_accvgpr_write_b32 a133, v178\n"
    "v_mfma_f32_16x16x32_bf16 v[176:179], v[50:53], a[66:69], v[76:79]\n"
    "ds_read_b64_tr_b16 a[192:193], v66 offset:10784\n"
    "s_add_u32 s65, s65, s64\n"
    "v_mfma_f32_16x16x32_bf16 v[232:235], a[230:233], v[92:95], 0\n"
    "ds_read_b64_tr_b16 a[194:195], v66 offset:13344\n"
    "s_add_u32 m0, s86, 24576\n"
    "v_mfma_f32_16x16x32_bf16 v[236:239], a[230:233], a[118:121], 0\n"
    "ds_read_b64_tr_b16 a[196:197], v66 offset:15904\n"
    "buffer_load_dwordx4 v20, s[60:63], s65 offen offset:0 lds\n"
    "v_mfma_f32_16x16x32_bf16 v[236:239], a[234:237], a[122:125], v[236:239]\n"
    "ds_read_b64_tr_b16 a[198:199], v64 offset:32768\n"
    "v_cvt_pk_bf16_f32 v108, v180, v181\n"
    "v_mfma_f32_16x16x32_bf16 v[232:235], a[234:237], v[96:99], v[232:235]\n"
    "ds_read_b64_tr_b16 a[200:201], v64 offset:35328\n"
    "s_waitcnt lgkmcnt(7)\n"
    "v_mfma_f32_16x16x32_bf16 v[240:243], a[238:241], v[92:95], 0\n"
    "ds_read_b64_tr_b16 a[202:203], v64 offset:37888\n"
    "buffer_load_dwordx4 v21, s[60:63], s65 offen offset:1024 lds\n"
    "v_mfma_f32_16x16x32_bf16 v[244:247], a[238:241], a[118:121], 0\n"
    "ds_read_b64_tr_b16 a[204:205], v64 offset:40448\n"
    "v_cvt_pk_bf16_f32 v109, v182, v183\n"
    "v_mfma_f32_16x16x32_bf16 v[244:247], a[242:245], a[122:125], v[244:247]\n"
    "ds_read_b64_tr_b16 a[206:207], v64 offset:32808\n"
    "v_cvt_pk_bf16_f32 v110, v184, v185\n"
    "v_mfma_f32_16x16x32_bf16 v[240:243], a[242:245], v[96:99], v[240:243]\n"
    "ds_read_b64_tr_b16 a[208:209], v64 offset:35368\n"
    "s_cmp_eq_u32 s93, 0\n"
    "s_cselect_b64 exec, -1, 0\n"
    "buffer_load_dword a0, v58, s[16:19], 0 offen\n"
    "s_mov_b64 exec, -1\n"
    "v_mfma_f32_16x16x32_bf16 v[180:183], v[46:49], a[138:141], v[80:83]\n"
    "ds_read_b64_tr_b16 a[210:211], v64 offset:37928\n"
    "v_cvt_pk_bf16_f32 v111, v186, v187\n"
    "v_mfma_f32_16x16x32_bf16 v[184:187], v[50:53], a[138:141], v[80:83]\n"
    "ds_read_b64_tr_b16 a[212:213], v64 offset:40488\n"
    "v_cvt_pk_bf16_f32 v164, v164, v165\n"
    "v_mfma_f32_16x16x32_bf16 v[248:251], a[246:249], v[92:95], 0\n"
    "ds_read_b64_tr_b16 a[214:215], v64 offset:32840\n"
    "v_cvt_pk_bf16_f32 v166, v166, v167\n"
    "v_mfma_f32_16x16x32_bf16 v[252:255], a[246:249], a[118:121], 0\n"
    "ds_read_b64_tr_b16 a[216:217], v64 offset:35400\n"
    "s_waitcnt lgkmcnt(6)\n"
    "v_mfma_f32_16x16x32_bf16 v[252:255], a[250:253], a[122:125], v[252:255]\n"
    "ds_read_b64_tr_b16 a[218:219], v64 offset:37960\n"
    "v_accvgpr_write_b32 a134, v164\n"
    "v_mfma_f32_16x16x32_bf16 v[248:251], a[250:253], v[96:99], v[248:251]\n"
    "ds_read_b64_tr_b16 a[220:221], v64 offset:40520\n"
    "buffer_load_dwordx4 v22, s[60:63], s65 offen offset:2048 lds\n"
    "v_mfma_f32_16x16x32_bf16 v[2:5], a[190:193], v[92:95], 0\n"
    "ds_read_b64_tr_b16 a[222:223], v64 offset:32880\n"
    "v_accvgpr_write_b32 a135, v166\n"
    "v_mfma_f32_16x16x32_bf16 v[6:9], a[190:193], a[118:121], 0\n"
    "ds_read_b64_tr_b16 a[224:225], v64 offset:35440\n"
    "v_cvt_pk_bf16_f32 v168, v168, v169\n"
    "v_mfma_f32_16x16x32_bf16 v[6:9], a[194:197], a[122:125], v[6:9]\n"
    "ds_read_b64_tr_b16 a[226:227], v64 offset:38000\n"
    "buffer_load_dwordx4 v23, s[60:63], s65 offen offset:3072 lds\n"
    "v_mfma_f32_16x16x32_bf16 v[2:5], a[194:197], v[96:99], v[2:5]\n"
    "ds_read_b64_tr_b16 a[228:229], v64 offset:40560\n"
    "v_cvt_pk_bf16_f32 v170, v170, v171\n"
    "v_mfma_f32_16x16x32_bf16 v[192:195], a[198:201], v[100:103], v[192:195]\n"
    "ds_read_b64_tr_b16 a[230:231], v64 offset:43008\n"
    "buffer_load_dword v26, v25, s[28:31], s91 offen offset:0 sc1\n"
    "v_mfma_f32_16x16x32_bf16 v[196:199], a[198:201], a[126:129], v[196:199]\n"
    "ds_read_b64_tr_b16 a[232:233], v64 offset:45568\n"
    "v_accvgpr_write_b32 a136, v168\n"
    "v_mfma_f32_16x16x32_bf16 v[196:199], a[202:205], a[130:133], v[196:199]\n"
    "ds_read_b64_tr_b16 a[234:235], v64 offset:48128\n"
    "v_accvgpr_write_b32 a137, v170\n"
    "v_mfma_f32_16x16x32_bf16 v[192:195], a[202:205], v[104:107], v[192:195]\n"
    "ds_read_b64_tr_b16 a[236:237], v64 offset:50688\n"
    "s_add_u32 m0, s86, 28672\n"
    "v_mfma_f32_16x16x32_bf16 v[164:167], v[46:49], a[70:73], v[76:79]\n"
    "buffer_load_dwordx4 v24, s[60:63], s65 offen offset:0 lds\n"
    "v_cvt_pk_bf16_f32 v112, v172, v173\n"
    "v_mfma_f32_16x16x32_bf16 v[168:171], v[50:53], a[70:73], v[76:79]\n"
    "v_cvt_pk_bf16_f32 v113, v174, v175\n"
    "s_waitcnt lgkmcnt(5)\n"
    "v_mfma_f32_16x16x32_bf16 v[200:203], a[206:209], v[100:103], v[200:203]\n"
    "ds_read_b64_tr_b16 a[238:239], v64 offset:43048\n"
    "v_cvt_pk_bf16_f32 v114, v176, v177\n"
    "v_mfma_f32_16x16x32_bf16 v[204:207], a[206:209], a[126:129], v[204:207]\n"
    "ds_read_b64_tr_b16 a[240:241], v64 offset:45608\n"
    "v_cvt_pk_bf16_f32 v115, v178, v179\n"
    "v_mfma_f32_16x16x32_bf16 v[204:207], a[210:213], a[130:133], v[204:207]\n"
    "ds_read_b64_tr_b16 a[242:243], v64 offset:48168\n"
    "s_add_u32 s65, s65, s64\n"
    "v_mfma_f32_16x16x32_bf16 v[200:203], a[210:213], v[104:107], v[200:203]\n"
    "ds_read_b64_tr_b16 a[244:245], v64 offset:50728\n"
    "s_add_u32 m0, s86, 49152\n"
    "v_mfma_f32_16x16x32_bf16 v[208:211], a[214:217], v[100:103], v[208:211]\n"
    "ds_read_b64_tr_b16 a[246:247], v64 offset:43080\n"
    "buffer_load_dwordx4 v20, s[60:63], s65 offen offset:0 lds\n"
    "v_mfma_f32_16x16x32_bf16 v[212:215], a[214:217], a[126:129], v[212:215]\n"
    "ds_read_b64_tr_b16 a[248:249], v64 offset:45640\n"
    "v_cvt_pk_bf16_f32 v180, v180, v181\n"
    "v_mfma_f32_16x16x32_bf16 v[212:215], a[218:221], a[130:133], v[212:215]\n"
    "ds_read_b64_tr_b16 a[250:251], v64 offset:48200\n"
    "v_cvt_pk_bf16_f32 v182, v182, v183\n"
    "v_mfma_f32_16x16x32_bf16 v[208:211], a[218:221], v[104:107], v[208:211]\n"
    "ds_read_b64_tr_b16 a[252:253], v64 offset:50760\n"
    "v_accvgpr_write_b32 a138, v180\n"
    "v_mfma_f32_16x16x32_bf16 v[172:175], v[46:49], a[142:145], v[80:83]\n"
    "buffer_load_dwordx4 v21, s[60:63], s65 offen offset:1024 lds\n"
    "v_accvgpr_write_b32 a139, v182\n"
    "v_mfma_f32_16x16x32_bf16 v[176:179], v[50:53], a[142:145], v[80:83]\n"
    "v_cvt_pk_bf16_f32 v184, v184, v185\n"
    "v_cvt_pk_bf16_f32 v186, v186, v187\n"
    "v_mfma_f32_16x16x32_bf16 v[216:219], a[222:225], v[100:103], v[216:219]\n"
    "ds_read_b64_tr_b16 a[190:191], v64 offset:43120\n"
    "v_accvgpr_write_b32 a140, v184\n"
    "v_mfma_f32_16x16x32_bf16 v[220:223], a[222:225], a[126:129], v[220:223]\n"
    "ds_read_b64_tr_b16 a[192:193], v64 offset:45680\n"
    "s_waitcnt lgkmcnt(5)\n"
    "v_mfma_f32_16x16x32_bf16 v[220:223], a[226:229], a[130:133], v[220:223]\n"
    "ds_read_b64_tr_b16 a[194:195], v64 offset:48240\n"
    "s_cmp_eq_u32 s93, 1\n"
    "s_cselect_b64 exec, -1, 0\n"
    "buffer_load_dword a0, v58, s[16:19], 0 offen\n"
    "s_mov_b64 exec, -1\n"
    "v_mfma_f32_16x16x32_bf16 v[216:219], a[226:229], v[104:107], v[216:219]\n"
    "ds_read_b64_tr_b16 a[196:197], v64 offset:50800\n"
    "v_accvgpr_write_b32 a141, v186\n"
    "v_mfma_f32_16x16x32_bf16 v[224:227], a[230:233], v[100:103], v[224:227]\n"
    "ds_read_b64_tr_b16 a[198:199], v66 offset:32800\n"
    "v_cvt_pk_bf16_f32 v116, v164, v165\n"
    "v_mfma_f32_16x16x32_bf16 v[228:231], a[230:233], a[126:129], v[228:231]\n"
    "ds_read_b64_tr_b16 a[200:201], v66 offset:35360\n"
    "v_cvt_pk_bf16_f32 v117, v166, v167\n"
    "v_mfma_f32_16x16x32_bf16 v[228:231], a[234:237], a[130:133], v[228:231]\n"
    "ds_read_b64_tr_b16 a[202:203], v66 offset:37920\n"
    "s_waitcnt vmcnt(8) lgkmcnt(6)\n"
    "v_mfma_f32_16x16x32_bf16 v[224:227], a[234:237], v[104:107], v[224:227]\n"
    "s_barrier\n"
    "ds_read_b64_tr_b16 a[204:205], v66 offset:40480\n"
    "v_mfma_f32_16x16x32_bf16 v[180:183], v[46:49], a[74:77], v[76:79]\n"
    "ds_read_b64_tr_b16 a[206:207], v63 offset:0\n"
    "buffer_load_dwordx4 v22, s[60:63], s65 offen offset:2048 lds\n"
    "v_mfma_f32_16x16x32_bf16 v[184:187], v[50:53], a[74:77], v[76:79]\n"
    "ds_read_b64_tr_b16 a[208:209], v63 offset:2560\n"
    "v_cvt_pk_bf16_f32 v118, v168, v169\n"
    "v_mfma_f32_16x16x32_bf16 v[232:235], a[238:241], v[100:103], v[232:235]\n"
    "ds_read_b64_tr_b16 a[210:211], v63 offset:5120\n"
    "v_cvt_pk_bf16_f32 v119, v170, v171\n"
    "v_mfma_f32_16x16x32_bf16 v[236:239], a[238:241], a[126:129], v[236:239]\n"
    "ds_read_b64_tr_b16 a[212:213], v63 offset:7680\n"
    "buffer_load_dwordx4 v23, s[60:63], s65 offen offset:3072 lds\n"
    "v_mfma_f32_16x16x32_bf16 v[236:239], a[242:245], a[130:133], v[236:239]\n"
    "s_add_u32 m0, s86, 53248\n"
    "v_cvt_pk_bf16_f32 v172, v172, v173\n"
    "v_mfma_f32_16x16x32_bf16 v[232:235], a[242:245], v[104:107], v[232:235]\n"
    "v_cvt_pk_bf16_f32 v174, v174, v175\n"
    "v_accvgpr_write_b32 a142, v172\n"
    "v_mfma_f32_16x16x32_bf16 v[240:243], a[246:249], v[100:103], v[240:243]\n"
    "ds_read_b64_tr_b16 a[214:215], v63 offset:40\n"
    "v_accvgpr_write_b32 a143, v174\n"
    "v_mfma_f32_16x16x32_bf16 v[244:247], a[246:249], a[126:129], v[244:247]\n"
    "ds_read_b64_tr_b16 a[216:217], v63 offset:2600\n"
    "v_cvt_pk_bf16_f32 v176, v176, v177\n"
    "v_mfma_f32_16x16x32_bf16 v[244:247], a[250:253], a[130:133], v[244:247]\n"
    "ds_read_b64_tr_b16 a[218:219], v63 offset:5160\n"
    "buffer_load_dwordx4 v24, s[60:63], s65 offen offset:0 lds\n"
    "v_mfma_f32_16x16x32_bf16 v[240:243], a[250:253], v[104:107], v[240:243]\n"
    "ds_read_b64_tr_b16 a[220:221], v63 offset:7720\n"
    "s_add_u32 s65, s65, s64\n"
    "v_mfma_f32_16x16x32_bf16 v[164:167], v[46:49], a[146:149], v[80:83]\n"
    "ds_read_b64_tr_b16 a[222:223], v63 offset:72\n"
    "v_cvt_pk_bf16_f32 v178, v178, v179\n"
    "v_mfma_f32_16x16x32_bf16 v[168:171], v[50:53], a[146:149], v[80:83]\n"
    "ds_read_b64_tr_b16 a[224:225], v63 offset:2632\n"
    "s_waitcnt lgkmcnt(6)\n"
    "v_mfma_f32_16x16x32_bf16 v[248:251], a[190:193], v[100:103], v[248:251]\n"
    "ds_read_b64_tr_b16 a[226:227], v63 offset:5192\n"
    "v_accvgpr_write_b32 a144, v176\n"
    "v_mfma_f32_16x16x32_bf16 v[252:255], a[190:193], a[126:129], v[252:255]\n"
    "ds_read_b64_tr_b16 a[228:229], v63 offset:7752\n"
    "v_accvgpr_write_b32 a145, v178\n"
    "v_mfma_f32_16x16x32_bf16 v[252:255], a[194:197], a[130:133], v[252:255]\n"
    "s_add_u32 m0, s86, 73728\n"
    "v_cvt_pk_bf16_f32 v120, v180, v181\n"
    "v_mfma_f32_16x16x32_bf16 v[248:251], a[194:197], v[104:107], v[248:251]\n"
    "buffer_load_dwordx4 v20, s[60:63], s65 offen offset:0 lds\n"
    "v_cvt_pk_bf16_f32 v121, v182, v183\n"
    "v_mfma_f32_16x16x32_bf16 v[2:5], a[198:201], v[100:103], v[2:5]\n"
    "ds_read_b64_tr_b16 a[230:231], v63 offset:112\n"
    "v_cvt_pk_bf16_f32 v122, v184, v185\n"
    "v_mfma_f32_16x16x32_bf16 v[6:9], a[198:201], a[126:129], v[6:9]\n"
    "ds_read_b64_tr_b16 a[232:233], v63 offset:2672\n"
    "v_cvt_pk_bf16_f32 v123, v186, v187\n"
    "v_mfma_f32_16x16x32_bf16 v[6:9], a[202:205], a[130:133], v[6:9]\n"
    "ds_read_b64_tr_b16 a[234:235], v63 offset:5232\n"
    "v_cvt_pk_bf16_f32 v164, v164, v165\n"
    "v_mfma_f32_16x16x32_bf16 v[2:5], a[202:205], v[104:107], v[2:5]\n"
    "ds_read_b64_tr_b16 a[236:237], v63 offset:7792\n"
    "v_cvt_pk_bf16_f32 v166, v166, v167\n"
    "v_mfma_f32_16x16x32_bf16 v[192:195], a[206:209], v[108:111], v[192:195]\n"
    "ds_read_b64_tr_b16 a[238:239], v63 offset:10240\n"
    "buffer_load_dwordx4 v21, s[60:63], s65 offen offset:1024 lds\n"
    "v_mfma_f32_16x16x32_bf16 v[196:199], a[206:209], a[134:137], v[196:199]\n"
    "ds_read_b64_tr_b16 a[240:241], v63 offset:12800\n"
    "v_accvgpr_write_b32 a146, v164\n"
    "v_mfma_f32_16x16x32_bf16 v[196:199], a[210:213], a[138:141], v[196:199]\n"
    "ds_read_b64_tr_b16 a[242:243], v63 offset:15360\n"
    "v_accvgpr_write_b32 a147, v166\n"
    "v_mfma_f32_16x16x32_bf16 v[192:195], a[210:213], v[112:115], v[192:195]\n"
    "ds_read_b64_tr_b16 a[244:245], v63 offset:17920\n"
    "v_cvt_pk_bf16_f32 v168, v168, v169\n"
    "v_mfma_f32_16x16x32_bf16 v[172:175], v[46:49], a[78:81], v[76:79]\n"
    "v_cvt_pk_bf16_f32 v170, v170, v171\n"
    "v_accvgpr_write_b32 a148, v168\n"
    "v_mfma_f32_16x16x32_bf16 v[176:179], v[50:53], a[78:81], v[76:79]\n"
    "v_accvgpr_write_b32 a149, v170\n"
    "s_waitcnt lgkmcnt(5)\n"
    "v_mfma_f32_16x16x32_bf16 v[200:203], a[214:217], v[108:111], v[200:203]\n"
    "ds_read_b64_tr_b16 a[246:247], v63 offset:10280\n"
    "buffer_load_dwordx4 v22, s[60:63], s65 offen offset:2048 lds\n"
    "v_mfma_f32_16x16x32_bf16 v[204:207], a[214:217], a[134:137], v[204:207]\n"
    "ds_read_b64_tr_b16 a[248:249], v63 offset:12840\n"
    "v_cvt_pk_bf16_f32 v124, v172, v173\n"
    "v_mfma_f32_16x16x32_bf16 v[204:207], a[218:221], a[138:141], v[204:207]\n"
    "ds_read_b64_tr_b16 a[250:251], v63 offset:15400\n"
    "v_cvt_pk_bf16_f32 v125, v174, v175\n"
    "v_mfma_f32_16x16x32_bf16 v[200:203], a[218:221], v[112:115], v[200:203]\n"
    "ds_read_b64_tr_b16 a[252:253], v63 offset:17960\n"
    "s_cmp_eq_u32 s93, 2\n"
    "s_cselect_b64 exec, -1, 0\n"
    "buffer_load_dword a0, v58, s[16:19], 0 offen\n"
    "s_mov_b64 exec, -1\n"
    "v_mfma_f32_16x16x32_bf16 v[208:211], a[222:225], v[108:111], v[208:211]\n"
    "ds_read_b64_tr_b16 a[190:191], v63 offset:10312\n"
    "v_cvt_pk_bf16_f32 v126, v176, v177\n"
    "v_mfma_f32_16x16x32_bf16 v[212:215], a[222:225], a[134:137], v[212:215]\n"
    "ds_read_b64_tr_b16 a[192:193], v63 offset:12872\n"
    "v_cvt_pk_bf16_f32 v127, v178, v179\n"
    "v_mfma_f32_16x16x32_bf16 v[212:215], a[226:229], a[138:141], v[212:215]\n"
    "ds_read_b64_tr_b16 a[194:195], v63 offset:15432\n"
    "s_lshl_b32 s97, s91, 1\n"
    "v_mfma_f32_16x16x32_bf16 v[208:211], a[226:229], v[112:115], v[208:211]\n"
    "ds_read_b64_tr_b16 a[196:197], v63 offset:17992\n"
    "s_add_u32 s97, s97, 8192\n"
    "v_mfma_f32_16x16x32_bf16 v[180:183], v[46:49], a[150:153], v[80:83]\n"
    "v_add_u32 v75, s44, v75\n"
    "s_mov_b32 s66, 4128768\n"
    "v_mfma_f32_16x16x32_bf16 v[184:187], v[50:53], a[150:153], v[80:83]\n"
    "buffer_load_dwordx4 v23, s[60:63], s65 offen offset:3072 lds\n"
    "s_add_u32 m0, s86, 77824\n"
    "v_mfma_f32_16x16x32_bf16 v[216:219], a[230:233], v[108:111], v[216:219]\n"
    "ds_read_b64_tr_b16 a[198:199], v63 offset:10352\n"
    "s_mov_b32 s67, 4718592\n"
    "v_mfma_f32_16x16x32_bf16 v[220:223], a[230:233], a[134:137], v[220:223]\n"
    "ds_read_b64_tr_b16 a[200:201], v63 offset:12912\n"
    "s_waitcnt lgkmcnt(5)\n"
    "v_mfma_f32_16x16x32_bf16 v[220:223], a[234:237], a[138:141], v[220:223]\n"
    "ds_read_b64_tr_b16 a[202:203], v63 offset:15472\n"
    "buffer_load_dwordx4 v24, s[60:63], s65 offen offset:0 lds\n"
    "v_mfma_f32_16x16x32_bf16 v[216:219], a[234:237], v[112:115], v[216:219]\n"
    "ds_read_b64_tr_b16 a[204:205], v63 offset:18032\n"
    "s_add_u32 s65, s65, s64\n"
    "v_mfma_f32_16x16x32_bf16 v[224:227], a[238:241], v[108:111], v[224:227]\n"
    "ds_read_b64_tr_b16 a[206:207], v65 offset:32\n"
    "v_cvt_pk_bf16_f32 v180, v180, v181\n"
    "v_mfma_f32_16x16x32_bf16 v[228:231], a[238:241], a[134:137], v[228:231]\n"
    "ds_read_b64_tr_b16 a[208:209], v65 offset:2592\n"
    "v_cvt_pk_bf16_f32 v182, v182, v183\n"
    "v_mfma_f32_16x16x32_bf16 v[228:231], a[242:245], a[138:141], v[228:231]\n"
    "ds_read_b64_tr_b16 a[210:211], v65 offset:5152\n"
    "s_waitcnt vmcnt(10) lgkmcnt(7)\n"
    "v_mfma_f32_16x16x32_bf16 v[224:227], a[242:245], v[112:115], v[224:227]\n"
    "s_barrier\n"
    "ds_read_b64_tr_b16 a[212:213], v65 offset:7712\n"
    "v_mfma_f32_16x16x32_bf16 v[164:167], v[46:49], a[82:85], v[76:79]\n"
    "ds_read_b64_tr_b16 a[214:215], v63 offset:24576\n"
    "v_accvgpr_write_b32 a150, v180\n"
    "v_mfma_f32_16x16x32_bf16 v[168:171], v[50:53], a[82:85], v[76:79]\n"
    "ds_read_b64_tr_b16 a[216:217], v63 offset:27136\n"
    "v_accvgpr_write_b32 a151, v182\n"
    "v_mfma_f32_16x16x32_bf16 v[232:235], a[246:249], v[108:111], v[232:235]\n"
    "ds_read_b64_tr_b16 a[218:219], v63 offset:29696\n"
    "s_add_u32 m0, s86, 98304\n"
    "v_mfma_f32_16x16x32_bf16 v[236:239], a[246:249], a[134:137], v[236:239]\n"
    "ds_read_b64_tr_b16 a[220:221], v63 offset:32256\n"
    "buffer_load_dwordx4 v20, s[60:63], s65 offen offset:0 lds\n"
    "v_mfma_f32_16x16x32_bf16 v[236:239], a[250:253], a[138:141], v[236:239]\n"
    "v_cvt_pk_bf16_f32 v184, v184, v185\n"
    "v_cvt_pk_bf16_f32 v186, v186, v187\n"
    "v_mfma_f32_16x16x32_bf16 v[232:235], a[250:253], v[112:115], v[232:235]\n"
    "v_accvgpr_write_b32 a152, v184\n"
    "v_accvgpr_write_b32 a153, v186\n"
    "v_mfma_f32_16x16x32_bf16 v[240:243], a[190:193], v[108:111], v[240:243]\n"
    "ds_read_b64_tr_b16 a[222:223], v63 offset:24616\n"
    "v_cvt_pk_bf16_f32 v128, v164, v165\n"
    "v_mfma_f32_16x16x32_bf16 v[244:247], a[190:193], a[134:137], v[244:247]\n"
    "ds_read_b64_tr_b16 a[224:225], v63 offset:27176\n"
    "v_cvt_pk_bf16_f32 v129, v166, v167\n"
    "v_mfma_f32_16x16x32_bf16 v[244:247], a[194:197], a[138:141], v[244:247]\n"
    "ds_read_b64_tr_b16 a[226:227], v63 offset:29736\n"
    "buffer_load_dwordx4 v21, s[60:63], s65 offen offset:1024 lds\n"
    "v_mfma_f32_16x16x32_bf16 v[240:243], a[194:197], v[112:115], v[240:243]\n"
    "ds_read_b64_tr_b16 a[228:229], v63 offset:32296\n"
    "v_cvt_pk_bf16_f32 v130, v168, v169\n"
    "v_mfma_f32_16x16x32_bf16 v[172:175], v[46:49], a[154:157], v[80:83]\n"
    "ds_read_b64_tr_b16 a[230:231], v63 offset:24648\n"
    "v_cvt_pk_bf16_f32 v131, v170, v171\n"
    "v_mfma_f32_16x16x32_bf16 v[176:179], v[50:53], a[154:157], v[80:83]\n"
    "ds_read_b64_tr_b16 a[232:233], v63 offset:27208\n"
    "s_waitcnt lgkmcnt(6)\n"
    "v_mfma_f32_16x16x32_bf16 v[248:251], a[198:201], v[108:111], v[248:251]\n"
    "ds_read_b64_tr_b16 a[234:235], v63 offset:29768\n"
    "v_lshrrev_b32 v16, 2, v14\n"
    "v_mfma_f32_16x16x32_bf16 v[252:255], a[198:201], a[134:137], v[252:255]\n"
    "ds_read_b64_tr_b16 a[236:237], v63 offset:32328\n"
    "v_cvt_pk_bf16_f32 v172, v172, v173\n"
    "v_mfma_f32_16x16x32_bf16 v[252:255], a[202:205], a[138:141], v[252:255]\n"
    "buffer_load_dwordx4 v22, s[60:63], s65 offen offset:2048 lds\n"
    "v_cvt_pk_bf16_f32 v174, v174, v175\n"
    "v_mfma_f32_16x16x32_bf16 v[248:251], a[202:205], v[112:115], v[248:251]\n"
    "v_accvgpr_write_b32 a154, v172\n"
    "v_accvgpr_write_b32 a155, v174\n"
    "v_mfma_f32_16x16x32_bf16 v[2:5], a[206:209], v[108:111], v[2:5]\n"
    "ds_read_b64_tr_b16 a[238:239], v63 offset:24688\n"
    "v_cvt_pk_bf16_f32 v176, v176, v177\n"
    "v_mfma_f32_16x16x32_bf16 v[6:9], a[206:209], a[134:137], v[6:9]\n"
    "ds_read_b64_tr_b16 a[240:241], v63 offset:27248\n"
    "s_cmp_eq_u32 s93, 3\n"
    "s_cselect_b64 exec, -1, 0\n"
    "buffer_load_dword a0, v58, s[16:19], 0 offen\n"
    "s_mov_b64 exec, -1\n"
    "v_mfma_f32_16x16x32_bf16 v[6:9], a[210:213], a[138:141], v[6:9]\n"
    "ds_read_b64_tr_b16 a[242:243], v63 offset:29808\n"
    "v_cvt_pk_bf16_f32 v178, v178, v179\n"
    "v_mfma_f32_16x16x32_bf16 v[2:5], a[210:213], v[112:115], v[2:5]\n"
    "ds_read_b64_tr_b16 a[244:245], v63 offset:32368\n"
    "v_accvgpr_write_b32 a156, v176\n"
    "v_mfma_f32_16x16x32_bf16 v[192:195], a[214:217], v[116:119], v[192:195]\n"
    ".p2align 12\n"
    "L_pg2_%=:\n"
    "s_cbranch_execz L_tramp_%=\n"
    "ds_read_b64_tr_b16 a[246:247], v63 offset:34816\n"
    "v_accvgpr_write_b32 a157, v178\n"
    "v_mfma_f32_16x16x32_bf16 v[196:199], a[214:217], a[142:145], v[196:199]\n"
    "ds_read_b64_tr_b16 a[248:249], v63 offset:37376\n"
    "v_lshl_add_u32 v17, v16, 4, v14\n"
    "v_mfma_f32_16x16x32_bf16 v[196:199], a[218:221], a[146:149], v[196:199]\n"
    "ds_read_b64_tr_b16 a[250:251], v63 offset:39936\n"
    "v_lshlrev_b32 v17, 2, v17\n"
    "v_mfma_f32_16x16x32_bf16 v[192:195], a[218:221], v[120:123], v[192:195]\n"
    "ds_read_b64_tr_b16 a[252:253], v63 offset:42496\n"
    "buffer_load_dwordx4 v23, s[60:63], s65 offen offset:3072 lds\n"
    "v_mfma_f32_16x16x32_bf16 v[180:183], v[46:49], a[86:89], v[76:79]\n"
    "s_add_u32 m0, s86, 102400\n"
    "v_add_u32 v27, 1, v27\n"
    "v_mfma_f32_16x16x32_bf16 v[184:187], v[50:53], a[86:89], v[76:79]\n"
    "v_and_b32 v16, 3, v14\n"
    "s_waitcnt lgkmcnt(5)\n"
    "v_mfma_f32_16x16x32_bf16 v[200:203], a[222:225], v[116:119], v[200:203]\n"
    "ds_read_b64_tr_b16 a[190:191], v63 offset:34856\n"
    "buffer_load_dwordx4 v24, s[60:63], s65 offen offset:0 lds\n"
    "v_mfma_f32_16x16x32_bf16 v[204:207], a[222:225], a[142:145], v[204:207]\n"
    "ds_read_b64_tr_b16 a[192:193], v63 offset:37416\n"
    "s_add_u32 s65, s65, s64\n"
    "v_mfma_f32_16x16x32_bf16 v[204:207], a[226:229], a[146:149], v[204:207]\n"
    "ds_read_b64_tr_b16 a[194:195], v63 offset:39976\n"
    "v_cvt_pk_bf16_f32 v132, v180, v181\n"
    "v_mfma_f32_16x16x32_bf16 v[200:203], a[226:229], v[120:123], v[200:203]\n"
    "ds_read_b64_tr_b16 a[196:197], v63 offset:42536\n"
    "v_cvt_pk_bf16_f32 v133, v182, v183\n"
    "v_mfma_f32_16x16x32_bf16 v[208:211], a[230:233], v[116:119], v[208:211]\n"
    "ds_read_b64_tr_b16 a[198:199], v63 offset:34888\n"
    "v_cvt_pk_bf16_f32 v134, v184, v185\n"
    "v_mfma_f32_16x16x32_bf16 v[212:215], a[230:233], a[142:145], v[212:215]\n"
    "ds_read_b64_tr_b16 a[200:201], v63 offset:37448\n"
    "v_cvt_pk_bf16_f32 v135, v186, v187\n"
    "v_mfma_f32_16x16x32_bf16 v[212:215], a[234:237], a[146:149], v[212:215]\n"
    "ds_read_b64_tr_b16 a[202:203], v63 offset:40008\n"
    "v_cmp_eq_u32 vcc, 1, v16\n"
    "v_mfma_f32_16x16x32_bf16 v[208:211], a[234:237], v[120:123], v[208:211]\n"
    "ds_read_b64_tr_b16 a[204:205], v63 offset:42568\n"
    "v_cndmask_b32 v84, v84, v85, vcc\n"
    "v_mfma_f32_16x16x32_bf16 v[164:167], v[46:49], a[158:161], v[80:83]\n"
    "buffer_load_dword a0, v60, s[12:15], 0 offen offset:0\n"
    "buffer_store_dwordx2 v[26:27], v19, s[28:31], s97 offen offset:0 sc1\n"
    "v_mfma_f32_16x16x32_bf16 v[168:171], v[50:53], a[158:161], v[80:83]\n"
    "v_cndmask_b32 v88, v88, v89, vcc\n"
    "v_cmp_eq_u32 vcc, 2, v16\n"
    "v_mfma_f32_16x16x32_bf16 v[216:219], a[238:241], v[116:119], v[216:219]\n"
    "ds_read_b64_tr_b16 a[206:207], v63 offset:34928\n"
    "v_cndmask_b32 v84, v84, v86, vcc\n"
    "v_mfma_f32_16x16x32_bf16 v[220:223], a[238:241], a[142:145], v[220:223]\n"
    "ds_read_b64_tr_b16 a[208:209], v63 offset:37488\n"
    "s_waitcnt lgkmcnt(5)\n"
    "v_mfma_f32_16x16x32_bf16 v[220:223], a[242:245], a[146:149], v[220:223]\n"
    "ds_read_b64_tr_b16 a[210:211], v63 offset:40048\n"
    "v_cvt_pk_bf16_f32 v164, v164, v165\n"
    "v_mfma_f32_16x16x32_bf16 v[216:219], a[242:245], v[120:123], v[216:219]\n"
    "ds_read_b64_tr_b16 a[212:213], v63 offset:42608\n"
    "v_cvt_pk_bf16_f32 v166, v166, v167\n"
    "v_mfma_f32_16x16x32_bf16 v[224:227], a[246:249], v[116:119], v[224:227]\n"
    "ds_read_b64_tr_b16 a[214:215], v65 offset:24608\n"
    "v_accvgpr_write_b32 a158, v164\n"
    "v_mfma_f32_16x16x32_bf16 v[228:231], a[246:249], a[142:145], v[228:231]\n"
    "ds_read_b64_tr_b16 a[216:217], v65 offset:27168\n"
    "v_accvgpr_write_b32 a159, v166\n"
    "v_mfma_f32_16x16x32_bf16 v[228:231], a[250:253], a[146:149], v[228:231]\n"
    "ds_read_b64_tr_b16 a[218:219], v65 offset:29728\n"
    "s_waitcnt vmcnt(12) lgkmcnt(7)\n"
    "v_mfma_f32_16x16x32_bf16 v[224:227], a[250:253], v[120:123], v[224:227]\n"
    "s_barrier\n"
    "ds_read_b64_tr_b16 a[220:221], v65 offset:32288\n"
    "v_mfma_f32_16x16x32_bf16 v[172:175], v[46:49], a[90:93], v[76:79]\n"
    "ds_read_b64_tr_b16 a[222:223], v63 offset:49152\n"
    "v_cvt_pk_bf16_f32 v168, v168, v169\n"
    "v_mfma_f32_16x16x32_bf16 v[176:179], v[50:53], a[90:93], v[76:79]\n"
    "ds_read_b64_tr_b16 a[224:225], v63 offset:51712\n"
    "v_cvt_pk_bf16_f32 v170, v170, v171\n"
    "v_mfma_f32_16x16x32_bf16 v[232:235], a[190:193], v[116:119], v[232:235]\n"
    "ds_read_b64_tr_b16 a[226:227], v63 offset:54272\n"
    "s_add_u32 m0, s86, 0\n"
    "v_mfma_f32_16x16x32_bf16 v[236:239], a[190:193], a[142:145], v[236:239]\n"
    "ds_read_b64_tr_b16 a[228:229], v63 offset:56832\n"
    "buffer_load_dwordx4 v20, s[60:63], s65 offen offset:0 lds\n"
    "v_mfma_f32_16x16x32_bf16 v[236:239], a[194:197], a[146:149], v[236:239]\n"
    "v_accvgpr_write_b32 a160, v168\n"
    "v_accvgpr_write_b32 a161, v170\n"
    "v_mfma_f32_16x16x32_bf16 v[232:235], a[194:197], v[120:123], v[232:235]\n"
    "v_cvt_pk_bf16_f32 v136, v172, v173\n"
    "v_cvt_pk_bf16_f32 v137, v174, v175\n"
    "v_mfma_f32_16x16x32_bf16 v[240:243], a[198:201], v[116:119], v[240:243]\n"
    "ds_read_b64_tr_b16 a[230:231], v63 offset:49192\n"
    "v_cvt_pk_bf16_f32 v138, v176, v177\n"
    "v_mfma_f32_16x16x32_bf16 v[244:247], a[198:201], a[142:145], v[244:247]\n"
    "ds_read_b64_tr_b16 a[232:233], v63 offset:51752\n"
    "v_cvt_pk_bf16_f32 v139, v178, v179\n"
    "v_mfma_f32_16x16x32_bf16 v[244:247], a[202:205], a[146:149], v[244:247]\n"
    "ds_read_b64_tr_b16 a[234:235], v63 offset:54312\n"
    "buffer_load_dwordx4 v21, s[60:63], s65 offen offset:1024 lds\n"
    "v_mfma_f32_16x16x32_bf16 v[240:243], a[202:205], v[120:123], v[240:243]\n"
    "ds_read_b64_tr_b16 a[236:237], v63 offset:56872\n"
    "v_cndmask_b32 v88, v88, v90, vcc\n"
    "v_mfma_f32_16x16x32_bf16 v[180:183], v[46:49], a[162:165], v[80:83]\n"
    "ds_read_b64_tr_b16 a[238:239], v63 offset:49224\n"
    "v_cmp_eq_u32 vcc, 3, v16\n"
    "v_mfma_f32_16x16x32_bf16 v[184:187], v[50:53], a[162:165], v[80:83]\n"
    "ds_read_b64_tr_b16 a[240:241], v63 offset:51784\n"
    "s_waitcnt lgkmcnt(6)\n"
    "v_mfma_f32_16x16x32_bf16 v[248:251], a[206:209], v[116:119], v[248:251]\n"
    "ds_read_b64_tr_b16 a[242:243], v63 offset:54344\n"
    "s_cmp_eq_u32 s93, 0\n"
    "s_cselect_b64 exec, -1, 0\n"
    "buffer_load_dword a0, v59, s[20:23], 0 offen\n"
    "s_mov_b64 exec, -1\n"
    "v_mfma_f32_16x16x32_bf16 v[252:255], a[206:209], a[142:145], v[252:255]\n"
    "ds_read_b64_tr_b16 a[244:245], v63 offset:56904\n"
    "v_cvt_pk_bf16_f32 v180, v180, v181\n"
    "v_mfma_f32_16x16x32_bf16 v[252:255], a[210:213], a[146:149], v[252:255]\n"
    "v_cvt_pk_bf16_f32 v182, v182, v183\n"
    "v_accvgpr_write_b32 a162, v180\n"
    "v_mfma_f32_16x16x32_bf16 v[248:251], a[210:213], v[120:123], v[248:251]\n"
    "v_accvgpr_write_b32 a163, v182\n"
    "v_cvt_pk_bf16_f32 v184, v184, v185\n"
    "v_mfma_f32_16x16x32_bf16 v[2:5], a[214:217], v[116:119], v[2:5]\n"
    "ds_read_b64_tr_b16 a[246:247], v63 offset:49264\n"
    "v_cvt_pk_bf16_f32 v186, v186, v187\n"
    "v_mfma_f32_16x16x32_bf16 v[6:9], a[214:217], a[142:145], v[6:9]\n"
    "ds_read_b64_tr_b16 a[248:249], v63 offset:51824\n"
    "v_accvgpr_write_b32 a164, v184\n"
    "v_mfma_f32_16x16x32_bf16 v[6:9], a[218:221], a[146:149], v[6:9]\n"
    "ds_read_b64_tr_b16 a[250:251], v63 offset:54384\n"
    "buffer_load_dwordx4 v22, s[60:63], s65 offen offset:2048 lds\n"
    "v_mfma_f32_16x16x32_bf16 v[2:5], a[218:221], v[120:123], v[2:5]\n"
    "ds_read_b64_tr_b16 a[252:253], v63 offset:56944\n"
    "v_accvgpr_write_b32 a165, v186\n"
    "v_mfma_f32_16x16x32_bf16 v[192:195], a[222:225], v[124:127], v[192:195]\n"
    "ds_read_b64_tr_b16 a[190:191], v63 offset:59392\n"
    "v_cndmask_b32 v84, v84, v87, vcc\n"
    "v_mfma_f32_16x16x32_bf16 v[196:199], a[222:225], a[150:153], v[196:199]\n"
    "ds_read_b64_tr_b16 a[192:193], v63 offset:61952\n"
    "buffer_load_dwordx4 v23, s[60:63], s65 offen offset:3072 lds\n"
    "v_mfma_f32_16x16x32_bf16 v[196:199], a[226:229], a[154:157], v[196:199]\n"
    "ds_read_b64_tr_b16 a[194:195], v63 offset:64512\n"
    "s_add_u32 m0, s86, 4096\n"
    "v_mfma_f32_16x16x32_bf16 v[192:195], a[226:229], v[128:131], v[192:195]\n"
    "ds_read_b64_tr_b16 a[196:197], v64 offset:1536\n"
    "v_mul_f32 v86, v76, v76\n"
    "v_mfma_f32_16x16x32_bf16 v[164:167], v[46:49], a[94:97], v[76:79]\n"
    "ds_bpermute_b32 v85, v17, v84\n"
    "v_mfma_f32_16x16x32_bf16 v[168:171], v[50:53], a[94:97], v[76:79]\n"
    "v_accvgpr_read_b32 v18, a35\n"
    "s_waitcnt lgkmcnt(6)\n"
    "v_mfma_f32_16x16x32_bf16 v[200:203], a[230:233], v[124:127], v[200:203]\n"
    "ds_read_b64_tr_b16 a[198:199], v63 offset:59432\n"
    "buffer_load_dwordx4 v24, s[60:63], s65 offen offset:0 lds\n"
    "v_mfma_f32_16x16x32_bf16 v[204:207], a[230:233], a[150:153], v[204:207]\n"
    "ds_read_b64_tr_b16 a[200:201], v63 offset:61992\n"
    "s_add_u32 s65, s65, s64\n"
    "v_mfma_f32_16x16x32_bf16 v[204:207], a[234:237], a[154:157], v[204:207]\n"
    "ds_read_b64_tr_b16 a[202:203], v63 offset:64552\n"
    "v_cvt_pk_bf16_f32 v140, v164, v165\n"
    "v_mfma_f32_16x16x32_bf16 v[200:203], a[234:237], v[128:131], v[200:203]\n"
    "ds_read_b64_tr_b16 a[204:205], v64 offset:1576\n"
    "v_cvt_pk_bf16_f32 v141, v166, v167\n"
    "v_mfma_f32_16x16x32_bf16 v[208:211], a[238:241], v[124:127], v[208:211]\n"
    "ds_read_b64_tr_b16 a[206:207], v63 offset:59464\n"
    "v_cvt_pk_bf16_f32 v142, v168, v169\n"
    "v_mfma_f32_16x16x32_bf16 v[212:215], a[238:241], a[150:153], v[212:215]\n"
    "ds_read_b64_tr_b16 a[208:209], v63 offset:62024\n"
    "v_cvt_pk_bf16_f32 v143, v170, v171\n"
    "v_mfma_f32_16x16x32_bf16 v[212:215], a[242:245], a[154:157], v[212:215]\n"
    "ds_read_b64_tr_b16 a[210:211], v63 offset:64584\n"
    "s_mov_b32 s64, 2949120\n"
    "v_mfma_f32_16x16x32_bf16 v[208:211], a[242:245], v[128:131], v[208:211]\n"
    "ds_read_b64_tr_b16 a[212:213], v64 offset:1608\n"
    "v_accvgpr_read_b32 v45, a30\n"
    "v_mfma_f32_16x16x32_bf16 v[172:175], v[46:49], a[166:169], v[80:83]\n"
    "s_waitcnt lgkmcnt(6)\n"
    "v_mul_f32 v85, 0x3ae38e39, v85\n"
    "v_mfma_f32_16x16x32_bf16 v[176:179], v[50:53], a[166:169], v[80:83]\n"
    "v_sub_f32 v85, v85, v86\n"
    "v_max_f32 v85, 0, v85\n"
    "v_mfma_f32_16x16x32_bf16 v[216:219], a[246:249], v[124:127], v[216:219]\n"
    "ds_read_b64_tr_b16 a[214:215], v63 offset:59504\n"
    "v_add_f32 v85, 0x3727c5ac, v85\n"
    "v_mfma_f32_16x16x32_bf16 v[220:223], a[246:249], a[150:153], v[220:223]\n"
    "ds_read_b64_tr_b16 a[216:217], v63 offset:62064\n"
    "v_cvt_pk_bf16_f32 v172, v172, v173\n"
    "v_mfma_f32_16x16x32_bf16 v[220:223], a[250:253], a[154:157], v[220:223]\n"
    "ds_read_b64_tr_b16 a[218:219], v63 offset:64624\n"
    "v_cvt_pk_bf16_f32 v174, v174, v175\n"
    "v_mfma_f32_16x16x32_bf16 v[216:219], a[250:253], v[128:131], v[216:219]\n"
    "ds_read_b64_tr_b16 a[220:221], v64 offset:1648\n"
    "v_accvgpr_write_b32 a166, v172\n"
    "v_mfma_f32_16x16x32_bf16 v[224:227], a[190:193], v[124:127], v[224:227]\n"
    "ds_read_b64_tr_b16 a[222:223], v65 offset:49184\n"
    "v_accvgpr_write_b32 a167, v174\n"
    "v_mfma_f32_16x16x32_bf16 v[228:231], a[190:193], a[150:153], v[228:231]\n"
    "ds_read_b64_tr_b16 a[224:225], v65 offset:51744\n"
    "v_cvt_pk_bf16_f32 v176, v176, v177\n"
    "v_mfma_f32_16x16x32_bf16 v[228:231], a[194:197], a[154:157], v[228:231]\n"
    "ds_read_b64_tr_b16 a[226:227], v65 offset:54304\n"
    "s_waitcnt vmcnt(12) lgkmcnt(7)\n"
    "v_mfma_f32_16x16x32_bf16 v[224:227], a[194:197], v[128:131], v[224:227]\n"
    "s_barrier\n"
    "ds_read_b64_tr_b16 a[228:229], v65 offset:56864\n"
    "v_mfma_f32_16x16x32_bf16 v[180:183], v[46:49], a[98:101], v[76:79]\n"
    "ds_read_b64_tr_b16 a[230:231], v64 offset:8192\n"
    "s_cmp_eq_u32 s93, 1\n"
    "s_cselect_b64 exec, -1, 0\n"
    "buffer_load_dword a0, v59, s[20:23], 0 offen\n"
    "s_mov_b64 exec, -1\n"
    "v_mfma_f32_16x16x32_bf16 v[184:187], v[50:53], a[98:101], v[76:79]\n"
    "ds_read_b64_tr_b16 a[232:233], v64 offset:10752\n"
    "v_cvt_pk_bf16_f32 v178, v178, v179\n"
    "v_mfma_f32_16x16x32_bf16 v[232:235], a[198:201], v[124:127], v[232:235]\n"
    "ds_read_b64_tr_b16 a[234:235], v64 offset:13312\n"
    "s_add_u32 m0, s86, 24576\n"
    "v_mfma_f32_16x16x32_bf16 v[236:239], a[198:201], a[150:153], v[236:239]\n"
    "ds_read_b64_tr_b16 a[236:237], v64 offset:15872\n"
    "v_accvgpr_write_b32 a168, v176\n"
    "v_mfma_f32_16x16x32_bf16 v[236:239], a[202:205], a[154:157], v[236:239]\n"
    "v_accvgpr_write_b32 a169, v178\n"
    "v_cvt_pk_bf16_f32 v144, v180, v181\n"
    "v_mfma_f32_16x16x32_bf16 v[232:235], a[202:205], v[128:131], v[232:235]\n"
    "v_cvt_pk_bf16_f32 v145, v182, v183\n"
    "v_cvt_pk_bf16_f32 v146, v184, v185\n"
    "v_mfma_f32_16x16x32_bf16 v[240:243], a[206:209], v[124:127], v[240:243]\n"
    "ds_read_b64_tr_b16 a[238:239], v64 offset:8232\n"
    "buffer_load_dwordx4 v20, s[60:63], s65 offen offset:0 lds\n"
    "v_mfma_f32_16x16x32_bf16 v[244:247], a[206:209], a[150:153], v[244:247]\n"
    "ds_read_b64_tr_b16 a[240:241], v64 offset:10792\n"
    "v_cvt_pk_bf16_f32 v147, v186, v187\n"
    "v_mfma_f32_16x16x32_bf16 v[244:247], a[210:213], a[154:157], v[244:247]\n"
    "ds_read_b64_tr_b16 a[242:243], v64 offset:13352\n"
    "v_rsq_f32 v84, v85\n"
    "v_mfma_f32_16x16x32_bf16 v[240:243], a[210:213], v[128:131], v[240:243]\n"
    "ds_read_b64_tr_b16 a[244:245], v64 offset:15912\n"
    "buffer_load_dwordx4 v21, s[60:63], s65 offen offset:1024 lds\n"
    "v_mfma_f32_16x16x32_bf16 v[164:167], v[46:49], a[170:173], v[80:83]\n"
    "ds_read_b64_tr_b16 a[246:247], v64 offset:8264\n"
    "v_readlane_b32 s80, v45, 0\n"
    "v_mfma_f32_16x16x32_bf16 v[168:171], v[50:53], a[170:173], v[80:83]\n"
    "ds_read_b64_tr_b16 a[248:249], v64 offset:10824\n"
    "s_waitcnt lgkmcnt(6)\n"
    "v_mfma_f32_16x16x32_bf16 v[248:251], a[214:217], v[124:127], v[248:251]\n"
    "ds_read_b64_tr_b16 a[250:251], v64 offset:13384\n"
    "v_mul_f32 v85, s80, v84\n"
    "v_mfma_f32_16x16x32_bf16 v[252:255], a[214:217], a[150:153], v[252:255]\n"
    "ds_read_b64_tr_b16 a[252:253], v64 offset:15944\n"
    "v_cvt_pk_bf16_f32 v164, v164, v165\n"
    "v_mfma_f32_16x16x32_bf16 v[252:255], a[218:221], a[154:157], v[252:255]\n"
    "buffer_load_dwordx4 v22, s[60:63], s65 offen offset:2048 lds\n"
    "v_cvt_pk_bf16_f32 v166, v166, v167\n"
    "v_mfma_f32_16x16x32_bf16 v[248:251], a[218:221], v[128:131], v[248:251]\n"
    "v_accvgpr_write_b32 a170, v164\n"
    "v_accvgpr_write_b32 a171, v166\n"
    "v_mfma_f32_16x16x32_bf16 v[2:5], a[222:225], v[124:127], v[2:5]\n"
    "ds_read_b64_tr_b16 a[190:191], v64 offset:8304\n"
    "v_cvt_pk_bf16_f32 v168, v168, v169\n"
    "v_mfma_f32_16x16x32_bf16 v[6:9], a[222:225], a[150:153], v[6:9]\n"
    "ds_read_b64_tr_b16 a[192:193], v64 offset:10864\n"
    "v_cvt_pk_bf16_f32 v170, v170, v171\n"
    "v_mfma_f32_16x16x32_bf16 v[6:9], a[226:229], a[154:157], v[6:9]\n"
    "ds_read_b64_tr_b16 a[194:195], v64 offset:13424\n"
    "v_accvgpr_write_b32 a172, v168\n"
    "v_mfma_f32_16x16x32_bf16 v[2:5], a[226:229], v[128:131], v[2:5]\n"
    "ds_read_b64_tr_b16 a[196:197], v64 offset:15984\n"
    "v_accvgpr_write_b32 a173, v170\n"
    "v_mfma_f32_16x16x32_bf16 v[192:195], a[230:233], v[132:135], v[192:195]\n"
    "ds_read_b64_tr_b16 a[198:199], v64 offset:18432\n"
    "buffer_load_dwordx4 v23, s[60:63], s65 offen offset:3072 lds\n"
    "v_mfma_f32_16x16x32_bf16 v[196:199], a[230:233], a[158:161], v[196:199]\n"
    "ds_read_b64_tr_b16 a[200:201], v64 offset:20992\n"
    "s_add_u32 m0, s86, 28672\n"
    "v_mfma_f32_16x16x32_bf16 v[196:199], a[234:237], a[162:165], v[196:199]\n"
    "ds_read_b64_tr_b16 a[202:203], v64 offset:23552\n"
    "v_cndmask_b32 v88, v88, v91, vcc\n"
    "v_mfma_f32_16x16x32_bf16 v[192:195], a[234:237], v[136:139], v[192:195]\n"
    "ds_read_b64_tr_b16 a[204:205], v64 offset:26112\n"
    "v_mul_f32 v90, v80, v80\n"
    "v_mfma_f32_16x16x32_bf16 v[172:175], v[46:49], a[102:105], v[76:79]\n"
    "ds_bpermute_b32 v89, v17, v88\n"
    "v_mfma_f32_16x16x32_bf16 v[176:179], v[50:53], a[102:105], v[76:79]\n"
    "v_add_u32 v54, 30720, v18\n"
    "s_waitcnt lgkmcnt(6)\n"
    "v_mfma_f32_16x16x32_bf16 v[200:203], a[238:241], v[132:135], v[200:203]\n"
    "ds_read_b64_tr_b16 a[206:207], v64 offset:18472\n"
    "buffer_load_dwordx4 v24, s[60:63], s65 offen offset:0 lds\n"
    "v_mfma_f32_16x16x32_bf16 v[204:207], a[238:241], a[158:161], v[204:207]\n"
    "ds_read_b64_tr_b16 a[208:209], v64 offset:21032\n"
    "v_cvt_pk_bf16_f32 v148, v172, v173\n"
    "v_mfma_f32_16x16x32_bf16 v[204:207], a[242:245], a[162:165], v[204:207]\n"
    "ds_read_b64_tr_b16 a[210:211], v64 offset:23592\n"
    "v_cvt_pk_bf16_f32 v149, v174, v175\n"
    "v_mfma_f32_16x16x32_bf16 v[200:203], a[242:245], v[136:139], v[200:203]\n"
    "ds_read_b64_tr_b16 a[212:213], v64 offset:26152\n"
    "v_cvt_pk_bf16_f32 v150, v176, v177\n"
    "v_mfma_f32_16x16x32_bf16 v[208:211], a[246:249], v[132:135], v[208:211]\n"
    "ds_read_b64_tr_b16 a[214:215], v64 offset:18504\n"
    "v_cvt_pk_bf16_f32 v151, v178, v179\n"
    "v_mfma_f32_16x16x32_bf16 v[212:215], a[246:249], a[158:161], v[212:215]\n"
    "ds_read_b64_tr_b16 a[216:217], v64 offset:21064\n"
    "s_mov_b32 s60, 589824\n"
    "v_mfma_f32_16x16x32_bf16 v[212:215], a[250:253], a[162:165], v[212:215]\n"
    "ds_read_b64_tr_b16 a[218:219], v64 offset:23624\n"
    "s_mov_b32 s61, 1179648\n"
    "v_mfma_f32_16x16x32_bf16 v[208:211], a[250:253], v[136:139], v[208:211]\n"
    "ds_read_b64_tr_b16 a[220:221], v64 offset:26184\n"
    "s_mov_b32 s62, 1769472\n"
    "v_mfma_f32_16x16x32_bf16 v[180:183], v[46:49], a[174:177], v[80:83]\n"
    "s_cmp_eq_u32 s93, 2\n"
    "s_cselect_b64 exec, -1, 0\n"
    "buffer_load_dword a0, v59, s[20:23], 0 offen\n"
    "s_mov_b64 exec, -1\n"
    "v_mfma_f32_16x16x32_bf16 v[184:187], v[50:53], a[174:177], v[80:83]\n"
    "s_mov_b32 s63, 2359296\n"
    "s_mov_b32 s65, 3538944\n"
    "v_mfma_f32_16x16x32_bf16 v[216:219], a[190:193], v[132:135], v[216:219]\n"
    "ds_read_b64_tr_b16 a[222:223], v64 offset:18544\n"
    "v_cvt_pk_bf16_f32 v180, v180, v181\n"
    "v_mfma_f32_16x16x32_bf16 v[220:223], a[190:193], a[158:161], v[220:223]\n"
    "ds_read_b64_tr_b16 a[224:225], v64 offset:21104\n"
    "s_waitcnt lgkmcnt(5)\n"
    "v_mfma_f32_16x16x32_bf16 v[220:223], a[194:197], a[162:165], v[220:223]\n"
    "ds_read_b64_tr_b16 a[226:227], v64 offset:23664\n"
    "v_cvt_pk_bf16_f32 v182, v182, v183\n"
    "v_mfma_f32_16x16x32_bf16 v[216:219], a[194:197], v[136:139], v[216:219]\n"
    "ds_read_b64_tr_b16 a[228:229], v64 offset:26224\n"
    "v_accvgpr_write_b32 a174, v180\n"
    "v_mfma_f32_16x16x32_bf16 v[224:227], a[198:201], v[132:135], v[224:227]\n"
    "ds_read_b64_tr_b16 a[230:231], v66 offset:8224\n"
    "v_accvgpr_write_b32 a175, v182\n"
    "v_mfma_f32_16x16x32_bf16 v[228:231], a[198:201], a[158:161], v[228:231]\n"
    "ds_read_b64_tr_b16 a[232:233], v66 offset:10784\n"
    "v_cvt_pk_bf16_f32 v184, v184, v185\n"
    "v_mfma_f32_16x16x32_bf16 v[228:231], a[202:205], a[162:165], v[228:231]\n"
    "ds_read_b64_tr_b16 a[234:235], v66 offset:13344\n"
    "s_waitcnt vmcnt(12) lgkmcnt(7)\n"
    "v_mfma_f32_16x16x32_bf16 v[224:227], a[202:205], v[136:139], v[224:227]\n"
    "s_barrier\n"
    "ds_read_b64_tr_b16 a[236:237], v66 offset:15904\n"
    "v_mfma_f32_16x16x32_bf16 v[164:167], v[46:49], a[106:109], v[76:79]\n"
    "ds_read_b64_tr_b16 a[238:239], v64 offset:32768\n"
    "v_cvt_pk_bf16_f32 v186, v186, v187\n"
    "v_mfma_f32_16x16x32_bf16 v[168:171], v[50:53], a[106:109], v[76:79]\n"
    "ds_read_b64_tr_b16 a[240:241], v64 offset:35328\n"
    "v_accvgpr_write_b32 a176, v184\n"
    "v_mfma_f32_16x16x32_bf16 v[232:235], a[206:209], v[132:135], v[232:235]\n"
    "ds_read_b64_tr_b16 a[242:243], v64 offset:37888\n"
    "s_add_u32 m0, s87, 49152\n"
    "v_mfma_f32_16x16x32_bf16 v[236:239], a[206:209], a[158:161], v[236:239]\n"
    "ds_read_b64_tr_b16 a[244:245], v64 offset:40448\n"
    "buffer_load_dwordx4 v61, s[16:19], 0 offen offset:0 lds\n"
    "v_mfma_f32_16x16x32_bf16 v[236:239], a[210:213], a[162:165], v[236:239]\n"
    "v_accvgpr_write_b32 a177, v186\n"
    "v_cvt_pk_bf16_f32 v152, v164, v165\n"
    "v_mfma_f32_16x16x32_bf16 v[232:235], a[210:213], v[136:139], v[232:235]\n"
    "v_cvt_pk_bf16_f32 v153, v166, v167\n"
    "v_cvt_pk_bf16_f32 v154, v168, v169\n"
    "v_mfma_f32_16x16x32_bf16 v[240:243], a[214:217], v[132:135], v[240:243]\n"
    "ds_read_b64_tr_b16 a[246:247], v64 offset:32808\n"
    "v_cvt_pk_bf16_f32 v155, v170, v171\n"
    "v_mfma_f32_16x16x32_bf16 v[244:247], a[214:217], a[158:161], v[244:247]\n"
    "ds_read_b64_tr_b16 a[248:249], v64 offset:35368\n"
    "v_mul_f32 v89, 0x3ae38e39, v89\n"
    "v_mfma_f32_16x16x32_bf16 v[244:247], a[218:221], a[162:165], v[244:247]\n"
    "ds_read_b64_tr_b16 a[250:251], v64 offset:37928\n"
    "buffer_load_dwordx4 v62, s[16:19], 0 offen offset:1024 lds\n"
    "v_mfma_f32_16x16x32_bf16 v[240:243], a[218:221], v[136:139], v[240:243]\n"
    "ds_read_b64_tr_b16 a[252:253], v64 offset:40488\n"
    "v_sub_f32 v89, v89, v90\n"
    "v_mfma_f32_16x16x32_bf16 v[172:175], v[46:49], a[178:181], v[80:83]\n"
    "ds_read_b64_tr_b16 a[190:191], v64 offset:32840\n"
    "v_max_f32 v89, 0, v89\n"
    "v_mfma_f32_16x16x32_bf16 v[176:179], v[50:53], a[178:181], v[80:83]\n"
    "ds_read_b64_tr_b16 a[192:193], v64 offset:35400\n"
    "s_waitcnt lgkmcnt(6)\n"
    "v_mfma_f32_16x16x32_bf16 v[248:251], a[222:225], v[132:135], v[248:251]\n"
    "ds_read_b64_tr_b16 a[194:195], v64 offset:37960\n"
    "v_add_f32 v89, 0x3727c5ac, v89\n"
    "v_mfma_f32_16x16x32_bf16 v[252:255], a[222:225], a[158:161], v[252:255]\n"
    "ds_read_b64_tr_b16 a[196:197], v64 offset:40520\n"
    "v_cvt_pk_bf16_f32 v172, v172, v173\n"
    "v_mfma_f32_16x16x32_bf16 v[252:255], a[226:229], a[162:165], v[252:255]\n"
    "s_add_u32 m0, s87, 57344\n"
    "v_cvt_pk_bf16_f32 v174, v174, v175\n"
    "v_mfma_f32_16x16x32_bf16 v[248:251], a[226:229], v[136:139], v[248:251]\n"
    "buffer_load_dwordx4 v61, s[16:19], s60 offen offset:0 lds\n"
    "v_accvgpr_write_b32 a178, v172\n"
    "v_mfma_f32_16x16x32_bf16 v[2:5], a[230:233], v[132:135], v[2:5]\n"
    "ds_read_b64_tr_b16 a[198:199], v64 offset:32880\n"
    "v_accvgpr_write_b32 a179, v174\n"
    "v_mfma_f32_16x16x32_bf16 v[6:9], a[230:233], a[158:161], v[6:9]\n"
    "ds_read_b64_tr_b16 a[200:201], v64 offset:35440\n"
    "v_cvt_pk_bf16_f32 v176, v176, v177\n"
    "v_mfma_f32_16x16x32_bf16 v[6:9], a[234:237], a[162:165], v[6:9]\n"
    "ds_read_b64_tr_b16 a[202:203], v64 offset:38000\n"
    "v_cvt_pk_bf16_f32 v178, v178, v179\n"
    "v_mfma_f32_16x16x32_bf16 v[2:5], a[234:237], v[136:139], v[2:5]\n"
    "ds_read_b64_tr_b16 a[204:205], v64 offset:40560\n"
    "v_accvgpr_write_b32 a180, v176\n"
    "v_mfma_f32_16x16x32_bf16 v[192:195], a[238:241], v[140:143], v[192:195]\n"
    "ds_read_b64_tr_b16 a[206:207], v64 offset:43008\n"
    "buffer_load_dwordx4 v62, s[16:19], s60 offen offset:1024 lds\n"
    "v_mfma_f32_16x16x32_bf16 v[196:199], a[238:241], a[166:169], v[196:199]\n"
    "ds_read_b64_tr_b16 a[208:209], v64 offset:45568\n"
    "v_accvgpr_write_b32 a181, v178\n"
    "v_mfma_f32_16x16x32_bf16 v[196:199], a[242:245], a[170:173], v[196:199]\n"
    "ds_read_b64_tr_b16 a[210:211], v64 offset:48128\n"
    "v_rsq_f32 v88, v89\n"
    "v_mfma_f32_16x16x32_bf16 v[192:195], a[242:245], v[144:147], v[192:195]\n"
    "ds_read_b64_tr_b16 a[212:213], v64 offset:50688\n"
    "buffer_load_dwordx2 a[46:47], v75, s[4:7], 0 offen offset:0\n"
    "v_mfma_f32_16x16x32_bf16 v[180:183], v[46:49], a[110:113], v[76:79]\n"
    "v_mul_f32 v89, s80, v88\n"
    "s_mul_i32 s85, s76, 3408896\n"
    "v_mfma_f32_16x16x32_bf16 v[184:187], v[50:53], a[110:113], v[76:79]\n"
    "s_add_u32 s46, s46, s85\n"
    "s_waitcnt lgkmcnt(5)\n"
    "v_mfma_f32_16x16x32_bf16 v[200:203], a[246:249], v[140:143], v[200:203]\n"
    "ds_read_b64_tr_b16 a[214:215], v64 offset:43048\n"
    "s_cmp_eq_u32 s93, 3\n"
    "s_cselect_b64 exec, -1, 0\n"
    "buffer_load_dword a0, v59, s[20:23], 0 offen\n"
    "s_mov_b64 exec, -1\n"
    "v_mfma_f32_16x16x32_bf16 v[204:207], a[246:249], a[166:169], v[204:207]\n"
    "ds_read_b64_tr_b16 a[216:217], v64 offset:45608\n"
    "s_add_u32 m0, s87, 65536\n"
    "v_mfma_f32_16x16x32_bf16 v[204:207], a[250:253], a[170:173], v[204:207]\n"
    "ds_read_b64_tr_b16 a[218:219], v64 offset:48168\n"
    "v_cvt_pk_bf16_f32 v156, v180, v181\n"
    "v_mfma_f32_16x16x32_bf16 v[200:203], a[250:253], v[144:147], v[200:203]\n"
    "ds_read_b64_tr_b16 a[220:221], v64 offset:50728\n"
    "v_cvt_pk_bf16_f32 v157, v182, v183\n"
    "v_mfma_f32_16x16x32_bf16 v[208:211], a[190:193], v[140:143], v[208:211]\n"
    "ds_read_b64_tr_b16 a[222:223], v64 offset:43080\n"
    "v_cvt_pk_bf16_f32 v158, v184, v185\n"
    "v_mfma_f32_16x16x32_bf16 v[212:215], a[190:193], a[166:169], v[212:215]\n"
    "ds_read_b64_tr_b16 a[224:225], v64 offset:45640\n"
    "v_cvt_pk_bf16_f32 v159, v186, v187\n"
    "v_mfma_f32_16x16x32_bf16 v[212:215], a[194:197], a[170:173], v[212:215]\n"
    "ds_read_b64_tr_b16 a[226:227], v64 offset:48200\n"
    "buffer_load_dwordx4 v61, s[16:19], s61 offen offset:0 lds\n"
    "v_mfma_f32_16x16x32_bf16 v[208:211], a[194:197], v[144:147], v[208:211]\n"
    "ds_read_b64_tr_b16 a[228:229], v64 offset:50760\n"
    "s_cmp_eq_u32 s79, 1\n"
    "s_cselect_b32 s95, s95, s46\n"
    "v_mfma_f32_16x16x32_bf16 v[164:167], v[46:49], a[182:185], v[80:83]\n"
    "s_mul_i32 s79, s79, 0x80008000\n"
    "v_readlane_b32 s81, v45, 32\n"
    "v_mfma_f32_16x16x32_bf16 v[168:171], v[50:53], a[182:185], v[80:83]\n"
    "buffer_load_dwordx4 v62, s[16:19], s61 offen offset:1024 lds\n"
    "s_mul_i32 s80, s78, 3072\n"
    "v_mfma_f32_16x16x32_bf16 v[216:219], a[198:201], v[140:143], v[216:219]\n"
    "ds_read_b64_tr_b16 a[230:231], v64 offset:43120\n"
    "s_bitcmp1_b32 s77, 0\n"
    "s_cselect_b32 s81, s81, 0\n"
    "s_cselect_b32 s82, 0, 1.0\n"
    "v_mfma_f32_16x16x32_bf16 v[220:223], a[198:201], a[166:169], v[220:223]\n"
    "ds_read_b64_tr_b16 a[232:233], v64 offset:45680\n"
    "s_waitcnt lgkmcnt(4)\n"
    "v_mfma_f32_16x16x32_bf16 v[220:223], a[202:205], a[170:173], v[220:223]\n"
    "ds_read_b64_tr_b16 a[234:235], v64 offset:48240\n"
    "buffer_load_dwordx2 a[48:49], v75, s[4:7], 0 offen offset:32\n"
    "v_mfma_f32_16x16x32_bf16 v[216:219], a[202:205], v[144:147], v[216:219]\n"
    "ds_read_b64_tr_b16 a[236:237], v64 offset:50800\n"
    "v_cvt_pk_bf16_f32 v164, v164, v165\n"
    "v_mfma_f32_16x16x32_bf16 v[224:227], a[206:209], v[140:143], v[224:227]\n"
    "ds_read_b64_tr_b16 a[238:239], v66 offset:32800\n"
    "v_cvt_pk_bf16_f32 v166, v166, v167\n"
    "v_mfma_f32_16x16x32_bf16 v[228:231], a[206:209], a[166:169], v[228:231]\n"
    "ds_read_b64_tr_b16 a[240:241], v66 offset:35360\n"
    "v_cvt_pk_bf16_f32 v168, v168, v169\n"
    "v_mfma_f32_16x16x32_bf16 v[228:231], a[210:213], a[170:173], v[228:231]\n"
    "ds_read_b64_tr_b16 a[242:243], v66 offset:37920\n"
    "s_waitcnt vmcnt(13) lgkmcnt(7)\n"
    "v_mfma_f32_16x16x32_bf16 v[224:227], a[210:213], v[144:147], v[224:227]\n"
    "s_barrier\n"
    "ds_read_b64_tr_b16 a[244:245], v66 offset:40480\n"
    "v_mfma_f32_16x16x32_bf16 v[172:175], v[46:49], a[114:117], v[76:79]\n"
    "ds_read_b64_tr_b16 a[246:247], v63 offset:0\n"
    "v_cvt_pk_bf16_f32 v170, v170, v171\n"
    "v_mfma_f32_16x16x32_bf16 v[176:179], v[50:53], a[114:117], v[76:79]\n"
    "ds_read_b64_tr_b16 a[248:249], v63 offset:2560\n"
    "v_accvgpr_write_b32 a182, v164\n"
    "v_mfma_f32_16x16x32_bf16 v[232:235], a[214:217], v[140:143], v[232:235]\n"
    "ds_read_b64_tr_b16 a[250:251], v63 offset:5120\n"
    "s_add_u32 m0, s87, 73728\n"
    "v_mfma_f32_16x16x32_bf16 v[236:239], a[214:217], a[166:169], v[236:239]\n"
    "ds_read_b64_tr_b16 a[252:253], v63 offset:7680\n"
    "buffer_load_dwordx4 v61, s[16:19], s62 offen offset:0 lds\n"
    "v_mfma_f32_16x16x32_bf16 v[236:239], a[218:221], a[170:173], v[236:239]\n"
    "ds_read_b64_tr_b16 a[190:191], v63 offset:40\n"
    "v_accvgpr_write_b32 a183, v166\n"
    "v_mfma_f32_16x16x32_bf16 v[232:235], a[218:221], v[144:147], v[232:235]\n"
    "ds_read_b64_tr_b16 a[192:193], v63 offset:2600\n"
    "v_accvgpr_write_b32 a184, v168\n"
    "v_mfma_f32_16x16x32_bf16 v[240:243], a[222:225], v[140:143], v[240:243]\n"
    "ds_read_b64_tr_b16 a[194:195], v63 offset:5160\n"
    "v_accvgpr_write_b32 a185, v170\n"
    "v_mfma_f32_16x16x32_bf16 v[244:247], a[222:225], a[166:169], v[244:247]\n"
    "ds_read_b64_tr_b16 a[196:197], v63 offset:7720\n"
    "v_cvt_pk_bf16_f32 v160, v172, v173\n"
    "v_mfma_f32_16x16x32_bf16 v[244:247], a[226:229], a[170:173], v[244:247]\n"
    "ds_read_b64_tr_b16 a[198:199], v63 offset:72\n"
    "buffer_load_dwordx4 v62, s[16:19], s62 offen offset:1024 lds\n"
    "v_mfma_f32_16x16x32_bf16 v[240:243], a[226:229], v[144:147], v[240:243]\n"
    "ds_read_b64_tr_b16 a[200:201], v63 offset:2632\n"
    "v_cvt_pk_bf16_f32 v161, v174, v175\n"
    "v_mfma_f32_16x16x32_bf16 v[180:183], v[46:49], a[186:189], v[80:83]\n"
    "ds_read_b64_tr_b16 a[202:203], v63 offset:5192\n"
    "v_cvt_pk_bf16_f32 v162, v176, v177\n"
    "v_mfma_f32_16x16x32_bf16 v[184:187], v[50:53], a[186:189], v[80:83]\n"
    "ds_read_b64_tr_b16 a[204:205], v63 offset:7752\n"
    "s_waitcnt lgkmcnt(7)\n"
    "v_mfma_f32_16x16x32_bf16 v[248:251], a[230:233], v[140:143], v[248:251]\n"
    "ds_read_b64_tr_b16 a[206:207], v63 offset:112\n"
    "v_cvt_pk_bf16_f32 v163, v178, v179\n"
    "v_mfma_f32_16x16x32_bf16 v[252:255], a[230:233], a[166:169], v[252:255]\n"
    "ds_read_b64_tr_b16 a[208:209], v63 offset:2672\n"
    "v_cvt_pk_bf16_f32 v180, v180, v181\n"
    "v_mfma_f32_16x16x32_bf16 v[252:255], a[234:237], a[170:173], v[252:255]\n"
    "ds_read_b64_tr_b16 a[210:211], v63 offset:5232\n"
    "s_add_u32 m0, s87, 81920\n"
    "v_mfma_f32_16x16x32_bf16 v[248:251], a[234:237], v[144:147], v[248:251]\n"
    "ds_read_b64_tr_b16 a[212:213], v63 offset:7792\n"
    "buffer_load_dwordx4 v61, s[16:19], s63 offen offset:0 lds\n"
    "v_mfma_f32_16x16x32_bf16 v[2:5], a[238:241], v[140:143], v[2:5]\n"
    "ds_read_b64_tr_b16 a[214:215], v63 offset:10240\n"
    "v_cvt_pk_bf16_f32 v182, v182, v183\n"
    "v_mfma_f32_16x16x32_bf16 v[6:9], a[238:241], a[166:169], v[6:9]\n"
    "ds_read_b64_tr_b16 a[216:217], v63 offset:12800\n"
    "v_cvt_pk_bf16_f32 v184, v184, v185\n"
    "v_mfma_f32_16x16x32_bf16 v[6:9], a[242:245], a[170:173], v[6:9]\n"
    "ds_read_b64_tr_b16 a[218:219], v63 offset:15360\n"
    "v_cvt_pk_bf16_f32 v186, v186, v187\n"
    "v_mfma_f32_16x16x32_bf16 v[2:5], a[242:245], v[144:147], v[2:5]\n"
    "ds_read_b64_tr_b16 a[220:221], v63 offset:17920\n"
    "v_accvgpr_write_b32 a186, v180\n"
    "v_mfma_f32_16x16x32_bf16 v[192:195], a[246:249], v[148:151], v[192:195]\n"
    "ds_read_b64_tr_b16 a[222:223], v63 offset:10280\n"
    "buffer_load_dwordx4 v62, s[16:19], s63 offen offset:1024 lds\n"
    "v_mfma_f32_16x16x32_bf16 v[196:199], a[246:249], a[174:177], v[196:199]\n"
    ".p2align 12\n"
    "L_pg3_%=:\n"
    "s_cbranch_execz L_tramp_%=\n"
    "ds_read_b64_tr_b16 a[224:225], v63 offset:12840\n"
    "v_accvgpr_write_b32 a187, v182\n"
    "v_mfma_f32_16x16x32_bf16 v[196:199], a[250:253], a[178:181], v[196:199]\n"
    "ds_read_b64_tr_b16 a[226:227], v63 offset:15400\n"
    "v_accvgpr_write_b32 a188, v184\n"
    "v_mfma_f32_16x16x32_bf16 v[192:195], a[250:253], v[152:155], v[192:195]\n"
    "ds_read_b64_tr_b16 a[228:229], v63 offset:17960\n"
    "s_waitcnt lgkmcnt(7)\n"
    "v_mfma_f32_16x16x32_bf16 v[200:203], a[190:193], v[148:151], v[200:203]\n"
    "ds_read_b64_tr_b16 a[230:231], v63 offset:10312\n"
    "v_accvgpr_write_b32 a189, v186\n"
    "v_mfma_f32_16x16x32_bf16 v[204:207], a[190:193], a[174:177], v[204:207]\n"
    "ds_read_b64_tr_b16 a[232:233], v63 offset:12872\n"
    "v_mul_f32 v86, s81, v84\n"
    "v_mfma_f32_16x16x32_bf16 v[204:207], a[194:197], a[178:181], v[204:207]\n"
    "ds_read_b64_tr_b16 a[234:235], v63 offset:15432\n"
    "s_add_u32 m0, s87, 90112\n"
    "v_mfma_f32_16x16x32_bf16 v[200:203], a[194:197], v[152:155], v[200:203]\n"
    "ds_read_b64_tr_b16 a[236:237], v63 offset:17992\n"
    "buffer_load_dwordx4 v61, s[16:19], s64 offen offset:0 lds\n"
    "v_mfma_f32_16x16x32_bf16 v[208:211], a[198:201], v[148:151], v[208:211]\n"
    "ds_read_b64_tr_b16 a[238:239], v63 offset:10352\n"
    "v_add_f32 v86, s82, v86\n"
    "v_mfma_f32_16x16x32_bf16 v[212:215], a[198:201], a[174:177], v[212:215]\n"
    "ds_read_b64_tr_b16 a[240:241], v63 offset:12912\n"
    "v_mul_f32 v90, s81, v88\n"
    "v_mfma_f32_16x16x32_bf16 v[212:215], a[202:205], a[178:181], v[212:215]\n"
    "ds_read_b64_tr_b16 a[242:243], v63 offset:15472\n"
    "v_add_f32 v90, s82, v90\n"
    "v_mfma_f32_16x16x32_bf16 v[208:211], a[202:205], v[152:155], v[208:211]\n"
    "ds_read_b64_tr_b16 a[244:245], v63 offset:18032\n"
    "v_accvgpr_read_b32 v44, a31\n"
    "v_mfma_f32_16x16x32_bf16 v[216:219], a[206:209], v[148:151], v[216:219]\n"
    "ds_read_b64_tr_b16 a[246:247], v65 offset:32\n"
    "buffer_load_dwordx4 v62, s[16:19], s64 offen offset:1024 lds\n"
    "v_mfma_f32_16x16x32_bf16 v[220:223], a[206:209], a[174:177], v[220:223]\n"
    "ds_read_b64_tr_b16 a[248:249], v65 offset:2592\n"
    "v_mul_f32 v87, v45, v84\n"
    "v_mfma_f32_16x16x32_bf16 v[220:223], a[210:213], a[178:181], v[220:223]\n"
    "ds_read_b64_tr_b16 a[250:251], v65 offset:5152\n"
    "s_waitcnt vmcnt(14) lgkmcnt(7)\n"
    "v_mfma_f32_16x16x32_bf16 v[216:219], a[210:213], v[152:155], v[216:219]\n"
    "s_barrier\n"
    "ds_read_b64_tr_b16 a[252:253], v65 offset:7712\n"
    "v_mfma_f32_16x16x32_bf16 v[224:227], a[214:217], v[148:151], v[224:227]\n"
    "ds_read_b64_tr_b16 a[190:191], v63 offset:24576\n"
    "v_mul_f32 v91, v45, v88\n"
    "v_mfma_f32_16x16x32_bf16 v[228:231], a[214:217], a[174:177], v[228:231]\n"
    "ds_read_b64_tr_b16 a[192:193], v63 offset:27136\n"
    "s_add_u32 s51, s51, s85\n"
    "v_mfma_f32_16x16x32_bf16 v[228:231], a[218:221], a[178:181], v[228:231]\n"
    "ds_read_b64_tr_b16 a[194:195], v63 offset:29696\n"
    "s_add_u32 m0, s87, 98304\n"
    "v_mfma_f32_16x16x32_bf16 v[224:227], a[218:221], v[152:155], v[224:227]\n"
    "ds_read_b64_tr_b16 a[196:197], v63 offset:32256\n"
    "buffer_load_dwordx4 v61, s[16:19], s65 offen offset:0 lds\n"
    "v_mfma_f32_16x16x32_bf16 v[232:235], a[222:225], v[148:151], v[232:235]\n"
    "ds_read_b64_tr_b16 a[198:199], v63 offset:24616\n"
    "s_add_u32 s58, s58, s85\n"
    "v_mfma_f32_16x16x32_bf16 v[236:239], a[222:225], a[174:177], v[236:239]\n"
    "ds_read_b64_tr_b16 a[200:201], v63 offset:27176\n"
    "v_accvgpr_read_b32 v16, a26\n"
    "v_mfma_f32_16x16x32_bf16 v[236:239], a[226:229], a[178:181], v[236:239]\n"
    "ds_read_b64_tr_b16 a[202:203], v63 offset:29736\n"
    "v_add_u32 v15, s43, v16\n"
    "v_mfma_f32_16x16x32_bf16 v[232:235], a[226:229], v[152:155], v[232:235]\n"
    "ds_read_b64_tr_b16 a[204:205], v63 offset:32296\n"
    "s_add_u32 s57, s57, s85\n"
    "v_mfma_f32_16x16x32_bf16 v[240:243], a[230:233], v[148:151], v[240:243]\n"
    "ds_read_b64_tr_b16 a[206:207], v63 offset:24648\n"
    "buffer_load_dwordx4 v62, s[16:19], s65 offen offset:1024 lds\n"
    "v_mfma_f32_16x16x32_bf16 v[244:247], a[230:233], a[174:177], v[244:247]\n"
    "ds_read_b64_tr_b16 a[208:209], v63 offset:27208\n"
    "s_add_u32 s56, s56, s85\n"
    "v_mfma_f32_16x16x32_bf16 v[244:247], a[234:237], a[178:181], v[244:247]\n"
    "ds_read_b64_tr_b16 a[210:211], v63 offset:29768\n"
    "s_cmp_ge_u32 s76, 1\n"
    "s_cselect_b32 s56, s56, 2146435072\n"
    "v_mfma_f32_16x16x32_bf16 v[240:243], a[234:237], v[152:155], v[240:243]\n"
    "ds_read_b64_tr_b16 a[212:213], v63 offset:32328\n"
    "s_waitcnt lgkmcnt(7)\n"
    "v_mfma_f32_16x16x32_bf16 v[248:251], a[238:241], v[148:151], v[248:251]\n"
    "ds_read_b64_tr_b16 a[214:215], v63 offset:24688\n"
    "s_add_u32 s55, s55, s85\n"
    "v_mfma_f32_16x16x32_bf16 v[252:255], a[238:241], a[174:177], v[252:255]\n"
    "ds_read_b64_tr_b16 a[216:217], v63 offset:27248\n"
    "s_cmp_ge_u32 s76, 2\n"
    "s_cselect_b32 s55, s55, 2146435072\n"
    "v_mfma_f32_16x16x32_bf16 v[252:255], a[242:245], a[178:181], v[252:255]\n"
    "ds_read_b64_tr_b16 a[218:219], v63 offset:29808\n"
    "s_add_u32 m0, s87, 106496\n"
    "v_mfma_f32_16x16x32_bf16 v[248:251], a[242:245], v[152:155], v[248:251]\n"
    "ds_read_b64_tr_b16 a[220:221], v63 offset:32368\n"
    "buffer_load_dwordx4 v61, s[16:19], s66 offen offset:0 lds\n"
    "v_mfma_f32_16x16x32_bf16 v[2:5], a[246:249], v[148:151], v[2:5]\n"
    "ds_read_b64_tr_b16 a[222:223], v63 offset:34816\n"
    "s_add_u32 s48, s48, s85\n"
    "v_mfma_f32_16x16x32_bf16 v[6:9], a[246:249], a[174:177], v[6:9]\n"
    "ds_read_b64_tr_b16 a[224:225], v63 offset:37376\n"
    "s_add_u32 s49, s49, s85\n"
    "v_mfma_f32_16x16x32_bf16 v[6:9], a[250:253], a[178:181], v[6:9]\n"
    "ds_read_b64_tr_b16 a[226:227], v63 offset:39936\n"
    "s_add_u32 s50, s50, s85\n"
    "v_mfma_f32_16x16x32_bf16 v[2:5], a[250:253], v[152:155], v[2:5]\n"
    "ds_read_b64_tr_b16 a[228:229], v63 offset:42496\n"
    "s_add_u32 s52, s52, s85\n"
    "v_mfma_f32_16x16x32_bf16 v[192:195], a[190:193], v[156:159], v[192:195]\n"
    "ds_read_b64_tr_b16 a[230:231], v63 offset:34856\n"
    "buffer_load_dwordx4 v62, s[16:19], s66 offen offset:1024 lds\n"
    "v_mfma_f32_16x16x32_bf16 v[196:199], a[190:193], a[182:185], v[196:199]\n"
    "ds_read_b64_tr_b16 a[232:233], v63 offset:37416\n"
    "s_cmp_eq_u32 s3, 0\n"
    "s_cbranch_scc1 L_ld2_%=\n"
    "L_ld2_ret_%=:\n"
    "v_mfma_f32_16x16x32_bf16 v[196:199], a[194:197], a[186:189], v[196:199]\n"
    "ds_read_b64_tr_b16 a[234:235], v63 offset:39976\n"
    "s_add_u32 s53, s53, s85\n"
    "v_mfma_f32_16x16x32_bf16 v[192:195], a[194:197], v[160:163], v[192:195]\n"
    "ds_read_b64_tr_b16 a[236:237], v63 offset:42536\n"
    "s_waitcnt lgkmcnt(7)\n"
    "v_mfma_f32_16x16x32_bf16 v[200:203], a[198:201], v[156:159], v[200:203]\n"
    "ds_read_b64_tr_b16 a[238:239], v63 offset:34888\n"
    "s_add_u32 s54, s54, s85\n"
    "v_mfma_f32_16x16x32_bf16 v[204:207], a[198:201], a[182:185], v[204:207]\n"
    "ds_read_b64_tr_b16 a[240:241], v63 offset:37448\n"
    "v_mul_f32 v196, v196, v89\n"
    "v_mfma_f32_16x16x32_bf16 v[204:207], a[202:205], a[186:189], v[204:207]\n"
    "ds_read_b64_tr_b16 a[242:243], v63 offset:40008\n"
    "s_add_u32 m0, s87, 114688\n"
    "v_mfma_f32_16x16x32_bf16 v[200:203], a[202:205], v[160:163], v[200:203]\n"
    "ds_read_b64_tr_b16 a[244:245], v63 offset:42568\n"
    "buffer_load_dwordx4 v61, s[16:19], s67 offen offset:0 lds\n"
    "v_mfma_f32_16x16x32_bf16 v[208:211], a[206:209], v[156:159], v[208:211]\n"
    "ds_read_b64_tr_b16 a[246:247], v63 offset:34928\n"
    "v_add_u32 v61, 128, v61\n"
    "v_mfma_f32_16x16x32_bf16 v[212:215], a[206:209], a[182:185], v[212:215]\n"
    "ds_read_b64_tr_b16 a[248:249], v63 offset:37488\n"
    "v_mul_f32 v192, v192, v85\n"
    "v_mfma_f32_16x16x32_bf16 v[212:215], a[210:213], a[186:189], v[212:215]\n"
    "ds_read_b64_tr_b16 a[250:251], v63 offset:40048\n"
    "v_mul_f32 v193, v193, v85\n"
    "v_mfma_f32_16x16x32_bf16 v[208:211], a[210:213], v[160:163], v[208:211]\n"
    "ds_read_b64_tr_b16 a[252:253], v63 offset:42608\n"
    "v_mul_f32 v194, v194, v85\n"
    "v_mfma_f32_16x16x32_bf16 v[216:219], a[214:217], v[156:159], v[216:219]\n"
    "ds_read_b64_tr_b16 a[190:191], v65 offset:24608\n"
    "buffer_load_dwordx4 v62, s[16:19], s67 offen offset:1024 lds\n"
    "v_mfma_f32_16x16x32_bf16 v[220:223], a[214:217], a[182:185], v[220:223]\n"
    "ds_read_b64_tr_b16 a[192:193], v65 offset:27168\n"
    "v_add_u32 v62, 128, v62\n"
    "v_mfma_f32_16x16x32_bf16 v[220:223], a[218:221], a[186:189], v[220:223]\n"
    "ds_read_b64_tr_b16 a[194:195], v65 offset:29728\n"
    "s_waitcnt vmcnt(13) lgkmcnt(7)\n"
    "v_mfma_f32_16x16x32_bf16 v[216:219], a[218:221], v[160:163], v[216:219]\n"
    "s_barrier\n"
    "ds_read_b64_tr_b16 a[196:197], v65 offset:32288\n"
    "v_mfma_f32_16x16x32_bf16 v[224:227], a[222:225], v[156:159], v[224:227]\n"
    "ds_read_b64_tr_b16 a[198:199], v67 offset:49152\n"
    "v_mul_f32 v195, v195, v85\n"
    "v_mfma_f32_16x16x32_bf16 v[228:231], a[222:225], a[182:185], v[228:231]\n"
    "ds_read_b64_tr_b16 a[200:201], v67 offset:51200\n"
    "v_cvt_pk_bf16_f32 v192, v192, v193\n"
    "v_mfma_f32_16x16x32_bf16 v[228:231], a[226:229], a[186:189], v[228:231]\n"
    "ds_read_b64_tr_b16 a[202:203], v67 offset:53248\n"
    "v_cvt_pk_bf16_f32 v193, v194, v195\n"
    "v_mfma_f32_16x16x32_bf16 v[224:227], a[226:229], v[160:163], v[224:227]\n"
    "ds_read_b64_tr_b16 a[204:205], v67 offset:55296\n"
    "v_mul_f32 v200, v200, v85\n"
    "v_mfma_f32_16x16x32_bf16 v[232:235], a[230:233], v[156:159], v[232:235]\n"
    "ds_read_b64_tr_b16 a[206:207], v67 offset:57344\n"
    "v_mul_f32 v201, v201, v85\n"
    "v_mfma_f32_16x16x32_bf16 v[236:239], a[230:233], a[182:185], v[236:239]\n"
    "ds_read_b64_tr_b16 a[208:209], v67 offset:59392\n"
    "v_mul_f32 v202, v202, v85\n"
    "v_mfma_f32_16x16x32_bf16 v[236:239], a[234:237], a[186:189], v[236:239]\n"
    "ds_read_b64_tr_b16 a[210:211], v67 offset:61440\n"
    "v_mul_f32 v203, v203, v85\n"
    "v_mfma_f32_16x16x32_bf16 v[232:235], a[234:237], v[160:163], v[232:235]\n"
    "ds_read_b64_tr_b16 a[212:213], v67 offset:63488\n"
    "v_cvt_pk_bf16_f32 v194, v200, v201\n"
    "v_mfma_f32_16x16x32_bf16 v[240:243], a[238:241], v[156:159], v[240:243]\n"
    "ds_read_b64_tr_b16 a[214:215], v68 offset:0\n"
    "v_cvt_pk_bf16_f32 v195, v202, v203\n"
    "v_mfma_f32_16x16x32_bf16 v[244:247], a[238:241], a[182:185], v[244:247]\n"
    "ds_read_b64_tr_b16 a[216:217], v68 offset:2048\n"
    "v_mul_f32 v197, v197, v89\n"
    "v_mfma_f32_16x16x32_bf16 v[244:247], a[242:245], a[186:189], v[244:247]\n"
    "ds_read_b64_tr_b16 a[218:219], v68 offset:4096\n"
    "v_mul_f32 v198, v198, v89\n"
    "v_mfma_f32_16x16x32_bf16 v[240:243], a[242:245], v[160:163], v[240:243]\n"
    "ds_read_b64_tr_b16 a[220:221], v68 offset:6144\n"
    "s_waitcnt lgkmcnt(7)\n"
    "v_mfma_f32_16x16x32_bf16 v[248:251], a[246:249], v[156:159], v[248:251]\n"
    "ds_read_b64_tr_b16 a[222:223], v69 offset:49152\n"
    "s_add_u32 m0, s87, 0\n"
    "v_mfma_f32_16x16x32_bf16 v[252:255], a[246:249], a[182:185], v[252:255]\n"
    "ds_read_b64_tr_b16 a[224:225], v69 offset:51200\n"
    "buffer_load_dwordx4 v61, s[16:19], 0 offen offset:0 lds\n"
    "v_mfma_f32_16x16x32_bf16 v[252:255], a[250:253], a[186:189], v[252:255]\n"
    "ds_read_b64_tr_b16 a[226:227], v69 offset:53248\n"
    "v_mul_f32 v199, v199, v89\n"
    "v_mfma_f32_16x16x32_bf16 v[248:251], a[250:253], v[160:163], v[248:251]\n"
    "ds_read_b64_tr_b16 a[228:229], v69 offset:55296\n"
    "v_cvt_pk_bf16_f32 v196, v196, v197\n"
    "v_mfma_f32_16x16x32_bf16 v[2:5], a[190:193], v[156:159], v[2:5]\n"
    "ds_read_b64_tr_b16 a[230:231], v69 offset:57344\n"
    "v_cvt_pk_bf16_f32 v197, v198, v199\n"
    "v_mfma_f32_16x16x32_bf16 v[6:9], a[190:193], a[182:185], v[6:9]\n"
    "ds_read_b64_tr_b16 a[232:233], v69 offset:59392\n"
    "v_mul_f32 v204, v204, v89\n"
    "v_mfma_f32_16x16x32_bf16 v[6:9], a[194:197], a[186:189], v[6:9]\n"
    "ds_read_b64_tr_b16 a[234:235], v69 offset:61440\n"
    "v_mul_f32 v205, v205, v89\n"
    "v_mfma_f32_16x16x32_bf16 v[2:5], a[194:197], v[160:163], v[2:5]\n"
    "ds_read_b64_tr_b16 a[236:237], v69 offset:63488\n"
    "v_mul_f32 v206, v206, v89\n"
    "v_mfma_f32_16x16x32_bf16 v[188:191], a[198:201], v[92:95], 0\n"
    "buffer_store_dwordx4 v[192:195], v18, s[28:31], s94 offen offset:0 sc1\n"
    "ds_read_b64_tr_b16 a[238:239], v70 offset:0\n"
    "v_mfma_f32_16x16x32_bf16 v[188:191], a[202:205], v[96:99], v[188:191]\n"
    "ds_read_b64_tr_b16 a[240:241], v70 offset:2048\n"
    "s_waitcnt lgkmcnt(6)\n"
    "v_mfma_f32_16x16x32_bf16 v[188:191], a[206:209], v[100:103], v[188:191]\n"
    "ds_read_b64_tr_b16 a[242:243], v70 offset:4096\n"
    "v_mul_f32 v207, v207, v89\n"
    "v_mfma_f32_16x16x32_bf16 v[188:191], a[210:213], v[104:107], v[188:191]\n"
    "ds_read_b64_tr_b16 a[244:245], v70 offset:6144\n"
    "v_cvt_pk_bf16_f32 v198, v204, v205\n"
    "v_mfma_f32_16x16x32_bf16 v[188:191], a[214:217], v[108:111], v[188:191]\n"
    "ds_read_b64_tr_b16 a[246:247], v71 offset:49152\n"
    "buffer_load_dwordx4 v62, s[16:19], 0 offen offset:1024 lds\n"
    "v_mfma_f32_16x16x32_bf16 v[188:191], a[218:221], v[112:115], v[188:191]\n"
    "ds_read_b64_tr_b16 a[248:249], v71 offset:51200\n"
    "v_cvt_pk_bf16_f32 v199, v206, v207\n"
    "v_mfma_f32_16x16x32_bf16 v[164:167], a[198:201], a[118:121], 0\n"
    "ds_read_b64_tr_b16 a[250:251], v71 offset:53248\n"
    "v_mul_f32 v208, v208, v85\n"
    "v_mfma_f32_16x16x32_bf16 v[164:167], a[202:205], a[122:125], v[164:167]\n"
    "ds_read_b64_tr_b16 a[252:253], v71 offset:55296\n"
    "v_mul_f32 v209, v209, v85\n"
    "v_mfma_f32_16x16x32_bf16 v[164:167], a[206:209], a[126:129], v[164:167]\n"
    "ds_read_b64_tr_b16 a[190:191], v71 offset:57344\n"
    "s_add_u32 m0, s87, 8192\n"
    "v_mfma_f32_16x16x32_bf16 v[164:167], a[210:213], a[130:133], v[164:167]\n"
    "ds_read_b64_tr_b16 a[192:193], v71 offset:59392\n"
    "buffer_load_dwordx4 v61, s[16:19], s60 offen offset:0 lds\n"
    "v_mfma_f32_16x16x32_bf16 v[164:167], a[214:217], a[134:137], v[164:167]\n"
    "ds_read_b64_tr_b16 a[194:195], v71 offset:61440\n"
    "v_mul_f32 v210, v210, v85\n"
    "v_mfma_f32_16x16x32_bf16 v[164:167], a[218:221], a[138:141], v[164:167]\n"
    "ds_read_b64_tr_b16 a[196:197], v71 offset:63488\n"
    "v_mul_f32 v211, v211, v85\n"
    "v_mfma_f32_16x16x32_bf16 v[168:171], a[222:225], v[92:95], 0\n"
    "ds_read_b64_tr_b16 a[198:199], v72 offset:0\n"
    "v_cvt_pk_bf16_f32 v208, v208, v209\n"
    "v_mfma_f32_16x16x32_bf16 v[168:171], a[226:229], v[96:99], v[168:171]\n"
    "ds_read_b64_tr_b16 a[200:201], v72 offset:2048\n"
    "s_waitcnt lgkmcnt(7)\n"
    "v_mfma_f32_16x16x32_bf16 v[168:171], a[230:233], v[100:103], v[168:171]\n"
    "buffer_store_dwordx4 v[196:199], v18, s[28:31], s94 offen offset:1280 sc1\n"
    "ds_read_b64_tr_b16 a[202:203], v72 offset:4096\n"
    "v_mfma_f32_16x16x32_bf16 v[168:171], a[234:237], v[104:107], v[168:171]\n"
    "ds_read_b64_tr_b16 a[204:205], v72 offset:6144\n"
    "v_cvt_pk_bf16_f32 v209, v210, v211\n"
    "v_mfma_f32_16x16x32_bf16 v[168:171], a[238:241], v[108:111], v[168:171]\n"
    "ds_read_b64_tr_b16 a[206:207], v73 offset:49152\n"
    "v_mul_f32 v216, v216, v85\n"
    "v_mfma_f32_16x16x32_bf16 v[168:171], a[242:245], v[112:115], v[168:171]\n"
    "ds_read_b64_tr_b16 a[208:209], v73 offset:51200\n"
    "v_mul_f32 v217, v217, v85\n"
    "v_mfma_f32_16x16x32_bf16 v[172:175], a[222:225], a[118:121], 0\n"
    "ds_read_b64_tr_b16 a[210:211], v73 offset:53248\n"
    "buffer_load_dwordx4 v62, s[16:19], s60 offen offset:1024 lds\n"
    "v_mfma_f32_16x16x32_bf16 v[172:175], a[226:229], a[122:125], v[172:175]\n"
    "ds_read_b64_tr_b16 a[212:213], v73 offset:55296\n"
    "v_mul_f32 v218, v218, v85\n"
    "v_mfma_f32_16x16x32_bf16 v[172:175], a[230:233], a[126:129], v[172:175]\n"
    "ds_read_b64_tr_b16 a[214:215], v73 offset:57344\n"
    "v_mul_f32 v219, v219, v85\n"
    "v_mfma_f32_16x16x32_bf16 v[172:175], a[234:237], a[130:133], v[172:175]\n"
    "ds_read_b64_tr_b16 a[216:217], v73 offset:59392\n"
    "v_cvt_pk_bf16_f32 v210, v216, v217\n"
    "v_mfma_f32_16x16x32_bf16 v[172:175], a[238:241], a[134:137], v[172:175]\n"
    "ds_read_b64_tr_b16 a[218:219], v73 offset:61440\n"
    "v_cvt_pk_bf16_f32 v211, v218, v219\n"
    "v_mfma_f32_16x16x32_bf16 v[172:175], a[242:245], a[138:141], v[172:175]\n"
    "ds_read_b64_tr_b16 a[220:221], v73 offset:63488\n"
    "v_mul_f32 v212, v212, v89\n"
    "v_mfma_f32_16x16x32_bf16 v[176:179], a[246:249], v[92:95], 0\n"
    "ds_read_b64_tr_b16 a[222:223], v74 offset:0\n"
    "s_waitcnt lgkmcnt(7)\n"
    "v_mfma_f32_16x16x32_bf16 v[176:179], a[250:253], v[96:99], v[176:179]\n"
    "ds_read_b64_tr_b16 a[224:225], v74 offset:2048\n"
    "s_cmp_eq_u32 s3, 0\n"
    "s_cbranch_scc1 L_ld3_%=\n"
    "L_ld3_ret_%=:\n"
    "v_mfma_f32_16x16x32_bf16 v[176:179], a[190:193], v[100:103], v[176:179]\n"
    "ds_read_b64_tr_b16 a[226:227], v74 offset:4096\n"
    "s_waitcnt vmcnt(12) lgkmcnt(7)\n"
    "v_mfma_f32_16x16x32_bf16 v[176:179], a[194:197], v[104:107], v[176:179]\n"
    "s_barrier\n"
    "ds_read_b64_tr_b16 a[228:229], v74 offset:6144\n"
    "v_mfma_f32_16x16x32_bf16 v[176:179], a[198:201], v[108:111], v[176:179]\n"
    "buffer_store_dwordx4 v[208:211], v54, s[28:31], s94 offen offset:0 sc1\n"
    "ds_read_b64_tr_b16 a[230:231], v68 offset:8192\n"
    "v_mfma_f32_16x16x32_bf16 v[176:179], a[202:205], v[112:115], v[176:179]\n"
    "ds_read_b64_tr_b16 a[232:233], v68 offset:10240\n"
    "s_add_u32 m0, s87, 16384\n"
    "v_mfma_f32_16x16x32_bf16 v[180:183], a[246:249], a[118:121], 0\n"
    "ds_read_b64_tr_b16 a[234:235], v68 offset:12288\n"
    "v_mul_f32 v213, v213, v89\n"
    "v_mfma_f32_16x16x32_bf16 v[180:183], a[250:253], a[122:125], v[180:183]\n"
    "ds_read_b64_tr_b16 a[236:237], v68 offset:14336\n"
    "v_mul_f32 v214, v214, v89\n"
    "v_mfma_f32_16x16x32_bf16 v[180:183], a[190:193], a[126:129], v[180:183]\n"
    "ds_read_b64_tr_b16 a[238:239], v68 offset:16384\n"
    "buffer_load_dwordx4 v61, s[16:19], s61 offen offset:0 lds\n"
    "v_mfma_f32_16x16x32_bf16 v[180:183], a[194:197], a[130:133], v[180:183]\n"
    "ds_read_b64_tr_b16 a[240:241], v68 offset:18432\n"
    "v_mul_f32 v215, v215, v89\n"
    "v_mfma_f32_16x16x32_bf16 v[180:183], a[198:201], a[134:137], v[180:183]\n"
    "ds_read_b64_tr_b16 a[242:243], v68 offset:20480\n"
    "v_cvt_pk_bf16_f32 v212, v212, v213\n"
    "v_mfma_f32_16x16x32_bf16 v[180:183], a[202:205], a[138:141], v[180:183]\n"
    "ds_read_b64_tr_b16 a[244:245], v68 offset:22528\n"
    "buffer_load_dwordx4 v62, s[16:19], s61 offen offset:1024 lds\n"
    "v_mfma_f32_16x16x32_bf16 v[184:187], a[206:209], v[92:95], 0\n"
    "ds_read_b64_tr_b16 a[246:247], v68 offset:24576\n"
    "v_cvt_pk_bf16_f32 v213, v214, v215\n"
    "v_mfma_f32_16x16x32_bf16 v[184:187], a[210:213], v[96:99], v[184:187]\n"
    "ds_read_b64_tr_b16 a[248:249], v68 offset:26624\n"
    "s_waitcnt lgkmcnt(7)\n"
    "v_mfma_f32_16x16x32_bf16 v[184:187], a[214:217], v[100:103], v[184:187]\n"
    "ds_read_b64_tr_b16 a[250:251], v68 offset:28672\n"
    "v_mul_f32 v220, v220, v89\n"
    "v_mfma_f32_16x16x32_bf16 v[184:187], a[218:221], v[104:107], v[184:187]\n"
    "ds_read_b64_tr_b16 a[252:253], v68 offset:30720\n"
    "v_mul_f32 v221, v221, v89\n"
    "v_mfma_f32_16x16x32_bf16 v[184:187], a[222:225], v[108:111], v[184:187]\n"
    "ds_read_b64_tr_b16 a[190:191], v70 offset:8192\n"
    "s_add_u32 m0, s87, 24576\n"
    "v_mfma_f32_16x16x32_bf16 v[184:187], a[226:229], v[112:115], v[184:187]\n"
    "ds_read_b64_tr_b16 a[192:193], v70 offset:10240\n"
    "buffer_load_dwordx4 v61, s[16:19], s62 offen offset:0 lds\n"
    "v_mfma_f32_16x16x32_bf16 v[30:33], a[206:209], a[118:121], 0\n"
    "ds_read_b64_tr_b16 a[194:195], v70 offset:12288\n"
    "v_mul_f32 v222, v222, v89\n"
    "v_mfma_f32_16x16x32_bf16 v[30:33], a[210:213], a[122:125], v[30:33]\n"
    "ds_read_b64_tr_b16 a[196:197], v70 offset:14336\n"
    "v_mul_f32 v223, v223, v89\n"
    "v_mfma_f32_16x16x32_bf16 v[30:33], a[214:217], a[126:129], v[30:33]\n"
    "ds_read_b64_tr_b16 a[198:199], v70 offset:16384\n"
    "v_cvt_pk_bf16_f32 v214, v220, v221\n"
    "v_mfma_f32_16x16x32_bf16 v[30:33], a[218:221], a[130:133], v[30:33]\n"
    "ds_read_b64_tr_b16 a[200:201], v70 offset:18432\n"
    "v_cvt_pk_bf16_f32 v215, v222, v223\n"
    "v_mfma_f32_16x16x32_bf16 v[30:33], a[222:225], a[134:137], v[30:33]\n"
    "buffer_store_dwordx4 v[212:215], v54, s[28:31], s94 offen offset:1280 sc1\n"
    "ds_read_b64_tr_b16 a[202:203], v70 offset:20480\n"
    "v_mfma_f32_16x16x32_bf16 v[30:33], a[226:229], a[138:141], v[30:33]\n"
    "ds_read_b64_tr_b16 a[204:205], v70 offset:22528\n"
    "s_cmp_eq_u32 s3, 0\n"
    "s_cbranch_scc1 L_ld4_%=\n"
    "L_ld4_ret_%=:\n"
    "v_mfma_f32_16x16x32_bf16 v[188:191], a[230:233], v[116:119], v[188:191]\n"
    "ds_read_b64_tr_b16 a[206:207], v70 offset:24576\n"
    "s_waitcnt lgkmcnt(6)\n"
    "v_mfma_f32_16x16x32_bf16 v[188:191], a[234:237], v[120:123], v[188:191]\n"
    "ds_read_b64_tr_b16 a[208:209], v70 offset:26624\n"
    "v_mul_f32 v224, v224, v86\n"
    "v_mfma_f32_16x16x32_bf16 v[188:191], a[238:241], v[124:127], v[188:191]\n"
    "ds_read_b64_tr_b16 a[210:211], v70 offset:28672\n"
    "buffer_load_dwordx4 v62, s[16:19], s62 offen offset:1024 lds\n"
    "v_mfma_f32_16x16x32_bf16 v[188:191], a[242:245], v[128:131], v[188:191]\n"
    "ds_read_b64_tr_b16 a[212:213], v70 offset:30720\n"
    "v_mul_f32 v225, v225, v86\n"
    "v_mfma_f32_16x16x32_bf16 v[188:191], a[246:249], v[132:135], v[188:191]\n"
    "ds_read_b64_tr_b16 a[214:215], v72 offset:8192\n"
    "v_mul_f32 v226, v226, v86\n"
    "v_mfma_f32_16x16x32_bf16 v[188:191], a[250:253], v[136:139], v[188:191]\n"
    "ds_read_b64_tr_b16 a[216:217], v72 offset:10240\n"
    "v_mul_f32 v227, v227, v86\n"
    "v_mfma_f32_16x16x32_bf16 v[164:167], a[230:233], a[142:145], v[164:167]\n"
    "ds_read_b64_tr_b16 a[218:219], v72 offset:12288\n"
    "v_cvt_pk_bf16_f32 v224, v224, v225\n"
    "v_mfma_f32_16x16x32_bf16 v[164:167], a[234:237], a[146:149], v[164:167]\n"
    "ds_read_b64_tr_b16 a[220:221], v72 offset:14336\n"
    "v_cvt_pk_bf16_f32 v225, v226, v227\n"
    "v_mfma_f32_16x16x32_bf16 v[164:167], a[238:241], a[150:153], v[164:167]\n"
    "ds_read_b64_tr_b16 a[222:223], v72 offset:16384\n"
    "s_add_u32 m0, s87, 32768\n"
    "v_mfma_f32_16x16x32_bf16 v[164:167], a[242:245], a[154:157], v[164:167]\n"
    "ds_read_b64_tr_b16 a[224:225], v72 offset:18432\n"
    "buffer_load_dwordx4 v61, s[16:19], s63 offen offset:0 lds\n"
    "v_mfma_f32_16x16x32_bf16 v[164:167], a[246:249], a[158:161], v[164:167]\n"
    "ds_read_b64_tr_b16 a[226:227], v72 offset:20480\n"
    "v_mul_f32 v232, v232, v86\n"
    "v_mfma_f32_16x16x32_bf16 v[164:167], a[250:253], a[162:165], v[164:167]\n"
    "ds_read_b64_tr_b16 a[228:229], v72 offset:22528\n"
    "v_mul_f32 v233, v233, v86\n"
    "v_mfma_f32_16x16x32_bf16 v[168:171], a[190:193], v[116:119], v[168:171]\n"
    "ds_read_b64_tr_b16 a[230:231], v72 offset:24576\n"
    "s_waitcnt lgkmcnt(7)\n"
    "v_mfma_f32_16x16x32_bf16 v[168:171], a[194:197], v[120:123], v[168:171]\n"
    "ds_read_b64_tr_b16 a[232:233], v72 offset:26624\n"
    "v_mul_f32 v234, v234, v86\n"
    "v_mfma_f32_16x16x32_bf16 v[168:171], a[198:201], v[124:127], v[168:171]\n"
    "ds_read_b64_tr_b16 a[234:235], v72 offset:28672\n"
    "v_mul_f32 v235, v235, v86\n"
    "v_mfma_f32_16x16x32_bf16 v[168:171], a[202:205], v[128:131], v[168:171]\n"
    "ds_read_b64_tr_b16 a[236:237], v72 offset:30720\n"
    "v_cvt_pk_bf16_f32 v226, v232, v233\n"
    "v_mfma_f32_16x16x32_bf16 v[168:171], a[206:209], v[132:135], v[168:171]\n"
    "ds_read_b64_tr_b16 a[238:239], v74 offset:8192\n"
    "buffer_load_dwordx4 v62, s[16:19], s63 offen offset:1024 lds\n"
    "v_cvt_pk_bf16_f32 v227, v234, v235\n"
    "v_pk_max_i16 v224, v224, s79\n"
    "v_pk_max_i16 v225, v225, s79\n"
    "v_pk_max_i16 v226, v226, s79\n"
    "v_pk_max_i16 v227, v227, s79\n"
    "v_mfma_f32_16x16x32_bf16 v[168:171], a[210:213], v[136:139], v[168:171]\n"
    "ds_read_b64_tr_b16 a[240:241], v74 offset:10240\n"
    "v_mul_f32 v228, v228, v90\n"
    "v_mfma_f32_16x16x32_bf16 v[172:175], a[190:193], a[142:145], v[172:175]\n"
    "buffer_store_dwordx4 v[224:227], v18, s[28:31], s95 offen offset:0 sc1\n"
    "ds_read_b64_tr_b16 a[242:243], v74 offset:12288\n"
    "v_mfma_f32_16x16x32_bf16 v[172:175], a[194:197], a[146:149], v[172:175]\n"
    "ds_read_b64_tr_b16 a[244:245], v74 offset:14336\n"
    "v_mul_f32 v229, v229, v90\n"
    "v_mfma_f32_16x16x32_bf16 v[172:175], a[198:201], a[150:153], v[172:175]\n"
    "ds_read_b64_tr_b16 a[246:247], v74 offset:16384\n"
    "v_mul_f32 v230, v230, v90\n"
    "v_mfma_f32_16x16x32_bf16 v[172:175], a[202:205], a[154:157], v[172:175]\n"
    "ds_read_b64_tr_b16 a[248:249], v74 offset:18432\n"
    "v_mul_f32 v231, v231, v90\n"
    "v_mfma_f32_16x16x32_bf16 v[172:175], a[206:209], a[158:161], v[172:175]\n"
    "ds_read_b64_tr_b16 a[250:251], v74 offset:20480\n"
    "v_cvt_pk_bf16_f32 v228, v228, v229\n"
    "v_mfma_f32_16x16x32_bf16 v[172:175], a[210:213], a[162:165], v[172:175]\n"
    "ds_read_b64_tr_b16 a[252:253], v74 offset:22528\n"
    "v_cvt_pk_bf16_f32 v229, v230, v231\n"
    "v_mfma_f32_16x16x32_bf16 v[176:179], a[214:217], v[116:119], v[176:179]\n"
    "ds_read_b64_tr_b16 a[190:191], v74 offset:24576\n"
    "s_waitcnt lgkmcnt(7)\n"
    "v_mfma_f32_16x16x32_bf16 v[176:179], a[218:221], v[120:123], v[176:179]\n"
    "ds_read_b64_tr_b16 a[192:193], v74 offset:26624\n"
    "v_mul_f32 v236, v236, v90\n"
    "v_mfma_f32_16x16x32_bf16 v[176:179], a[222:225], v[124:127], v[176:179]\n"
    "ds_read_b64_tr_b16 a[194:195], v74 offset:28672\n"
    "s_waitcnt vmcnt(15) lgkmcnt(7)\n"
    "v_mfma_f32_16x16x32_bf16 v[176:179], a[226:229], v[128:131], v[176:179]\n"
    "s_barrier\n"
    "ds_read_b64_tr_b16 a[196:197], v74 offset:30720\n"
    "v_mfma_f32_16x16x32_bf16 v[176:179], a[230:233], v[132:135], v[176:179]\n"
    "ds_read_b64_tr_b16 a[198:199], v68 offset:32768\n"
    "v_mul_f32 v237, v237, v90\n"
    "v_mfma_f32_16x16x32_bf16 v[176:179], a[234:237], v[136:139], v[176:179]\n"
    "ds_read_b64_tr_b16 a[200:201], v68 offset:34816\n"
    "v_mul_f32 v238, v238, v90\n"
    "v_mfma_f32_16x16x32_bf16 v[180:183], a[214:217], a[142:145], v[180:183]\n"
    "ds_read_b64_tr_b16 a[202:203], v68 offset:36864\n"
    "v_mul_f32 v239, v239, v90\n"
    "v_cvt_pk_bf16_f32 v230, v236, v237\n"
    "v_cvt_pk_bf16_f32 v231, v238, v239\n"
    "v_pk_max_i16 v228, v228, s79\n"
    "v_pk_max_i16 v229, v229, s79\n"
    "v_pk_max_i16 v230, v230, s79\n"
    "v_pk_max_i16 v231, v231, s79\n"
    "s_add_u32 m0, s87, 40960\n"
    "v_mfma_f32_16x16x32_bf16 v[180:183], a[218:221], a[146:149], v[180:183]\n"
    "buffer_store_dwordx4 v[228:231], v18, s[28:31], s95 offen offset:1280 sc1\n"
    "ds_read_b64_tr_b16 a[204:205], v68 offset:38912\n"
    "v_mfma_f32_16x16x32_bf16 v[180:183], a[222:225], a[150:153], v[180:183]\n"
    "ds_read_b64_tr_b16 a[206:207], v68 offset:40960\n"
    "v_mul_f32 v240, v240, v86\n"
    "v_mfma_f32_16x16x32_bf16 v[180:183], a[226:229], a[154:157], v[180:183]\n"
    "ds_read_b64_tr_b16 a[208:209], v68 offset:43008\n"
    "v_mul_f32 v241, v241, v86\n"
    "v_mfma_f32_16x16x32_bf16 v[180:183], a[230:233], a[158:161], v[180:183]\n"
    "ds_read_b64_tr_b16 a[210:211], v68 offset:45056\n"
    "v_mul_f32 v242, v242, v86\n"
    "v_mfma_f32_16x16x32_bf16 v[180:183], a[234:237], a[162:165], v[180:183]\n"
    "ds_read_b64_tr_b16 a[212:213], v68 offset:47104\n"
    "buffer_load_dwordx4 v61, s[16:19], s64 offen offset:0 lds\n"
    "v_mfma_f32_16x16x32_bf16 v[184:187], a[238:241], v[116:119], v[184:187]\n"
    "ds_read_b64_tr_b16 a[214:215], v68 offset:49152\n"
    "v_mul_f32 v243, v243, v86\n"
    "v_mfma_f32_16x16x32_bf16 v[184:187], a[242:245], v[120:123], v[184:187]\n"
    "ds_read_b64_tr_b16 a[216:217], v68 offset:51200\n"
    "s_waitcnt lgkmcnt(7)\n"
    "v_mfma_f32_16x16x32_bf16 v[184:187], a[246:249], v[124:127], v[184:187]\n"
    "ds_read_b64_tr_b16 a[218:219], v68 offset:53248\n"
    "buffer_load_dwordx4 v62, s[16:19], s64 offen offset:1024 lds\n"
    "v_mfma_f32_16x16x32_bf16 v[184:187], a[250:253], v[128:131], v[184:187]\n"
    "ds_read_b64_tr_b16 a[220:221], v68 offset:55296\n"
    "s_add_u32 m0, s80, 122880\n"
    "v_mfma_f32_16x16x32_bf16 v[184:187], a[190:193], v[132:135], v[184:187]\n"
    "ds_read_b64_tr_b16 a[222:223], v70 offset:32768\n"
    "v_cvt_pk_bf16_f32 v240, v240, v241\n"
    "v_mfma_f32_16x16x32_bf16 v[184:187], a[194:197], v[136:139], v[184:187]\n"
    "ds_read_b64_tr_b16 a[224:225], v70 offset:34816\n"
    "buffer_load_dwordx4 v28, s[12:15], s47 offen offset:0 lds\n"
    "v_mfma_f32_16x16x32_bf16 v[30:33], a[238:241], a[142:145], v[30:33]\n"
    "ds_read_b64_tr_b16 a[226:227], v70 offset:36864\n"
    "v_cvt_pk_bf16_f32 v241, v242, v243\n"
    "v_mfma_f32_16x16x32_bf16 v[30:33], a[242:245], a[146:149], v[30:33]\n"
    "ds_read_b64_tr_b16 a[228:229], v70 offset:38912\n"
    "v_mul_f32 v248, v248, v86\n"
    "v_mfma_f32_16x16x32_bf16 v[30:33], a[246:249], a[150:153], v[30:33]\n"
    "ds_read_b64_tr_b16 a[230:231], v70 offset:40960\n"
    "buffer_load_dwordx4 v28, s[12:15], s47 offen offset:1024 lds\n"
    "v_mul_f32 v249, v249, v86\n"
    "v_mul_f32 v250, v250, v86\n"
    "v_mul_f32 v251, v251, v86\n"
    "v_cvt_pk_bf16_f32 v242, v248, v249\n"
    "v_cvt_pk_bf16_f32 v243, v250, v251\n"
    "v_pk_max_i16 v240, v240, s79\n"
    "v_pk_max_i16 v241, v241, s79\n"
    "v_pk_max_i16 v242, v242, s79\n"
    "v_pk_max_i16 v243, v243, s79\n"
    "v_mfma_f32_16x16x32_bf16 v[30:33], a[250:253], a[154:157], v[30:33]\n"
    "buffer_store_dwordx4 v[240:243], v54, s[28:31], s95 offen offset:0 sc1\n"
    "ds_read_b64_tr_b16 a[232:233], v70 offset:43008\n"
    "v_mfma_f32_16x16x32_bf16 v[30:33], a[190:193], a[158:161], v[30:33]\n"
    "ds_read_b64_tr_b16 a[234:235], v70 offset:45056\n"
    "v_mul_f32 v244, v244, v90\n"
    "v_mfma_f32_16x16x32_bf16 v[30:33], a[194:197], a[162:165], v[30:33]\n"
    "ds_read_b64_tr_b16 a[236:237], v70 offset:47104\n"
    "v_mul_f32 v245, v245, v90\n"
    "v_mfma_f32_16x16x32_bf16 v[188:191], a[198:201], v[140:143], v[188:191]\n"
    "ds_read_b64_tr_b16 a[238:239], v70 offset:49152\n"
    "s_waitcnt lgkmcnt(4)\n"
    "v_mfma_f32_16x16x32_bf16 v[188:191], a[202:205], v[144:147], v[188:191]\n"
    "ds_read_b64_tr_b16 a[240:241], v70 offset:51200\n"
    "buffer_load_dwordx4 v28, s[12:15], s47 offen offset:2048 lds\n"
    "s_add_u32 m0, s87, 49152\n"
    "v_mfma_f32_16x16x32_bf16 v[188:191], a[206:209], v[148:151], v[188:191]\n"
    "ds_read_b64_tr_b16 a[242:243], v70 offset:53248\n"
    "v_mul_f32 v246, v246, v90\n"
    "v_mfma_f32_16x16x32_bf16 v[188:191], a[210:213], v[152:155], v[188:191]\n"
    "ds_read_b64_tr_b16 a[244:245], v70 offset:55296\n"
    "v_mul_f32 v247, v247, v90\n"
    "v_mfma_f32_16x16x32_bf16 v[188:191], a[214:217], v[156:159], v[188:191]\n"
    "ds_read_b64_tr_b16 a[246:247], v72 offset:32768\n"
    "buffer_load_dwordx4 v61, s[16:19], s65 offen offset:0 lds\n"
    "v_mfma_f32_16x16x32_bf16 v[188:191], a[218:221], v[160:163], v[188:191]\n"
    "ds_read_b64_tr_b16 a[248:249], v72 offset:34816\n"
    "v_cvt_pk_bf16_f32 v244, v244, v245\n"
    "v_mfma_f32_16x16x32_bf16 v[164:167], a[198:201], a[166:169], v[164:167]\n"
    "ds_read_b64_tr_b16 a[250:251], v72 offset:36864\n"
    "v_cvt_pk_bf16_f32 v245, v246, v247\n"
    "v_mfma_f32_16x16x32_bf16 v[164:167], a[202:205], a[170:173], v[164:167]\n"
    "ds_read_b64_tr_b16 a[252:253], v72 offset:38912\n"
    "buffer_load_dwordx4 v62, s[16:19], s65 offen offset:1024 lds\n"
    "v_mfma_f32_16x16x32_bf16 v[164:167], a[206:209], a[174:177], v[164:167]\n"
    "ds_read_b64_tr_b16 a[190:191], v72 offset:40960\n"
    "v_mul_f32 v252, v252, v90\n"
    "v_mfma_f32_16x16x32_bf16 v[164:167], a[210:213], a[178:181], v[164:167]\n"
    "ds_read_b64_tr_b16 a[192:193], v72 offset:43008\n"
    "v_mul_f32 v253, v253, v90\n"
    "v_mfma_f32_16x16x32_bf16 v[164:167], a[214:217], a[182:185], v[164:167]\n"
    "ds_read_b64_tr_b16 a[194:195], v72 offset:45056\n"
    "v_mul_f32 v254, v254, v90\n"
    "v_mul_f32 v255, v255, v90\n"
    "v_cvt_pk_bf16_f32 v246, v252, v253\n"
    "v_cvt_pk_bf16_f32 v247, v254, v255\n"
    "v_pk_max_i16 v244, v244, s79\n"
    "v_pk_max_i16 v245, v245, s79\n"
    "v_pk_max_i16 v246, v246, s79\n"
    "v_pk_max_i16 v247, v247, s79\n"
    ".p2align 12\n"
    "L_pg4_%=:\n"
    "s_cbranch_execz L_tramp_%=\n"
    "v_mfma_f32_16x16x32_bf16 v[164:167], a[218:221], a[186:189], v[164:167]\n"
    "buffer_store_dwordx4 v[244:247], v54, s[28:31], s95 offen offset:1280 sc1\n"
    "ds_read_b64_tr_b16 a[196:197], v72 offset:47104\n"
    "v_mfma_f32_16x16x32_bf16 v[168:171], a[222:225], v[140:143], v[168:171]\n"
    "ds_read_b64_tr_b16 a[198:199], v72 offset:49152\n"
    "s_add_u32 m0, s87, 57344\n"
    "v_mfma_f32_16x16x32_bf16 v[168:171], a[226:229], v[144:147], v[168:171]\n"
    "ds_read_b64_tr_b16 a[200:201], v72 offset:51200\n"
    "s_waitcnt lgkmcnt(4)\n"
    "v_mfma_f32_16x16x32_bf16 v[168:171], a[230:233], v[148:151], v[168:171]\n"
    "ds_read_b64_tr_b16 a[202:203], v72 offset:53248\n"
    "v_mul_f32 v2, v2, v87\n"
    "v_mfma_f32_16x16x32_bf16 v[168:171], a[234:237], v[152:155], v[168:171]\n"
    "ds_read_b64_tr_b16 a[204:205], v72 offset:55296\n"
    "buffer_load_dwordx4 v61, s[16:19], s66 offen offset:0 lds\n"
    "v_mfma_f32_16x16x32_bf16 v[168:171], a[238:241], v[156:159], v[168:171]\n"
    "ds_read_b64_tr_b16 a[206:207], v74 offset:32768\n"
    "v_mul_f32 v3, v3, v87\n"
    "v_mfma_f32_16x16x32_bf16 v[168:171], a[242:245], v[160:163], v[168:171]\n"
    "ds_read_b64_tr_b16 a[208:209], v74 offset:34816\n"
    "v_mul_f32 v4, v4, v87\n"
    "v_mfma_f32_16x16x32_bf16 v[172:175], a[222:225], a[166:169], v[172:175]\n"
    "ds_read_b64_tr_b16 a[210:211], v74 offset:36864\n"
    "buffer_load_dwordx4 v62, s[16:19], s66 offen offset:1024 lds\n"
    "v_mfma_f32_16x16x32_bf16 v[172:175], a[226:229], a[170:173], v[172:175]\n"
    "ds_read_b64_tr_b16 a[212:213], v74 offset:38912\n"
    "v_mul_f32 v5, v5, v87\n"
    "v_mfma_f32_16x16x32_bf16 v[172:175], a[230:233], a[174:177], v[172:175]\n"
    "ds_read_b64_tr_b16 a[214:215], v74 offset:40960\n"
    "v_cvt_pk_bf16_f32 v2, v2, v3\n"
    "v_mfma_f32_16x16x32_bf16 v[172:175], a[234:237], a[178:181], v[172:175]\n"
    "ds_read_b64_tr_b16 a[216:217], v74 offset:43008\n"
    "v_cvt_pk_bf16_f32 v5, v4, v5\n"
    "v_mfma_f32_16x16x32_bf16 v[172:175], a[238:241], a[182:185], v[172:175]\n"
    "ds_read_b64_tr_b16 a[218:219], v74 offset:45056\n"
    "v_cndmask_b32 v3, v5, v29, s[74:75]\n"
    "v_mfma_f32_16x16x32_bf16 v[172:175], a[242:245], a[186:189], v[172:175]\n"
    "ds_read_b64_tr_b16 a[220:221], v74 offset:47104\n"
    "v_cndmask_b32 v4, v42, v5, s[74:75]\n"
    "v_mfma_f32_16x16x32_bf16 v[176:179], a[246:249], v[140:143], v[176:179]\n"
    "ds_read_b64_tr_b16 a[222:223], v74 offset:49152\n"
    "v_mov_b32 v5, v29\n"
    "v_mfma_f32_16x16x32_bf16 v[176:179], a[250:253], v[144:147], v[176:179]\n"
    "buffer_store_dwordx4 v[2:5], v44, s[28:31], 0 offen offset:0 sc1\n"
    "ds_read_b64_tr_b16 a[224:225], v74 offset:51200\n"
    "v_mfma_f32_16x16x32_bf16 v[176:179], a[190:193], v[148:151], v[176:179]\n"
    "ds_read_b64_tr_b16 a[226:227], v74 offset:53248\n"
    "s_waitcnt vmcnt(19) lgkmcnt(6)\n"
    "v_mfma_f32_16x16x32_bf16 v[176:179], a[194:197], v[152:155], v[176:179]\n"
    "s_barrier\n"
    "ds_read_b64_tr_b16 a[228:229], v74 offset:55296\n"
    "v_mfma_f32_16x16x32_bf16 v[176:179], a[198:201], v[156:159], v[176:179]\n"
    "ds_read_b64_tr_b16 a[230:231], v67 offset:0\n"
    "s_add_u32 m0, s87, 65536\n"
    "v_mfma_f32_16x16x32_bf16 v[176:179], a[202:205], v[160:163], v[176:179]\n"
    "ds_read_b64_tr_b16 a[232:233], v67 offset:2048\n"
    "buffer_load_dwordx4 v61, s[16:19], s67 offen offset:0 lds\n"
    "v_mfma_f32_16x16x32_bf16 v[180:183], a[246:249], a[166:169], v[180:183]\n"
    "ds_read_b64_tr_b16 a[234:235], v67 offset:4096\n"
    "v_mul_f32 v6, v6, v91\n"
    "v_mfma_f32_16x16x32_bf16 v[180:183], a[250:253], a[170:173], v[180:183]\n"
    "ds_read_b64_tr_b16 a[236:237], v67 offset:6144\n"
    "v_mul_f32 v7, v7, v91\n"
    "v_mfma_f32_16x16x32_bf16 v[180:183], a[190:193], a[174:177], v[180:183]\n"
    "ds_read_b64_tr_b16 a[238:239], v67 offset:8192\n"
    "buffer_load_dwordx4 v62, s[16:19], s67 offen offset:1024 lds\n"
    "v_mfma_f32_16x16x32_bf16 v[180:183], a[194:197], a[178:181], v[180:183]\n"
    "ds_read_b64_tr_b16 a[240:241], v67 offset:10240\n"
    "v_mul_f32 v8, v8, v91\n"
    "v_mfma_f32_16x16x32_bf16 v[180:183], a[198:201], a[182:185], v[180:183]\n"
    "ds_read_b64_tr_b16 a[242:243], v67 offset:12288\n"
    "v_mul_f32 v9, v9, v91\n"
    "v_mfma_f32_16x16x32_bf16 v[180:183], a[202:205], a[186:189], v[180:183]\n"
    "ds_read_b64_tr_b16 a[244:245], v67 offset:14336\n"
    "v_cvt_pk_bf16_f32 v6, v6, v7\n"
    "v_mfma_f32_16x16x32_bf16 v[184:187], a[206:209], v[140:143], v[184:187]\n"
    "ds_read_b64_tr_b16 a[246:247], v67 offset:16384\n"
    "v_cvt_pk_bf16_f32 v9, v8, v9\n"
    "v_mfma_f32_16x16x32_bf16 v[184:187], a[210:213], v[144:147], v[184:187]\n"
    "ds_read_b64_tr_b16 a[248:249], v67 offset:18432\n"
    "s_waitcnt lgkmcnt(7)\n"
    "v_mfma_f32_16x16x32_bf16 v[184:187], a[214:217], v[148:151], v[184:187]\n"
    "ds_read_b64_tr_b16 a[250:251], v67 offset:20480\n"
    "v_cndmask_b32 v7, v9, v29, s[74:75]\n"
    "v_cndmask_b32 v8, v42, v9, s[74:75]\n"
    "v_mov_b32 v9, v29\n"
    "v_mfma_f32_16x16x32_bf16 v[184:187], a[218:221], v[152:155], v[184:187]\n"
    "buffer_store_dwordx4 v[6:9], v44, s[28:31], 0 offen offset:1280 sc1\n"
    "ds_read_b64_tr_b16 a[252:253], v67 offset:22528\n"
    "v_mfma_f32_16x16x32_bf16 v[184:187], a[222:225], v[156:159], v[184:187]\n"
    "ds_read_b64_tr_b16 a[190:191], v69 offset:0\n"
    "v_add_u32 v61, 128, v61\n"
    "v_mfma_f32_16x16x32_bf16 v[184:187], a[226:229], v[160:163], v[184:187]\n"
    "ds_read_b64_tr_b16 a[192:193], v69 offset:2048\n"
    "s_add_u32 m0, s87, 73728\n"
    "v_mfma_f32_16x16x32_bf16 v[30:33], a[206:209], a[166:169], v[30:33]\n"
    "ds_read_b64_tr_b16 a[194:195], v69 offset:4096\n"
    "v_cvt_pk_bf16_f32 v34, v188, v189\n"
    "v_mfma_f32_16x16x32_bf16 v[30:33], a[210:213], a[170:173], v[30:33]\n"
    "ds_read_b64_tr_b16 a[196:197], v69 offset:6144\n"
    "buffer_load_dwordx4 v61, s[16:19], 0 offen offset:0 lds\n"
    "v_mfma_f32_16x16x32_bf16 v[30:33], a[214:217], a[174:177], v[30:33]\n"
    "ds_read_b64_tr_b16 a[198:199], v69 offset:8192\n"
    "v_cvt_pk_bf16_f32 v35, v190, v191\n"
    "v_mfma_f32_16x16x32_bf16 v[30:33], a[218:221], a[178:181], v[30:33]\n"
    "ds_read_b64_tr_b16 a[200:201], v69 offset:10240\n"
    "v_cvt_pk_bf16_f32 v46, v164, v165\n"
    "v_mfma_f32_16x16x32_bf16 v[30:33], a[222:225], a[182:185], v[30:33]\n"
    "ds_read_b64_tr_b16 a[202:203], v69 offset:12288\n"
    "v_cvt_pk_bf16_f32 v47, v166, v167\n"
    "v_mfma_f32_16x16x32_bf16 v[30:33], a[226:229], a[186:189], v[30:33]\n"
    "ds_read_b64_tr_b16 a[204:205], v69 offset:14336\n"
    "v_add_u32 v62, 128, v62\n"
    "v_mfma_f32_16x16x32_bf16 v[188:191], a[230:233], v[92:95], 0\n"
    "ds_read_b64_tr_b16 a[206:207], v69 offset:16384\n"
    "s_waitcnt lgkmcnt(7)\n"
    "v_mfma_f32_16x16x32_bf16 v[188:191], a[234:237], v[96:99], v[188:191]\n"
    "ds_read_b64_tr_b16 a[208:209], v69 offset:18432\n"
    "buffer_load_dwordx4 v62, s[16:19], 0 offen offset:1024 lds\n"
    "v_mfma_f32_16x16x32_bf16 v[188:191], a[238:241], v[100:103], v[188:191]\n"
    "ds_read_b64_tr_b16 a[210:211], v69 offset:20480\n"
    "v_cvt_pk_bf16_f32 v36, v168, v169\n"
    "v_mfma_f32_16x16x32_bf16 v[188:191], a[242:245], v[104:107], v[188:191]\n"
    "ds_read_b64_tr_b16 a[212:213], v69 offset:22528\n"
    "v_cvt_pk_bf16_f32 v37, v170, v171\n"
    "v_mfma_f32_16x16x32_bf16 v[188:191], a[246:249], v[108:111], v[188:191]\n"
    "ds_read_b64_tr_b16 a[214:215], v71 offset:0\n"
    "v_cvt_pk_bf16_f32 v48, v172, v173\n"
    "v_mfma_f32_16x16x32_bf16 v[188:191], a[250:253], v[112:115], v[188:191]\n"
    "ds_read_b64_tr_b16 a[216:217], v71 offset:2048\n"
    "v_cvt_pk_bf16_f32 v49, v174, v175\n"
    "v_mfma_f32_16x16x32_bf16 v[164:167], a[230:233], a[118:121], 0\n"
    "buffer_store_dwordx4 a[42:45], v28, s[28:31], s51 offen offset:0 sc1\n"
    "ds_read_b64_tr_b16 a[218:219], v71 offset:4096\n"
    "v_mfma_f32_16x16x32_bf16 v[164:167], a[234:237], a[122:125], v[164:167]\n"
    "ds_read_b64_tr_b16 a[220:221], v71 offset:6144\n"
    "s_add_u32 s82, s91, 4096\n"
    "v_mfma_f32_16x16x32_bf16 v[164:167], a[238:241], a[126:129], v[164:167]\n"
    "ds_read_b64_tr_b16 a[222:223], v71 offset:8192\n"
    "s_add_u32 m0, s87, 81920\n"
    "v_mfma_f32_16x16x32_bf16 v[164:167], a[242:245], a[130:133], v[164:167]\n"
    "ds_read_b64_tr_b16 a[224:225], v71 offset:10240\n"
    "v_cvt_pk_bf16_f32 v50, v176, v177\n"
    "v_mfma_f32_16x16x32_bf16 v[164:167], a[246:249], a[134:137], v[164:167]\n"
    "ds_read_b64_tr_b16 a[226:227], v71 offset:12288\n"
    "buffer_load_dwordx4 v61, s[16:19], s60 offen offset:0 lds\n"
    "v_mfma_f32_16x16x32_bf16 v[164:167], a[250:253], a[138:141], v[164:167]\n"
    "ds_read_b64_tr_b16 a[228:229], v71 offset:14336\n"
    "v_cvt_pk_bf16_f32 v51, v178, v179\n"
    "v_mfma_f32_16x16x32_bf16 v[168:171], a[190:193], v[92:95], 0\n"
    "ds_read_b64_tr_b16 a[230:231], v71 offset:16384\n"
    "s_waitcnt lgkmcnt(7)\n"
    "v_mfma_f32_16x16x32_bf16 v[168:171], a[194:197], v[96:99], v[168:171]\n"
    "ds_read_b64_tr_b16 a[232:233], v71 offset:18432\n"
    "v_cvt_pk_bf16_f32 v76, v180, v181\n"
    "v_mfma_f32_16x16x32_bf16 v[168:171], a[198:201], v[100:103], v[168:171]\n"
    "ds_read_b64_tr_b16 a[234:235], v71 offset:20480\n"
    "v_cvt_pk_bf16_f32 v77, v182, v183\n"
    "v_mfma_f32_16x16x32_bf16 v[168:171], a[202:205], v[104:107], v[168:171]\n"
    "ds_read_b64_tr_b16 a[236:237], v71 offset:22528\n"
    "v_cvt_pk_bf16_f32 v52, v184, v185\n"
    "v_mfma_f32_16x16x32_bf16 v[168:171], a[206:209], v[108:111], v[168:171]\n"
    "ds_read_b64_tr_b16 a[238:239], v73 offset:0\n"
    "buffer_load_dword v43, v25, s[28:31], s82 offen offset:0 sc1\n"
    "v_mfma_f32_16x16x32_bf16 v[168:171], a[210:213], v[112:115], v[168:171]\n"
    "ds_read_b64_tr_b16 a[240:241], v73 offset:2048\n"
    "v_cvt_pk_bf16_f32 v53, v186, v187\n"
    "v_mfma_f32_16x16x32_bf16 v[172:175], a[190:193], a[118:121], 0\n"
    "ds_read_b64_tr_b16 a[242:243], v73 offset:4096\n"
    "buffer_load_dwordx4 v62, s[16:19], s60 offen offset:1024 lds\n"
    "v_mfma_f32_16x16x32_bf16 v[172:175], a[194:197], a[122:125], v[172:175]\n"
    "ds_read_b64_tr_b16 a[244:245], v73 offset:6144\n"
    "v_cvt_pk_bf16_f32 v78, v30, v31\n"
    "v_mfma_f32_16x16x32_bf16 v[172:175], a[198:201], a[126:129], v[172:175]\n"
    "ds_read_b64_tr_b16 a[246:247], v73 offset:8192\n"
    "v_cvt_pk_bf16_f32 v79, v32, v33\n"
    "v_mfma_f32_16x16x32_bf16 v[172:175], a[202:205], a[130:133], v[172:175]\n"
    "ds_read_b64_tr_b16 a[248:249], v73 offset:10240\n"
    "v_pk_max_i16 v34, v34, 0\n"
    "v_mfma_f32_16x16x32_bf16 v[172:175], a[206:209], a[134:137], v[172:175]\n"
    "ds_read_b64_tr_b16 a[250:251], v73 offset:12288\n"
    "v_pk_max_i16 v35, v35, 0\n"
    "v_mfma_f32_16x16x32_bf16 v[172:175], a[210:213], a[138:141], v[172:175]\n"
    "ds_read_b64_tr_b16 a[252:253], v73 offset:14336\n"
    "v_pk_max_i16 v36, v36, 0\n"
    "v_mfma_f32_16x16x32_bf16 v[176:179], a[214:217], v[92:95], 0\n"
    "ds_read_b64_tr_b16 a[190:191], v73 offset:16384\n"
    "s_waitcnt lgkmcnt(7)\n"
    "v_mfma_f32_16x16x32_bf16 v[176:179], a[218:221], v[96:99], v[176:179]\n"
    "ds_read_b64_tr_b16 a[192:193], v73 offset:18432\n"
    "s_add_u32 m0, s87, 90112\n"
    "v_mfma_f32_16x16x32_bf16 v[176:179], a[222:225], v[100:103], v[176:179]\n"
    "ds_read_b64_tr_b16 a[194:195], v73 offset:20480\n"
    "s_waitcnt vmcnt(15) lgkmcnt(7)\n"
    "v_mfma_f32_16x16x32_bf16 v[176:179], a[226:229], v[104:107], v[176:179]\n"
    "s_barrier\n"
    "ds_read_b64_tr_b16 a[196:197], v73 offset:22528\n"
    "v_mfma_f32_16x16x32_bf16 v[176:179], a[230:233], v[108:111], v[176:179]\n"
    "ds_read_b64_tr_b16 a[198:199], v67 offset:24576\n"
    "buffer_load_dwordx4 v61, s[16:19], s61 offen offset:0 lds\n"
    "v_mfma_f32_16x16x32_bf16 v[176:179], a[234:237], v[112:115], v[176:179]\n"
    "ds_read_b64_tr_b16 a[200:201], v67 offset:26624\n"
    "v_pk_max_i16 v37, v37, 0\n"
    "v_mfma_f32_16x16x32_bf16 v[180:183], a[214:217], a[118:121], 0\n"
    "ds_read_b64_tr_b16 a[202:203], v67 offset:28672\n"
    "v_pk_max_i16 v46, v46, 0\n"
    "v_mfma_f32_16x16x32_bf16 v[180:183], a[218:221], a[122:125], v[180:183]\n"
    "buffer_store_dwordx4 a[42:45], v28, s[28:31], s51 offen offset:1024 sc1\n"
    "ds_read_b64_tr_b16 a[204:205], v67 offset:30720\n"
    "v_mfma_f32_16x16x32_bf16 v[180:183], a[222:225], a[126:129], v[180:183]\n"
    "ds_read_b64_tr_b16 a[206:207], v67 offset:32768\n"
    "v_pk_max_i16 v47, v47, 0\n"
    "v_mfma_f32_16x16x32_bf16 v[180:183], a[226:229], a[130:133], v[180:183]\n"
    "ds_read_b64_tr_b16 a[208:209], v67 offset:34816\n"
    "v_pk_max_i16 v48, v48, 0\n"
    "v_mfma_f32_16x16x32_bf16 v[180:183], a[230:233], a[134:137], v[180:183]\n"
    "ds_read_b64_tr_b16 a[210:211], v67 offset:36864\n"
    "v_pk_max_i16 v49, v49, 0\n"
    "v_mfma_f32_16x16x32_bf16 v[180:183], a[234:237], a[138:141], v[180:183]\n"
    "ds_read_b64_tr_b16 a[212:213], v67 offset:38912\n"
    "buffer_load_dwordx4 v62, s[16:19], s61 offen offset:1024 lds\n"
    "v_mfma_f32_16x16x32_bf16 v[184:187], a[238:241], v[92:95], 0\n"
    "ds_read_b64_tr_b16 a[214:215], v67 offset:40960\n"
    "v_pk_max_i16 v50, v50, 0\n"
    "v_mfma_f32_16x16x32_bf16 v[184:187], a[242:245], v[96:99], v[184:187]\n"
    "ds_read_b64_tr_b16 a[216:217], v67 offset:43008\n"
    "s_waitcnt lgkmcnt(7)\n"
    "v_mfma_f32_16x16x32_bf16 v[184:187], a[246:249], v[100:103], v[184:187]\n"
    "ds_read_b64_tr_b16 a[218:219], v67 offset:45056\n"
    "v_pk_max_i16 v51, v51, 0\n"
    "v_mfma_f32_16x16x32_bf16 v[184:187], a[250:253], v[104:107], v[184:187]\n"
    "ds_read_b64_tr_b16 a[220:221], v67 offset:47104\n"
    "v_pk_max_i16 v52, v52, 0\n"
    "v_mfma_f32_16x16x32_bf16 v[184:187], a[190:193], v[108:111], v[184:187]\n"
    "ds_read_b64_tr_b16 a[222:223], v69 offset:24576\n"
    "s_add_u32 m0, s87, 98304\n"
    "v_mfma_f32_16x16x32_bf16 v[184:187], a[194:197], v[112:115], v[184:187]\n"
    "ds_read_b64_tr_b16 a[224:225], v69 offset:26624\n"
    "buffer_load_dwordx4 v61, s[16:19], s62 offen offset:0 lds\n"
    "v_mfma_f32_16x16x32_bf16 v[30:33], a[238:241], a[118:121], 0\n"
    "ds_read_b64_tr_b16 a[226:227], v69 offset:28672\n"
    "v_pk_max_i16 v53, v53, 0\n"
    "v_mfma_f32_16x16x32_bf16 v[30:33], a[242:245], a[122:125], v[30:33]\n"
    "ds_read_b64_tr_b16 a[228:229], v69 offset:30720\n"
    "v_pk_max_i16 v76, v76, 0\n"
    "v_mfma_f32_16x16x32_bf16 v[30:33], a[246:249], a[126:129], v[30:33]\n"
    "ds_read_b64_tr_b16 a[230:231], v69 offset:32768\n"
    "v_pk_max_i16 v77, v77, 0\n"
    "v_mfma_f32_16x16x32_bf16 v[30:33], a[250:253], a[130:133], v[30:33]\n"
    "ds_read_b64_tr_b16 a[232:233], v69 offset:34816\n"
    "v_pk_max_i16 v78, v78, 0\n"
    "v_mfma_f32_16x16x32_bf16 v[30:33], a[190:193], a[134:137], v[30:33]\n"
    "ds_read_b64_tr_b16 a[234:235], v69 offset:36864\n"
    "v_pk_max_i16 v79, v79, 0\n"
    "v_mfma_f32_16x16x32_bf16 v[30:33], a[194:197], a[138:141], v[30:33]\n"
    "ds_read_b64_tr_b16 a[236:237], v69 offset:38912\n"
    "s_waitcnt vmcnt(10) lgkmcnt(7)\n"
    "v_mfma_f32_16x16x32_bf16 v[188:191], a[198:201], v[116:119], v[188:191]\n"
    "ds_read_b64_tr_b16 a[238:239], v69 offset:40960\n"
    "buffer_store_dword a42, v19, s[28:31], s90 offen offset:0 sc1\n"
    "v_mfma_f32_16x16x32_bf16 v[188:191], a[202:205], v[120:123], v[188:191]\n"
    "ds_read_b64_tr_b16 a[240:241], v69 offset:43008\n"
    "v_accvgpr_read_b32 v55, a39\n"
    "v_mfma_f32_16x16x32_bf16 v[188:191], a[206:209], v[124:127], v[188:191]\n"
    "ds_read_b64_tr_b16 a[242:243], v69 offset:45056\n"
    "buffer_load_dwordx4 v62, s[16:19], s62 offen offset:1024 lds\n"
    "v_mfma_f32_16x16x32_bf16 v[188:191], a[210:213], v[128:131], v[188:191]\n"
    "ds_read_b64_tr_b16 a[244:245], v69 offset:47104\n"
    "s_add_u32 s98, s58, 4096\n"
    "v_mfma_f32_16x16x32_bf16 v[188:191], a[214:217], v[132:135], v[188:191]\n"
    "ds_read_b64_tr_b16 a[246:247], v71 offset:24576\n"
    "s_add_u32 s81, s57, 4096\n"
    "v_mfma_f32_16x16x32_bf16 v[188:191], a[218:221], v[136:139], v[188:191]\n"
    "ds_read_b64_tr_b16 a[248:249], v71 offset:26624\n"
    "v_accvgpr_read_b32 v56, a1\n"
    "v_mfma_f32_16x16x32_bf16 v[164:167], a[198:201], a[142:145], v[164:167]\n"
    "buffer_store_dwordx4 v[34:37], v28, s[28:31], s58 offen offset:0 sc1\n"
    "ds_read_b64_tr_b16 a[250:251], v71 offset:28672\n"
    "v_mfma_f32_16x16x32_bf16 v[164:167], a[202:205], a[146:149], v[164:167]\n"
    "ds_read_b64_tr_b16 a[252:253], v71 offset:30720\n"
    "s_add_u32 s84, s37, s32\n"
    "v_mfma_f32_16x16x32_bf16 v[164:167], a[206:209], a[150:153], v[164:167]\n"
    "ds_read_b64_tr_b16 a[190:191], v71 offset:32768\n"
    "s_add_u32 m0, s87, 106496\n"
    "v_mfma_f32_16x16x32_bf16 v[164:167], a[210:213], a[154:157], v[164:167]\n"
    "ds_read_b64_tr_b16 a[192:193], v71 offset:34816\n"
    "v_accvgpr_read_b32 v29, a32\n"
    "v_mfma_f32_16x16x32_bf16 v[164:167], a[214:217], a[158:161], v[164:167]\n"
    "ds_read_b64_tr_b16 a[194:195], v71 offset:36864\n"
    "buffer_load_dwordx4 v61, s[16:19], s63 offen offset:0 lds\n"
    "v_mfma_f32_16x16x32_bf16 v[164:167], a[218:221], a[162:165], v[164:167]\n"
    "ds_read_b64_tr_b16 a[196:197], v71 offset:38912\n"
    "s_waitcnt lgkmcnt(6)\n"
    "v_mfma_f32_16x16x32_bf16 v[168:171], a[222:225], v[116:119], v[168:171]\n"
    "ds_read_b64_tr_b16 a[198:199], v71 offset:40960\n"
    "s_sub_u32 s80, s57, s86\n"
    "v_mfma_f32_16x16x32_bf16 v[168:171], a[226:229], v[120:123], v[168:171]\n"
    "ds_read_b64_tr_b16 a[200:201], v71 offset:43008\n"
    "v_add_u32 v57, 35840, v56\n"
    "v_mfma_f32_16x16x32_bf16 v[168:171], a[230:233], v[124:127], v[168:171]\n"
    "ds_read_b64_tr_b16 a[202:203], v71 offset:45056\n"
    "v_add_u32 v24, 35840, v57\n"
    "v_mfma_f32_16x16x32_bf16 v[168:171], a[234:237], v[128:131], v[168:171]\n"
    "ds_read_b64_tr_b16 a[204:205], v71 offset:47104\n"
    "v_add_u32 v75, 35840, v24\n"
    "v_mfma_f32_16x16x32_bf16 v[168:171], a[238:241], v[132:135], v[168:171]\n"
    "ds_read_b64_tr_b16 a[206:207], v73 offset:24576\n"
    "buffer_load_dwordx4 v62, s[16:19], s63 offen offset:1024 lds\n"
    "v_mfma_f32_16x16x32_bf16 v[168:171], a[242:245], v[136:139], v[168:171]\n"
    "ds_read_b64_tr_b16 a[208:209], v73 offset:26624\n"
    "s_add_u32 s89, s39, s32\n"
    "v_mfma_f32_16x16x32_bf16 v[172:175], a[222:225], a[142:145], v[172:175]\n"
    "ds_read_b64_tr_b16 a[210:211], v73 offset:28672\n"
    "s_xor_b32 s99, s90, 1024\n"
    "v_mfma_f32_16x16x32_bf16 v[172:175], a[226:229], a[146:149], v[172:175]\n"
    "ds_read_b64_tr_b16 a[212:213], v73 offset:30720\n"
    "v_accvgpr_read_b32 v86, a38\n"
    "v_mfma_f32_16x16x32_bf16 v[172:175], a[230:233], a[150:153], v[172:175]\n"
    "ds_read_b64_tr_b16 a[214:215], v73 offset:32768\n"
    "s_add_u32 s59, s59, s85\n"
    "v_mfma_f32_16x16x32_bf16 v[172:175], a[234:237], a[154:157], v[172:175]\n"
    "ds_read_b64_tr_b16 a[216:217], v73 offset:34816\n"
    "s_mov_b32 s26, 2359296\n"
    "v_mfma_f32_16x16x32_bf16 v[172:175], a[238:241], a[158:161], v[172:175]\n"
    "ds_read_b64_tr_b16 a[218:219], v73 offset:36864\n"
    "s_mov_b32 s27, 0x20000\n"
    "v_mfma_f32_16x16x32_bf16 v[172:175], a[242:245], a[162:165], v[172:175]\n"
    "ds_read_b64_tr_b16 a[220:221], v73 offset:38912\n"
    "v_accvgpr_read_b32 v44, a34\n"
    "v_mfma_f32_16x16x32_bf16 v[176:179], a[246:249], v[116:119], v[176:179]\n"
    "ds_read_b64_tr_b16 a[222:223], v73 offset:40960\n"
    "s_waitcnt lgkmcnt(7)\n"
    "v_mfma_f32_16x16x32_bf16 v[176:179], a[250:253], v[120:123], v[176:179]\n"
    "ds_read_b64_tr_b16 a[224:225], v73 offset:43008\n"
    "s_add_u32 m0, s87, 114688\n"
    "v_mfma_f32_16x16x32_bf16 v[176:179], a[190:193], v[124:127], v[176:179]\n"
    "s_barrier\n"
    "ds_read_b64_tr_b16 a[226:227], v73 offset:45056\n"
    "v_mfma_f32_16x16x32_bf16 v[176:179], a[194:197], v[128:131], v[176:179]\n"
    "ds_read_b64_tr_b16 a[228:229], v73 offset:47104\n"
    "buffer_load_dwordx4 v61, s[16:19], s64 offen offset:0 lds\n"
    "v_mfma_f32_16x16x32_bf16 v[176:179], a[198:201], v[132:135], v[176:179]\n"
    "ds_read_b64_tr_b16 a[230:231], v67 offset:49152\n"
    "v_accvgpr_read_b32 v45, a37\n"
    "v_mfma_f32_16x16x32_bf16 v[176:179], a[202:205], v[136:139], v[176:179]\n"
    "ds_read_b64_tr_b16 a[232:233], v67 offset:51200\n"
    "v_accvgpr_read_b32 v85, a36\n"
    "v_mfma_f32_16x16x32_bf16 v[180:183], a[246:249], a[142:145], v[180:183]\n"
    "buffer_store_dwordx4 v[46:49], v28, s[28:31], s58 offen offset:1024 sc1\n"
    "ds_read_b64_tr_b16 a[234:235], v67 offset:53248\n"
    "v_mfma_f32_16x16x32_bf16 v[180:183], a[250:253], a[146:149], v[180:183]\n"
    "ds_read_b64_tr_b16 a[236:237], v67 offset:55296\n"
    "v_accvgpr_read_b32 v248, a20\n"
    "v_mfma_f32_16x16x32_bf16 v[180:183], a[190:193], a[150:153], v[180:183]\n"
    "ds_read_b64_tr_b16 a[238:239], v67 offset:57344\n"
    "v_xor_b32 v236, 0xf149f2ca, v248\n"
    "v_mfma_f32_16x16x32_bf16 v[180:183], a[194:197], a[154:157], v[180:183]\n"
    "ds_read_b64_tr_b16 a[240:241], v67 offset:59392\n"
    "v_accvgpr_read_b32 v249, a21\n"
    "v_mfma_f32_16x16x32_bf16 v[180:183], a[198:201], a[158:161], v[180:183]\n"
    "ds_read_b64_tr_b16 a[242:243], v67 offset:61440\n"
    "buffer_load_dwordx4 v62, s[16:19], s64 offen offset:1024 lds\n"
    "v_mfma_f32_16x16x32_bf16 v[180:183], a[202:205], a[162:165], v[180:183]\n"
    "ds_read_b64_tr_b16 a[244:245], v67 offset:63488\n"
    "v_xor_b32 v237, 0xf149f2ca, v249\n"
    "v_mfma_f32_16x16x32_bf16 v[184:187], a[206:209], v[116:119], v[184:187]\n"
    "ds_read_b64_tr_b16 a[246:247], v68 offset:0\n"
    "s_waitcnt lgkmcnt(7)\n"
    "v_mfma_f32_16x16x32_bf16 v[184:187], a[210:213], v[120:123], v[184:187]\n"
    "ds_read_b64_tr_b16 a[248:249], v68 offset:2048\n"
    "v_accvgpr_read_b32 v250, a22\n"
    "v_mfma_f32_16x16x32_bf16 v[184:187], a[214:217], v[124:127], v[184:187]\n"
    "ds_read_b64_tr_b16 a[250:251], v68 offset:4096\n"
    "v_xor_b32 v238, 0xf149f2ca, v250\n"
    "v_mfma_f32_16x16x32_bf16 v[184:187], a[218:221], v[128:131], v[184:187]\n"
    "ds_read_b64_tr_b16 a[252:253], v68 offset:6144\n"
    "v_accvgpr_read_b32 v251, a23\n"
    "v_mfma_f32_16x16x32_bf16 v[184:187], a[222:225], v[132:135], v[184:187]\n"
    "ds_read_b64_tr_b16 a[190:191], v69 offset:49152\n"
    "s_add_u32 m0, s87, 0\n"
    "v_mfma_f32_16x16x32_bf16 v[184:187], a[226:229], v[136:139], v[184:187]\n"
    "ds_read_b64_tr_b16 a[192:193], v69 offset:51200\n"
    "buffer_load_dwordx4 v61, s[16:19], s65 offen offset:0 lds\n"
    "v_mfma_f32_16x16x32_bf16 v[30:33], a[206:209], a[142:145], v[30:33]\n"
    "ds_read_b64_tr_b16 a[194:195], v69 offset:53248\n"
    "v_xor_b32 v239, 0xf149f2ca, v251\n"
    "v_mfma_f32_16x16x32_bf16 v[30:33], a[210:213], a[146:149], v[30:33]\n"
    "ds_read_b64_tr_b16 a[196:197], v69 offset:55296\n"
    "ds_read_b64_tr_b16 a[50:51], v15 offset:0\n"
    "v_mfma_f32_16x16x32_bf16 v[30:33], a[214:217], a[150:153], v[30:33]\n"
    "ds_read_b64_tr_b16 a[198:199], v69 offset:57344\n"
    "v_add_u32 v15, s32, v16\n"
    "v_mfma_f32_16x16x32_bf16 v[30:33], a[218:221], a[154:157], v[30:33]\n"
    "ds_read_b64_tr_b16 a[200:201], v69 offset:59392\n"
    "v_mfma_f32_16x16x32_bf16 v[30:33], a[222:225], a[158:161], v[30:33]\n"
    "ds_read_b64_tr_b16 a[202:203], v69 offset:61440\n"
    "ds_read_b64_tr_b16 a[54:55], v15 offset:0\n"
    "v_mfma_f32_16x16x32_bf16 v[30:33], a[226:229], a[162:165], v[30:33]\n"
    "ds_read_b64_tr_b16 a[204:205], v69 offset:63488\n"
    "v_mfma_f32_16x16x32_bf16 v[188:191], a[230:233], v[140:143], v[188:191]\n"
    "ds_read_b64_tr_b16 a[206:207], v70 offset:0\n"
    "s_waitcnt lgkmcnt(9)\n"
    "v_mfma_f32_16x16x32_bf16 v[188:191], a[234:237], v[144:147], v[188:191]\n"
    "ds_read_b64_tr_b16 a[208:209], v70 offset:2048\n"
    "buffer_load_dwordx4 v62, s[16:19], s65 offen offset:1024 lds\n"
    "v_mfma_f32_16x16x32_bf16 v[188:191], a[238:241], v[148:151], v[188:191]\n"
    "ds_read_b64_tr_b16 a[210:211], v70 offset:4096\n"
    "s_waitcnt vmcnt(15) lgkmcnt(8)\n"
    "v_cmp_ne_u32 vcc, v43, v27\n"
    "v_mfma_f32_16x16x32_bf16 v[188:191], a[242:245], v[152:155], v[188:191]\n"
    "ds_read_b64_tr_b16 a[212:213], v70 offset:6144\n"
    "ds_read_b64_tr_b16 a[56:57], v15 offset:6144\n"
    "v_mfma_f32_16x16x32_bf16 v[188:191], a[246:249], v[156:159], v[188:191]\n"
    "ds_read_b64_tr_b16 a[214:215], v71 offset:49152\n"
    "s_cbranch_vccnz L_rel_%=\n"
    "L_rel_ret_%=:\n"
    "v_mfma_f32_16x16x32_bf16 v[188:191], a[250:253], v[160:163], v[188:191]\n"
    "ds_read_b64_tr_b16 a[216:217], v71 offset:51200\n"
    "v_add_u32 v15, s33, v16\n"
    "v_mfma_f32_16x16x32_bf16 v[164:167], a[230:233], a[166:169], v[164:167]\n"
    "buffer_store_dwordx4 v[50:53], v28, s[28:31], s58 offen offset:2048 sc1\n"
    "ds_read_b64_tr_b16 a[218:219], v71 offset:53248\n"
    "v_mfma_f32_16x16x32_bf16 v[164:167], a[234:237], a[170:173], v[164:167]\n"
    "ds_read_b64_tr_b16 a[220:221], v71 offset:55296\n"
    "ds_read_b64_tr_b16 a[58:59], v15 offset:0\n"
    "v_mfma_f32_16x16x32_bf16 v[164:167], a[238:241], a[174:177], v[164:167]\n"
    "ds_read_b64_tr_b16 a[222:223], v71 offset:57344\n"
    "s_add_u32 m0, s87, 8192\n"
    "v_mfma_f32_16x16x32_bf16 v[164:167], a[242:245], a[178:181], v[164:167]\n"
    "ds_read_b64_tr_b16 a[224:225], v71 offset:59392\n"
    "v_cvt_pk_bf16_f32 v80, v188, v189\n"
    "v_mfma_f32_16x16x32_bf16 v[164:167], a[246:249], a[182:185], v[164:167]\n"
    "ds_read_b64_tr_b16 a[226:227], v71 offset:61440\n"
    "buffer_load_dwordx4 v61, s[16:19], s66 offen offset:0 lds\n"
    "v_mfma_f32_16x16x32_bf16 v[164:167], a[250:253], a[186:189], v[164:167]\n"
    "ds_read_b64_tr_b16 a[228:229], v71 offset:63488\n"
    "v_cvt_pk_bf16_f32 v81, v190, v191\n"
    "v_mfma_f32_16x16x32_bf16 v[168:171], a[190:193], v[140:143], v[168:171]\n"
    "ds_read_b64_tr_b16 a[230:231], v72 offset:0\n"
    "v_pk_max_i16 v80, v80, 0\n"
    "v_mfma_f32_16x16x32_bf16 v[168:171], a[194:197], v[144:147], v[168:171]\n"
    "ds_read_b64_tr_b16 a[232:233], v72 offset:2048\n"
    "s_waitcnt lgkmcnt(8)\n"
    "v_mfma_f32_16x16x32_bf16 v[168:171], a[198:201], v[148:151], v[168:171]\n"
    "ds_read_b64_tr_b16 a[234:235], v72 offset:4096\n"
    "v_cvt_pk_bf16_f32 v10, v164, v165\n"
    "v_mfma_f32_16x16x32_bf16 v[168:171], a[202:205], v[152:155], v[168:171]\n"
    "ds_read_b64_tr_b16 a[236:237], v72 offset:6144\n"
    "v_cvt_pk_bf16_f32 v11, v166, v167\n"
    "v_mfma_f32_16x16x32_bf16 v[168:171], a[206:209], v[156:159], v[168:171]\n"
    "ds_read_b64_tr_b16 a[238:239], v73 offset:49152\n"
    "buffer_load_dwordx4 v62, s[16:19], s66 offen offset:1024 lds\n"
    "v_mfma_f32_16x16x32_bf16 v[168:171], a[210:213], v[160:163], v[168:171]\n"
    "ds_read_b64_tr_b16 a[240:241], v73 offset:51200\n"
    "v_pk_max_i16 v81, v81, 0\n"
    "v_mfma_f32_16x16x32_bf16 v[172:175], a[190:193], a[166:169], v[172:175]\n"
    "ds_read_b64_tr_b16 a[242:243], v73 offset:53248\n"
    "v_pk_max_i16 v10, v10, 0\n"
    "v_mfma_f32_16x16x32_bf16 v[172:175], a[194:197], a[170:173], v[172:175]\n"
    "ds_read_b64_tr_b16 a[244:245], v73 offset:55296\n"
    "v_pk_max_i16 v11, v11, 0\n"
    "v_mfma_f32_16x16x32_bf16 v[172:175], a[198:201], a[174:177], v[172:175]\n"
    "ds_read_b64_tr_b16 a[246:247], v73 offset:57344\n"
    "v_cvt_pk_bf16_f32 v82, v168, v169\n"
    "v_mfma_f32_16x16x32_bf16 v[172:175], a[202:205], a[178:181], v[172:175]\n"
    "ds_read_b64_tr_b16 a[248:249], v73 offset:59392\n"
    "v_cvt_pk_bf16_f32 v83, v170, v171\n"
    "v_mfma_f32_16x16x32_bf16 v[172:175], a[206:209], a[182:185], v[172:175]\n"
    "ds_read_b64_tr_b16 a[250:251], v73 offset:61440\n"
    "v_pk_max_i16 v82, v82, 0\n"
    "ds_read_b64_tr_b16 a[60:61], v15 offset:6144\n"
    "v_add_u32 v15, s34, v16\n"
    "ds_read_b64_tr_b16 a[62:63], v15 offset:0\n"
    "ds_read_b64_tr_b16 a[64:65], v15 offset:6144\n"
    "v_add_u32 v15, s35, v16\n"
    "ds_read_b64_tr_b16 a[66:67], v15 offset:0\n"
    "ds_read_b64_tr_b16 a[68:69], v15 offset:6144\n"
    "v_mfma_f32_16x16x32_bf16 v[172:175], a[210:213], a[186:189], v[172:175]\n"
    "ds_read_b64_tr_b16 a[252:253], v73 offset:63488\n"
    "v_pk_max_i16 v83, v83, 0\n"
    "v_mfma_f32_16x16x32_bf16 v[176:179], a[214:217], v[140:143], v[176:179]\n"
    "ds_read_b64_tr_b16 a[190:191], v74 offset:0\n"
    "s_waitcnt lgkmcnt(8)\n"
    "v_mfma_f32_16x16x32_bf16 v[176:179], a[218:221], v[144:147], v[176:179]\n"
    "ds_read_b64_tr_b16 a[192:193], v74 offset:2048\n"
    "s_add_u32 m0, s87, 16384\n"
    "v_mfma_f32_16x16x32_bf16 v[176:179], a[222:225], v[148:151], v[176:179]\n"
    "ds_read_b64_tr_b16 a[194:195], v74 offset:4096\n"
    "s_waitcnt vmcnt(14) lgkmcnt(4)\n"
    "v_mfma_f32_16x16x32_bf16 v[176:179], a[226:229], v[152:155], v[176:179]\n"
    "s_barrier\n"
    "ds_read_b64_tr_b16 a[196:197], v74 offset:6144\n"
    "v_mfma_f32_16x16x32_bf16 v[176:179], a[230:233], v[156:159], v[176:179]\n"
    "ds_read_b64_tr_b16 a[198:199], v68 offset:8192\n"
    "buffer_load_dwordx4 v61, s[16:19], s67 offen offset:0 lds\n"
    "v_mfma_f32_16x16x32_bf16 v[176:179], a[234:237], v[160:163], v[176:179]\n"
    "ds_read_b64_tr_b16 a[200:201], v68 offset:10240\n"
    "v_add_u32 v61, 128, v61\n"
    "v_mfma_f32_16x16x32_bf16 v[180:183], a[214:217], a[166:169], v[180:183]\n"
    "ds_read_b64_tr_b16 a[202:203], v68 offset:12288\n"
    "v_cvt_pk_bf16_f32 v12, v172, v173\n"
    "v_mfma_f32_16x16x32_bf16 v[180:183], a[218:221], a[170:173], v[180:183]\n"
    "buffer_store_dwordx4 v[76:79], v28, s[28:31], s58 offen offset:3072 sc1\n"
    "ds_read_b64_tr_b16 a[204:205], v68 offset:14336\n"
    "v_mfma_f32_16x16x32_bf16 v[180:183], a[222:225], a[174:177], v[180:183]\n"
    "ds_read_b64_tr_b16 a[206:207], v68 offset:16384\n"
    "v_cvt_pk_bf16_f32 v13, v174, v175\n"
    "v_mfma_f32_16x16x32_bf16 v[180:183], a[226:229], a[178:181], v[180:183]\n"
    "ds_read_b64_tr_b16 a[208:209], v68 offset:18432\n"
    "v_cvt_pk_bf16_f32 v20, v176, v177\n"
    "v_mfma_f32_16x16x32_bf16 v[180:183], a[230:233], a[182:185], v[180:183]\n"
    "ds_read_b64_tr_b16 a[210:211], v68 offset:20480\n"
    "v_cvt_pk_bf16_f32 v21, v178, v179\n"
    "v_mfma_f32_16x16x32_bf16 v[180:183], a[234:237], a[186:189], v[180:183]\n"
    "ds_read_b64_tr_b16 a[212:213], v68 offset:22528\n"
    "buffer_load_dwordx4 v62, s[16:19], s67 offen offset:1024 lds\n"
    "v_mfma_f32_16x16x32_bf16 v[184:187], a[238:241], v[140:143], v[184:187]\n"
    "ds_read_b64_tr_b16 a[214:215], v68 offset:24576\n"
    "v_add_u32 v62, 128, v62\n"
    "v_mfma_f32_16x16x32_bf16 v[184:187], a[242:245], v[144:147], v[184:187]\n"
    "ds_read_b64_tr_b16 a[216:217], v68 offset:26624\n"
    "v_pk_max_i16 v12, v12, 0\n"
    "v_mfma_f32_16x16x32_bf16 v[184:187], a[246:249], v[148:151], v[184:187]\n"
    "ds_read_b64_tr_b16 a[218:219], v68 offset:28672\n"
    "s_waitcnt lgkmcnt(7)\n"
    "v_mfma_f32_16x16x32_bf16 v[184:187], a[250:253], v[152:155], v[184:187]\n"
    "ds_read_b64_tr_b16 a[220:221], v68 offset:30720\n"
    "v_cvt_pk_bf16_f32 v192, v180, v181\n"
    "v_mfma_f32_16x16x32_bf16 v[184:187], a[190:193], v[156:159], v[184:187]\n"
    "ds_read_b64_tr_b16 a[222:223], v70 offset:8192\n"
    "s_add_u32 m0, s87, 24576\n"
    "v_mfma_f32_16x16x32_bf16 v[184:187], a[194:197], v[160:163], v[184:187]\n"
    "ds_read_b64_tr_b16 a[224:225], v70 offset:10240\n"
    "buffer_load_dwordx4 v61, s[16:19], 0 offen offset:0 lds\n"
    ".p2align 12\n"
    "L_pg5_%=:\n"
    "s_cbranch_execz L_tramp_%=\n"
    "v_mfma_f32_16x16x32_bf16 v[30:33], a[238:241], a[166:169], v[30:33]\n"
    "ds_read_b64_tr_b16 a[226:227], v70 offset:12288\n"
    "buffer_load_dword v43, v55, s[28:31], 0 offen offset:0 sc1\n"
    "v_mfma_f32_16x16x32_bf16 v[30:33], a[242:245], a[170:173], v[30:33]\n"
    "ds_read_b64_tr_b16 a[228:229], v70 offset:14336\n"
    "v_cvt_pk_bf16_f32 v193, v182, v183\n"
    "v_mfma_f32_16x16x32_bf16 v[30:33], a[246:249], a[174:177], v[30:33]\n"
    "ds_read_b64_tr_b16 a[230:231], v70 offset:16384\n"
    "v_cvt_pk_bf16_f32 v22, v184, v185\n"
    "v_mfma_f32_16x16x32_bf16 v[30:33], a[250:253], a[178:181], v[30:33]\n"
    "ds_read_b64_tr_b16 a[232:233], v70 offset:18432\n"
    "v_cvt_pk_bf16_f32 v23, v186, v187\n"
    "v_mfma_f32_16x16x32_bf16 v[30:33], a[190:193], a[182:185], v[30:33]\n"
    "ds_read_b64_tr_b16 a[234:235], v70 offset:20480\n"
    "v_pk_max_i16 v13, v13, 0\n"
    "v_mfma_f32_16x16x32_bf16 v[30:33], a[194:197], a[186:189], v[30:33]\n"
    "ds_read_b64_tr_b16 a[236:237], v70 offset:22528\n"
    "v_pk_max_i16 v20, v20, 0\n"
    "v_mfma_f32_16x16x32_bf16 v[188:191], a[198:201], v[92:95], 0\n"
    "ds_read_b64_tr_b16 a[238:239], v70 offset:24576\n"
    "buffer_load_dwordx4 v62, s[16:19], 0 offen offset:1024 lds\n"
    "v_mfma_f32_16x16x32_bf16 v[188:191], a[202:205], v[96:99], v[188:191]\n"
    "ds_read_b64_tr_b16 a[240:241], v70 offset:26624\n"
    "s_waitcnt lgkmcnt(7)\n"
    "v_mfma_f32_16x16x32_bf16 v[188:191], a[206:209], v[100:103], v[188:191]\n"
    "ds_read_b64_tr_b16 a[242:243], v70 offset:28672\n"
    "v_cvt_pk_bf16_f32 v194, v30, v31\n"
    "v_mfma_f32_16x16x32_bf16 v[188:191], a[210:213], v[104:107], v[188:191]\n"
    "ds_read_b64_tr_b16 a[244:245], v70 offset:30720\n"
    "v_cvt_pk_bf16_f32 v195, v32, v33\n"
    "v_mfma_f32_16x16x32_bf16 v[188:191], a[214:217], v[108:111], v[188:191]\n"
    "ds_read_b64_tr_b16 a[246:247], v72 offset:8192\n"
    "v_pk_max_i16 v21, v21, 0\n"
    "v_mfma_f32_16x16x32_bf16 v[188:191], a[218:221], v[112:115], v[188:191]\n"
    "ds_read_b64_tr_b16 a[248:249], v72 offset:10240\n"
    "v_pk_max_i16 v22, v22, 0\n"
    "v_mfma_f32_16x16x32_bf16 v[164:167], a[198:201], a[118:121], 0\n"
    "ds_read_b64_tr_b16 a[250:251], v72 offset:12288\n"
    "v_pk_max_i16 v23, v23, 0\n"
    "v_mfma_f32_16x16x32_bf16 v[164:167], a[202:205], a[122:125], v[164:167]\n"
    "ds_read_b64_tr_b16 a[252:253], v72 offset:14336\n"
    "v_pk_max_i16 v192, v192, 0\n"
    "v_mfma_f32_16x16x32_bf16 v[164:167], a[206:209], a[126:129], v[164:167]\n"
    "ds_read_b64_tr_b16 a[190:191], v72 offset:16384\n"
    "s_add_u32 m0, s87, 32768\n"
    "v_mfma_f32_16x16x32_bf16 v[164:167], a[210:213], a[130:133], v[164:167]\n"
    "ds_read_b64_tr_b16 a[192:193], v72 offset:18432\n"
    "buffer_load_dwordx4 v61, s[16:19], s60 offen offset:0 lds\n"
    "v_mfma_f32_16x16x32_bf16 v[164:167], a[214:217], a[134:137], v[164:167]\n"
    "ds_read_b64_tr_b16 a[194:195], v72 offset:20480\n"
    "v_pk_max_i16 v193, v193, 0\n"
    "v_mfma_f32_16x16x32_bf16 v[164:167], a[218:221], a[138:141], v[164:167]\n"
    "ds_read_b64_tr_b16 a[196:197], v72 offset:22528\n"
    "v_pk_max_i16 v194, v194, 0\n"
    "v_mfma_f32_16x16x32_bf16 v[168:171], a[222:225], v[92:95], 0\n"
    "buffer_store_dwordx4 v[80:83], v28, s[28:31], s98 offen offset:0 sc1\n"
    "ds_read_b64_tr_b16 a[198:199], v72 offset:24576\n"
    "s_waitcnt lgkmcnt(6)\n"
    "v_mfma_f32_16x16x32_bf16 v[168:171], a[226:229], v[96:99], v[168:171]\n"
    "ds_read_b64_tr_b16 a[200:201], v72 offset:26624\n"
    "v_pk_max_i16 v195, v195, 0\n"
    "v_mfma_f32_16x16x32_bf16 v[168:171], a[230:233], v[100:103], v[168:171]\n"
    "ds_read_b64_tr_b16 a[202:203], v72 offset:28672\n"
    "v_accvgpr_read_b32 v42, a25\n"
    "v_mfma_f32_16x16x32_bf16 v[168:171], a[234:237], v[104:107], v[168:171]\n"
    "ds_read_b64_tr_b16 a[204:205], v72 offset:30720\n"
    "v_xor_b32 v87, 32, v42\n"
    "v_mfma_f32_16x16x32_bf16 v[168:171], a[238:241], v[108:111], v[168:171]\n"
    "ds_read_b64_tr_b16 a[206:207], v74 offset:8192\n"
    "buffer_load_dwordx4 v62, s[16:19], s60 offen offset:1024 lds\n"
    "v_mfma_f32_16x16x32_bf16 v[168:171], a[242:245], v[112:115], v[168:171]\n"
    "ds_read_b64_tr_b16 a[208:209], v74 offset:10240\n"
    "v_xor_b32 v89, 64, v42\n"
    "v_mfma_f32_16x16x32_bf16 v[172:175], a[222:225], a[118:121], 0\n"
    "ds_read_b64_tr_b16 a[210:211], v74 offset:12288\n"
    "v_xor_b32 v90, 96, v42\n"
    "v_mfma_f32_16x16x32_bf16 v[172:175], a[226:229], a[122:125], v[172:175]\n"
    "ds_read_b64_tr_b16 a[212:213], v74 offset:14336\n"
    "v_mfma_f32_16x16x32_bf16 v[172:175], a[230:233], a[126:129], v[172:175]\n"
    "ds_read_b64_tr_b16 a[214:215], v74 offset:16384\n"
    "v_mfma_f32_16x16x32_bf16 v[172:175], a[234:237], a[130:133], v[172:175]\n"
    "ds_read_b64_tr_b16 a[216:217], v74 offset:18432\n"
    "v_mfma_f32_16x16x32_bf16 v[172:175], a[238:241], a[134:137], v[172:175]\n"
    "ds_read_b64_tr_b16 a[218:219], v74 offset:20480\n"
    "v_mfma_f32_16x16x32_bf16 v[172:175], a[242:245], a[138:141], v[172:175]\n"
    "ds_read_b64_tr_b16 a[220:221], v74 offset:22528\n"
    "v_mfma_f32_16x16x32_bf16 v[176:179], a[246:249], v[92:95], 0\n"
    "ds_read_b64_tr_b16 a[222:223], v74 offset:24576\n"
    "s_waitcnt lgkmcnt(7)\n"
    "v_mfma_f32_16x16x32_bf16 v[176:179], a[250:253], v[96:99], v[176:179]\n"
    "ds_read_b64_tr_b16 a[224:225], v74 offset:26624\n"
    "s_add_u32 m0, s87, 40960\n"
    "v_mfma_f32_16x16x32_bf16 v[176:179], a[190:193], v[100:103], v[176:179]\n"
    "ds_read_b64_tr_b16 a[226:227], v74 offset:28672\n"
    "s_waitcnt vmcnt(14) lgkmcnt(7)\n"
    "v_mfma_f32_16x16x32_bf16 v[176:179], a[194:197], v[104:107], v[176:179]\n"
    "s_barrier\n"
    "ds_read_b64_tr_b16 a[228:229], v74 offset:30720\n"
    "v_mfma_f32_16x16x32_bf16 v[176:179], a[198:201], v[108:111], v[176:179]\n"
    "ds_read_b64_tr_b16 a[230:231], v68 offset:32768\n"
    "buffer_load_dwordx4 v61, s[16:19], s61 offen offset:0 lds\n"
    "v_mfma_f32_16x16x32_bf16 v[176:179], a[202:205], v[112:115], v[176:179]\n"
    "ds_read_b64_tr_b16 a[232:233], v68 offset:34816\n"
    "v_mfma_f32_16x16x32_bf16 v[180:183], a[246:249], a[118:121], 0\n"
    "ds_read_b64_tr_b16 a[234:235], v68 offset:36864\n"
    "v_mfma_f32_16x16x32_bf16 v[180:183], a[250:253], a[122:125], v[180:183]\n"
    "ds_read_b64_tr_b16 a[236:237], v68 offset:38912\n"
    "v_mfma_f32_16x16x32_bf16 v[180:183], a[190:193], a[126:129], v[180:183]\n"
    "ds_read_b64_tr_b16 a[238:239], v68 offset:40960\n"
    "buffer_load_dwordx4 v62, s[16:19], s61 offen offset:1024 lds\n"
    "v_mfma_f32_16x16x32_bf16 v[180:183], a[194:197], a[130:133], v[180:183]\n"
    "ds_read_b64_tr_b16 a[240:241], v68 offset:43008\n"
    "v_mfma_f32_16x16x32_bf16 v[180:183], a[198:201], a[134:137], v[180:183]\n"
    "ds_read_b64_tr_b16 a[242:243], v68 offset:45056\n"
    "v_mfma_f32_16x16x32_bf16 v[180:183], a[202:205], a[138:141], v[180:183]\n"
    "ds_read_b64_tr_b16 a[244:245], v68 offset:47104\n"
    "v_mfma_f32_16x16x32_bf16 v[184:187], a[206:209], v[92:95], 0\n"
    "ds_read_b64_tr_b16 a[246:247], v68 offset:49152\n"
    "v_mfma_f32_16x16x32_bf16 v[184:187], a[210:213], v[96:99], v[184:187]\n"
    "ds_read_b64_tr_b16 a[248:249], v68 offset:51200\n"
    "s_waitcnt lgkmcnt(7)\n"
    "v_mfma_f32_16x16x32_bf16 v[184:187], a[214:217], v[100:103], v[184:187]\n"
    "buffer_store_dwordx4 v[10:13], v28, s[28:31], s98 offen offset:1024 sc1\n"
    "ds_read_b64_tr_b16 a[250:251], v68 offset:53248\n"
    "v_mfma_f32_16x16x32_bf16 v[184:187], a[218:221], v[104:107], v[184:187]\n"
    "ds_read_b64_tr_b16 a[252:253], v68 offset:55296\n"
    "v_mfma_f32_16x16x32_bf16 v[184:187], a[222:225], v[108:111], v[184:187]\n"
    "ds_read_b64_tr_b16 a[190:191], v70 offset:32768\n"
    "s_add_u32 m0, s87, 49152\n"
    "v_mfma_f32_16x16x32_bf16 v[184:187], a[226:229], v[112:115], v[184:187]\n"
    "ds_read_b64_tr_b16 a[192:193], v70 offset:34816\n"
    "v_mfma_f32_16x16x32_bf16 v[30:33], a[206:209], a[118:121], 0\n"
    "ds_read_b64_tr_b16 a[194:195], v70 offset:36864\n"
    "buffer_load_dwordx4 v61, s[16:19], s62 offen offset:0 lds\n"
    "v_mfma_f32_16x16x32_bf16 v[30:33], a[210:213], a[122:125], v[30:33]\n"
    "ds_read_b64_tr_b16 a[196:197], v70 offset:38912\n"
    "v_mfma_f32_16x16x32_bf16 v[30:33], a[214:217], a[126:129], v[30:33]\n"
    "ds_read_b64_tr_b16 a[198:199], v70 offset:40960\n"
    "v_mfma_f32_16x16x32_bf16 v[30:33], a[218:221], a[130:133], v[30:33]\n"
    "ds_read_b64_tr_b16 a[200:201], v70 offset:43008\n"
    "v_mfma_f32_16x16x32_bf16 v[30:33], a[222:225], a[134:137], v[30:33]\n"
    "ds_read_b64_tr_b16 a[202:203], v70 offset:45056\n"
    "v_mfma_f32_16x16x32_bf16 v[30:33], a[226:229], a[138:141], v[30:33]\n"
    "ds_read_b64_tr_b16 a[204:205], v70 offset:47104\n"
    "v_mfma_f32_16x16x32_bf16 v[188:191], a[230:233], v[116:119], v[188:191]\n"
    "ds_read_b64_tr_b16 a[206:207], v70 offset:49152\n"
    "s_waitcnt lgkmcnt(7)\n"
    "v_mfma_f32_16x16x32_bf16 v[188:191], a[234:237], v[120:123], v[188:191]\n"
    "ds_read_b64_tr_b16 a[208:209], v70 offset:51200\n"
    "buffer_load_dwordx4 v62, s[16:19], s62 offen offset:1024 lds\n"
    "v_mfma_f32_16x16x32_bf16 v[188:191], a[238:241], v[124:127], v[188:191]\n"
    "ds_read_b64_tr_b16 a[210:211], v70 offset:53248\n"
    "v_mfma_f32_16x16x32_bf16 v[188:191], a[242:245], v[128:131], v[188:191]\n"
    "ds_read_b64_tr_b16 a[212:213], v70 offset:55296\n"
    "v_mfma_f32_16x16x32_bf16 v[188:191], a[246:249], v[132:135], v[188:191]\n"
    "ds_read_b64_tr_b16 a[214:215], v72 offset:32768\n"
    "v_mfma_f32_16x16x32_bf16 v[188:191], a[250:253], v[136:139], v[188:191]\n"
    "ds_read_b64_tr_b16 a[216:217], v72 offset:34816\n"
    "v_mfma_f32_16x16x32_bf16 v[164:167], a[230:233], a[142:145], v[164:167]\n"
    "ds_read_b64_tr_b16 a[218:219], v72 offset:36864\n"
    "s_waitcnt vmcnt(9) lgkmcnt(7)\n"
    "v_cmp_eq_u32 vcc, 0, v43\n"
    "v_mfma_f32_16x16x32_bf16 v[164:167], a[234:237], a[146:149], v[164:167]\n"
    "ds_read_b64_tr_b16 a[220:221], v72 offset:38912\n"
    "v_mfma_f32_16x16x32_bf16 v[164:167], a[238:241], a[150:153], v[164:167]\n"
    "ds_read_b64_tr_b16 a[222:223], v72 offset:40960\n"
    "s_add_u32 m0, s87, 57344\n"
    "v_mfma_f32_16x16x32_bf16 v[164:167], a[242:245], a[154:157], v[164:167]\n"
    "ds_read_b64_tr_b16 a[224:225], v72 offset:43008\n"
    "buffer_load_dwordx4 v61, s[16:19], s63 offen offset:0 lds\n"
    "s_cbranch_vccnz L_kv_%=\n"
    "L_kv_ret_%=:\n"
    "v_mfma_f32_16x16x32_bf16 v[164:167], a[246:249], a[158:161], v[164:167]\n"
    "ds_read_b64_tr_b16 a[226:227], v72 offset:45056\n"
    "s_add_u32 s80, s80, 20480\n"
    "v_mfma_f32_16x16x32_bf16 v[164:167], a[250:253], a[162:165], v[164:167]\n"
    "ds_read_b64_tr_b16 a[228:229], v72 offset:47104\n"
    "v_mfma_f32_16x16x32_bf16 v[168:171], a[190:193], v[116:119], v[168:171]\n"
    "ds_read_b64_tr_b16 a[230:231], v72 offset:49152\n"
    "v_mfma_f32_16x16x32_bf16 v[168:171], a[194:197], v[120:123], v[168:171]\n"
    "ds_read_b64_tr_b16 a[232:233], v72 offset:51200\n"
    "v_mfma_f32_16x16x32_bf16 v[168:171], a[198:201], v[124:127], v[168:171]\n"
    "ds_read_b64_tr_b16 a[234:235], v72 offset:53248\n"
    "v_mfma_f32_16x16x32_bf16 v[168:171], a[202:205], v[128:131], v[168:171]\n"
    "ds_read_b64_tr_b16 a[236:237], v72 offset:55296\n"
    "s_waitcnt lgkmcnt(7)\n"
    "v_mfma_f32_16x16x32_bf16 v[168:171], a[206:209], v[132:135], v[168:171]\n"
    "ds_read_b64_tr_b16 a[238:239], v74 offset:32768\n"
    "buffer_load_dwordx4 v62, s[16:19], s63 offen offset:1024 lds\n"
    "v_mfma_f32_16x16x32_bf16 v[168:171], a[210:213], v[136:139], v[168:171]\n"
    "ds_read_b64_tr_b16 a[240:241], v74 offset:34816\n"
    "s_add_u32 m0, s96, 20480\n"
    "v_mfma_f32_16x16x32_bf16 v[172:175], a[190:193], a[142:145], v[172:175]\n"
    "ds_read_b64_tr_b16 a[242:243], v74 offset:36864\n"
    "v_mfma_f32_16x16x32_bf16 v[172:175], a[194:197], a[146:149], v[172:175]\n"
    "ds_read_b64_tr_b16 a[244:245], v74 offset:38912\n"
    "buffer_load_dwordx4 v28, s[28:31], s57 offen offset:0 sc1 lds\n"
    "v_mfma_f32_16x16x32_bf16 v[172:175], a[198:201], a[150:153], v[172:175]\n"
    "ds_read_b64_tr_b16 a[246:247], v74 offset:40960\n"
    "v_mfma_f32_16x16x32_bf16 v[172:175], a[202:205], a[154:157], v[172:175]\n"
    "ds_read_b64_tr_b16 a[248:249], v74 offset:43008\n"
    "v_mfma_f32_16x16x32_bf16 v[172:175], a[206:209], a[158:161], v[172:175]\n"
    "ds_read_b64_tr_b16 a[250:251], v74 offset:45056\n"
    "v_mfma_f32_16x16x32_bf16 v[172:175], a[210:213], a[162:165], v[172:175]\n"
    "ds_read_b64_tr_b16 a[252:253], v74 offset:47104\n"
    "v_mfma_f32_16x16x32_bf16 v[176:179], a[214:217], v[116:119], v[176:179]\n"
    "ds_read_b64_tr_b16 a[190:191], v74 offset:49152\n"
    "s_add_u32 m0, s87, 65536\n"
    "v_mfma_f32_16x16x32_bf16 v[176:179], a[218:221], v[120:123], v[176:179]\n"
    "ds_read_b64_tr_b16 a[192:193], v74 offset:51200\n"
    "s_waitcnt lgkmcnt(7)\n"
    "v_mfma_f32_16x16x32_bf16 v[176:179], a[222:225], v[124:127], v[176:179]\n"
    "s_barrier\n"
    "ds_read_b64_tr_b16 a[194:195], v74 offset:53248\n"
    "v_mfma_f32_16x16x32_bf16 v[176:179], a[226:229], v[128:131], v[176:179]\n"
    "ds_read_b64_tr_b16 a[196:197], v74 offset:55296\n"
    "buffer_load_dwordx4 v61, s[16:19], s64 offen offset:0 lds\n"
    "v_mfma_f32_16x16x32_bf16 v[176:179], a[230:233], v[132:135], v[176:179]\n"
    "ds_read_b64_tr_b16 a[198:199], v67 offset:0\n"
    "v_mfma_f32_16x16x32_bf16 v[176:179], a[234:237], v[136:139], v[176:179]\n"
    "ds_read_b64_tr_b16 a[200:201], v67 offset:2048\n"
    "v_mfma_f32_16x16x32_bf16 v[180:183], a[214:217], a[142:145], v[180:183]\n"
    "ds_read_b64_tr_b16 a[202:203], v67 offset:4096\n"
    "v_mfma_f32_16x16x32_bf16 v[180:183], a[218:221], a[146:149], v[180:183]\n"
    "ds_read_b64_tr_b16 a[204:205], v67 offset:6144\n"
    "v_mfma_f32_16x16x32_bf16 v[180:183], a[222:225], a[150:153], v[180:183]\n"
    "ds_read_b64_tr_b16 a[206:207], v67 offset:8192\n"
    "buffer_load_dwordx4 v62, s[16:19], s64 offen offset:1024 lds\n"
    "v_mfma_f32_16x16x32_bf16 v[180:183], a[226:229], a[154:157], v[180:183]\n"
    "ds_read_b64_tr_b16 a[208:209], v67 offset:10240\n"
    "s_add_u32 m0, s96, 20480\n"
    "v_mfma_f32_16x16x32_bf16 v[180:183], a[230:233], a[158:161], v[180:183]\n"
    "ds_read_b64_tr_b16 a[210:211], v67 offset:12288\n"
    "v_mfma_f32_16x16x32_bf16 v[180:183], a[234:237], a[162:165], v[180:183]\n"
    "ds_read_b64_tr_b16 a[212:213], v67 offset:14336\n"
    "buffer_load_dwordx4 v28, s[28:31], s57 offen offset:1024 sc1 lds\n"
    "v_mfma_f32_16x16x32_bf16 v[184:187], a[238:241], v[116:119], v[184:187]\n"
    "ds_read_b64_tr_b16 a[214:215], v67 offset:16384\n"
    "s_waitcnt lgkmcnt(7)\n"
    "v_mfma_f32_16x16x32_bf16 v[184:187], a[242:245], v[120:123], v[184:187]\n"
    "ds_read_b64_tr_b16 a[216:217], v67 offset:18432\n"
    "v_mfma_f32_16x16x32_bf16 v[184:187], a[246:249], v[124:127], v[184:187]\n"
    "ds_read_b64_tr_b16 a[218:219], v67 offset:20480\n"
    "v_mfma_f32_16x16x32_bf16 v[184:187], a[250:253], v[128:131], v[184:187]\n"
    "ds_read_b64_tr_b16 a[220:221], v67 offset:22528\n"
    "v_mfma_f32_16x16x32_bf16 v[184:187], a[190:193], v[132:135], v[184:187]\n"
    "ds_read_b64_tr_b16 a[222:223], v69 offset:0\n"
    "s_add_u32 m0, s87, 73728\n"
    "v_mfma_f32_16x16x32_bf16 v[184:187], a[194:197], v[136:139], v[184:187]\n"
    "ds_read_b64_tr_b16 a[224:225], v69 offset:2048\n"
    "buffer_load_dwordx4 v61, s[16:19], s65 offen offset:0 lds\n"
    "v_mfma_f32_16x16x32_bf16 v[30:33], a[238:241], a[142:145], v[30:33]\n"
    "ds_read_b64_tr_b16 a[226:227], v69 offset:4096\n"
    "v_mfma_f32_16x16x32_bf16 v[30:33], a[242:245], a[146:149], v[30:33]\n"
    "ds_read_b64_tr_b16 a[228:229], v69 offset:6144\n"
    "v_mfma_f32_16x16x32_bf16 v[30:33], a[246:249], a[150:153], v[30:33]\n"
    "ds_read_b64_tr_b16 a[230:231], v69 offset:8192\n"
    "v_mfma_f32_16x16x32_bf16 v[30:33], a[250:253], a[154:157], v[30:33]\n"
    "ds_read_b64_tr_b16 a[232:233], v69 offset:10240\n"
    "v_mfma_f32_16x16x32_bf16 v[30:33], a[190:193], a[158:161], v[30:33]\n"
    "ds_read_b64_tr_b16 a[234:235], v69 offset:12288\n"
    "v_mfma_f32_16x16x32_bf16 v[30:33], a[194:197], a[162:165], v[30:33]\n"
    "ds_read_b64_tr_b16 a[236:237], v69 offset:14336\n"
    "v_mfma_f32_16x16x32_bf16 v[188:191], a[198:201], v[140:143], v[188:191]\n"
    "ds_read_b64_tr_b16 a[238:239], v69 offset:16384\n"
    "s_waitcnt lgkmcnt(7)\n"
    "v_mfma_f32_16x16x32_bf16 v[188:191], a[202:205], v[144:147], v[188:191]\n"
    "ds_read_b64_tr_b16 a[240:241], v69 offset:18432\n"
    "buffer_load_dwordx4 v62, s[16:19], s65 offen offset:1024 lds\n"
    "v_mfma_f32_16x16x32_bf16 v[188:191], a[206:209], v[148:151], v[188:191]\n"
    "ds_read_b64_tr_b16 a[242:243], v69 offset:20480\n"
    "s_add_u32 m0, s96, 20480\n"
    "v_mfma_f32_16x16x32_bf16 v[188:191], a[210:213], v[152:155], v[188:191]\n"
    "ds_read_b64_tr_b16 a[244:245], v69 offset:22528\n"
    "v_mfma_f32_16x16x32_bf16 v[188:191], a[214:217], v[156:159], v[188:191]\n"
    "ds_read_b64_tr_b16 a[246:247], v71 offset:0\n"
    "buffer_load_dwordx4 v28, s[28:31], s57 offen offset:2048 sc1 lds\n"
    "v_mfma_f32_16x16x32_bf16 v[188:191], a[218:221], v[160:163], v[188:191]\n"
    "ds_read_b64_tr_b16 a[248:249], v71 offset:2048\n"
    "v_mfma_f32_16x16x32_bf16 v[164:167], a[198:201], a[166:169], v[164:167]\n"
    "ds_read_b64_tr_b16 a[250:251], v71 offset:4096\n"
    "v_mfma_f32_16x16x32_bf16 v[164:167], a[202:205], a[170:173], v[164:167]\n"
    "ds_read_b64_tr_b16 a[252:253], v71 offset:6144\n"
    "s_waitcnt vmcnt(11) lgkmcnt(7)\n"
    "v_mfma_f32_16x16x32_bf16 v[164:167], a[206:209], a[174:177], v[164:167]\n"
    "ds_read_b64_tr_b16 a[190:191], v71 offset:8192\n"
    "buffer_store_dword a42, v19, s[28:31], s90 offen offset:128 sc1\n"
    "v_mfma_f32_16x16x32_bf16 v[164:167], a[210:213], a[178:181], v[164:167]\n"
    "ds_read_b64_tr_b16 a[192:193], v71 offset:10240\n"
    "s_add_u32 m0, s87, 81920\n"
    "v_mfma_f32_16x16x32_bf16 v[164:167], a[214:217], a[182:185], v[164:167]\n"
    "ds_read_b64_tr_b16 a[194:195], v71 offset:12288\n"
    "buffer_load_dwordx4 v61, s[16:19], s66 offen offset:0 lds\n"
    "v_mfma_f32_16x16x32_bf16 v[164:167], a[218:221], a[186:189], v[164:167]\n"
    "ds_read_b64_tr_b16 a[196:197], v71 offset:14336\n"
    "v_cvt_pk_bf16_f32 v196, v188, v189\n"
    "v_mfma_f32_16x16x32_bf16 v[168:171], a[222:225], v[140:143], v[168:171]\n"
    "ds_read_b64_tr_b16 a[198:199], v71 offset:16384\n"
    "v_cvt_pk_bf16_f32 v197, v190, v191\n"
    "v_mfma_f32_16x16x32_bf16 v[168:171], a[226:229], v[144:147], v[168:171]\n"
    "ds_read_b64_tr_b16 a[200:201], v71 offset:18432\n"
    "v_pk_max_i16 v196, v196, 0\n"
    "v_mfma_f32_16x16x32_bf16 v[168:171], a[230:233], v[148:151], v[168:171]\n"
    "ds_read_b64_tr_b16 a[202:203], v71 offset:20480\n"
    "v_cvt_pk_bf16_f32 v200, v164, v165\n"
    "v_mfma_f32_16x16x32_bf16 v[168:171], a[234:237], v[152:155], v[168:171]\n"
    "ds_read_b64_tr_b16 a[204:205], v71 offset:22528\n"
    "s_waitcnt lgkmcnt(7)\n"
    "v_mfma_f32_16x16x32_bf16 v[168:171], a[238:241], v[156:159], v[168:171]\n"
    "ds_read_b64_tr_b16 a[206:207], v73 offset:0\n"
    "buffer_load_dwordx4 v62, s[16:19], s66 offen offset:1024 lds\n"
    "v_mfma_f32_16x16x32_bf16 v[168:171], a[242:245], v[160:163], v[168:171]\n"
    "ds_read_b64_tr_b16 a[208:209], v73 offset:2048\n"
    "s_add_u32 m0, s96, 20480\n"
    "v_mfma_f32_16x16x32_bf16 v[172:175], a[222:225], a[166:169], v[172:175]\n"
    "ds_read_b64_tr_b16 a[210:211], v73 offset:4096\n"
    "v_cvt_pk_bf16_f32 v201, v166, v167\n"
    "v_mfma_f32_16x16x32_bf16 v[172:175], a[226:229], a[170:173], v[172:175]\n"
    "ds_read_b64_tr_b16 a[212:213], v73 offset:6144\n"
    "buffer_load_dwordx4 v28, s[28:31], s57 offen offset:3072 sc1 lds\n"
    "v_mfma_f32_16x16x32_bf16 v[172:175], a[230:233], a[174:177], v[172:175]\n"
    "ds_read_b64_tr_b16 a[214:215], v73 offset:8192\n"
    "v_cvt_pk_bf16_f32 v198, v168, v169\n"
    "v_mfma_f32_16x16x32_bf16 v[172:175], a[234:237], a[178:181], v[172:175]\n"
    "ds_read_b64_tr_b16 a[216:217], v73 offset:10240\n"
    "v_cvt_pk_bf16_f32 v199, v170, v171\n"
    "v_mfma_f32_16x16x32_bf16 v[172:175], a[238:241], a[182:185], v[172:175]\n"
    "ds_read_b64_tr_b16 a[218:219], v73 offset:12288\n"
    "v_pk_max_i16 v197, v197, 0\n"
    "v_mfma_f32_16x16x32_bf16 v[172:175], a[242:245], a[186:189], v[172:175]\n"
    "ds_read_b64_tr_b16 a[220:221], v73 offset:14336\n"
    "v_pk_max_i16 v198, v198, 0\n"
    "v_mfma_f32_16x16x32_bf16 v[176:179], a[246:249], v[140:143], v[176:179]\n"
    "ds_read_b64_tr_b16 a[222:223], v73 offset:16384\n"
    "s_add_u32 m0, s87, 90112\n"
    "v_mfma_f32_16x16x32_bf16 v[176:179], a[250:253], v[144:147], v[176:179]\n"
    "ds_read_b64_tr_b16 a[224:225], v73 offset:18432\n"
    "s_waitcnt lgkmcnt(7)\n"
    "v_mfma_f32_16x16x32_bf16 v[176:179], a[190:193], v[148:151], v[176:179]\n"
    "s_barrier\n"
    "ds_read_b64_tr_b16 a[226:227], v73 offset:20480\n"
    "v_mfma_f32_16x16x32_bf16 v[176:179], a[194:197], v[152:155], v[176:179]\n"
    "ds_read_b64_tr_b16 a[228:229], v73 offset:22528\n"
    "buffer_load_dwordx4 v61, s[16:19], s67 offen offset:0 lds\n"
    "v_mfma_f32_16x16x32_bf16 v[176:179], a[198:201], v[156:159], v[176:179]\n"
    "ds_read_b64_tr_b16 a[230:231], v67 offset:24576\n"
    "v_cvt_pk_bf16_f32 v202, v172, v173\n"
    "v_mfma_f32_16x16x32_bf16 v[176:179], a[202:205], v[160:163], v[176:179]\n"
    "ds_read_b64_tr_b16 a[232:233], v67 offset:26624\n"
    "v_cvt_pk_bf16_f32 v203, v174, v175\n"
    "v_mfma_f32_16x16x32_bf16 v[180:183], a[246:249], a[166:169], v[180:183]\n"
    "ds_read_b64_tr_b16 a[234:235], v67 offset:28672\n"
    "v_pk_max_i16 v199, v199, 0\n"
    "v_mfma_f32_16x16x32_bf16 v[180:183], a[250:253], a[170:173], v[180:183]\n"
    "ds_read_b64_tr_b16 a[236:237], v67 offset:30720\n"
    "v_pk_max_i16 v200, v200, 0\n"
    "v_mfma_f32_16x16x32_bf16 v[180:183], a[190:193], a[174:177], v[180:183]\n"
    "ds_read_b64_tr_b16 a[238:239], v67 offset:32768\n"
    "buffer_load_dwordx4 v62, s[16:19], s67 offen offset:1024 lds\n"
    "v_mfma_f32_16x16x32_bf16 v[180:183], a[194:197], a[178:181], v[180:183]\n"
    "ds_read_b64_tr_b16 a[240:241], v67 offset:34816\n"
    "s_add_u32 m0, s96, 24576\n"
    "v_mfma_f32_16x16x32_bf16 v[180:183], a[198:201], a[182:185], v[180:183]\n"
    "ds_read_b64_tr_b16 a[242:243], v67 offset:36864\n"
    "v_cvt_pk_bf16_f32 v204, v176, v177\n"
    "v_mfma_f32_16x16x32_bf16 v[180:183], a[202:205], a[186:189], v[180:183]\n"
    "ds_read_b64_tr_b16 a[244:245], v67 offset:38912\n"
    "buffer_load_dwordx4 v28, s[28:31], s81 offen offset:0 sc1 lds\n"
    "v_mfma_f32_16x16x32_bf16 v[184:187], a[206:209], v[140:143], v[184:187]\n"
    "ds_read_b64_tr_b16 a[246:247], v67 offset:40960\n"
    "s_waitcnt lgkmcnt(7)\n"
    "v_mfma_f32_16x16x32_bf16 v[184:187], a[210:213], v[144:147], v[184:187]\n"
    "ds_read_b64_tr_b16 a[248:249], v67 offset:43008\n"
    "s_add_u32 m0, s96, 10240\n"
    "v_mfma_f32_16x16x32_bf16 v[184:187], a[214:217], v[148:151], v[184:187]\n"
    "ds_read_b64_tr_b16 a[250:251], v67 offset:45056\n"
    "s_add_u32 s81, s56, 4096\n"
    "v_mfma_f32_16x16x32_bf16 v[184:187], a[218:221], v[152:155], v[184:187]\n"
    "ds_read_b64_tr_b16 a[252:253], v67 offset:47104\n"
    "v_cvt_pk_bf16_f32 v205, v178, v179\n"
    "v_mfma_f32_16x16x32_bf16 v[184:187], a[222:225], v[156:159], v[184:187]\n"
    "ds_read_b64_tr_b16 a[190:191], v69 offset:24576\n"
    "buffer_load_dwordx4 v28, s[28:31], s56 offen offset:0 sc1 lds\n"
    "v_mfma_f32_16x16x32_bf16 v[184:187], a[226:229], v[160:163], v[184:187]\n"
    "ds_read_b64_tr_b16 a[192:193], v69 offset:26624\n"
    "v_cvt_pk_bf16_f32 v208, v180, v181\n"
    "v_mfma_f32_16x16x32_bf16 v[30:33], a[206:209], a[166:169], v[30:33]\n"
    "ds_read_b64_tr_b16 a[194:195], v69 offset:28672\n"
    "v_cvt_pk_bf16_f32 v209, v182, v183\n"
    "v_mfma_f32_16x16x32_bf16 v[30:33], a[210:213], a[170:173], v[30:33]\n"
    "ds_read_b64_tr_b16 a[196:197], v69 offset:30720\n"
    "v_pk_max_i16 v201, v201, 0\n"
    "v_mfma_f32_16x16x32_bf16 v[30:33], a[214:217], a[174:177], v[30:33]\n"
    "ds_read_b64_tr_b16 a[198:199], v69 offset:32768\n"
    "v_cvt_pk_bf16_f32 v206, v184, v185\n"
    "v_mfma_f32_16x16x32_bf16 v[30:33], a[218:221], a[178:181], v[30:33]\n"
    "ds_read_b64_tr_b16 a[200:201], v69 offset:34816\n"
    "buffer_load_dwordx4 v28, s[28:31], s56 offen offset:1024 sc1 lds\n"
    "v_mfma_f32_16x16x32_bf16 v[30:33], a[222:225], a[182:185], v[30:33]\n"
    "ds_read_b64_tr_b16 a[202:203], v69 offset:36864\n"
    "v_cvt_pk_bf16_f32 v207, v186, v187\n"
    "v_mfma_f32_16x16x32_bf16 v[30:33], a[226:229], a[186:189], v[30:33]\n"
    "ds_read_b64_tr_b16 a[204:205], v69 offset:38912\n"
    "v_pk_max_i16 v202, v202, 0\n"
    "v_mfma_f32_16x16x32_bf16 v[188:191], a[230:233], v[92:95], 0\n"
    "ds_read_b64_tr_b16 a[206:207], v69 offset:40960\n"
    "s_waitcnt lgkmcnt(7)\n"
    "v_mfma_f32_16x16x32_bf16 v[188:191], a[234:237], v[96:99], v[188:191]\n"
    "ds_read_b64_tr_b16 a[208:209], v69 offset:43008\n"
    "v_pk_max_i16 v203, v203, 0\n"
    "v_mfma_f32_16x16x32_bf16 v[188:191], a[238:241], v[100:103], v[188:191]\n"
    "ds_read_b64_tr_b16 a[210:211], v69 offset:45056\n"
    "buffer_load_dwordx4 v28, s[28:31], s56 offen offset:2048 sc1 lds\n"
    "v_mfma_f32_16x16x32_bf16 v[188:191], a[242:245], v[104:107], v[188:191]\n"
    "ds_read_b64_tr_b16 a[212:213], v69 offset:47104\n"
    "v_cvt_pk_bf16_f32 v210, v30, v31\n"
    "v_mfma_f32_16x16x32_bf16 v[188:191], a[246:249], v[108:111], v[188:191]\n"
    "ds_read_b64_tr_b16 a[214:215], v71 offset:24576\n"
    "v_cvt_pk_bf16_f32 v211, v32, v33\n"
    "v_mfma_f32_16x16x32_bf16 v[188:191], a[250:253], v[112:115], v[188:191]\n"
    "ds_read_b64_tr_b16 a[216:217], v71 offset:26624\n"
    "v_pk_max_i16 v204, v204, 0\n"
    "v_mfma_f32_16x16x32_bf16 v[164:167], a[230:233], a[118:121], 0\n"
    "ds_read_b64_tr_b16 a[218:219], v71 offset:28672\n"
    "v_pk_max_i16 v205, v205, 0\n"
    "v_mfma_f32_16x16x32_bf16 v[164:167], a[234:237], a[122:125], v[164:167]\n"
    "ds_read_b64_tr_b16 a[220:221], v71 offset:30720\n"
    "buffer_load_dwordx4 v28, s[28:31], s56 offen offset:3072 sc1 lds\n"
    "v_mfma_f32_16x16x32_bf16 v[164:167], a[238:241], a[126:129], v[164:167]\n"
    "ds_read_b64_tr_b16 a[222:223], v71 offset:32768\n"
    "s_add_u32 m0, s96, 14336\n"
    "v_mfma_f32_16x16x32_bf16 v[164:167], a[242:245], a[130:133], v[164:167]\n"
    "ds_read_b64_tr_b16 a[224:225], v71 offset:34816\n"
    "v_pk_max_i16 v208, v208, 0\n"
    "v_mfma_f32_16x16x32_bf16 v[164:167], a[246:249], a[134:137], v[164:167]\n"
    "ds_read_b64_tr_b16 a[226:227], v71 offset:36864\n"
    "v_pk_max_i16 v209, v209, 0\n"
    "v_mfma_f32_16x16x32_bf16 v[164:167], a[250:253], a[138:141], v[164:167]\n"
    "ds_read_b64_tr_b16 a[228:229], v71 offset:38912\n"
    "v_pk_max_i16 v206, v206, 0\n"
    "v_mfma_f32_16x16x32_bf16 v[168:171], a[190:193], v[92:95], 0\n"
    "ds_read_b64_tr_b16 a[230:231], v71 offset:40960\n"
    "s_waitcnt lgkmcnt(7)\n"
    "v_mfma_f32_16x16x32_bf16 v[168:171], a[194:197], v[96:99], v[168:171]\n"
    "ds_read_b64_tr_b16 a[232:233], v71 offset:43008\n"
    "buffer_load_dwordx4 v28, s[28:31], s81 offen offset:0 sc1 lds\n"
    "v_mfma_f32_16x16x32_bf16 v[168:171], a[198:201], v[100:103], v[168:171]\n"
    "ds_read_b64_tr_b16 a[234:235], v71 offset:45056\n"
    "s_add_u32 m0, s96, 0\n"
    "v_mfma_f32_16x16x32_bf16 v[168:171], a[202:205], v[104:107], v[168:171]\n"
    "ds_read_b64_tr_b16 a[236:237], v71 offset:47104\n"
    "s_add_u32 s81, s55, 4096\n"
    "v_mfma_f32_16x16x32_bf16 v[168:171], a[206:209], v[108:111], v[168:171]\n"
    "ds_read_b64_tr_b16 a[238:239], v73 offset:24576\n"
    "v_pk_max_i16 v207, v207, 0\n"
    "v_mfma_f32_16x16x32_bf16 v[168:171], a[210:213], v[112:115], v[168:171]\n"
    "ds_read_b64_tr_b16 a[240:241], v73 offset:26624\n"
    "buffer_load_dwordx4 v28, s[28:31], s55 offen offset:0 sc1 lds\n"
    "v_mfma_f32_16x16x32_bf16 v[172:175], a[190:193], a[118:121], 0\n"
    "ds_read_b64_tr_b16 a[242:243], v73 offset:28672\n"
    "v_pk_max_i16 v210, v210, 0\n"
    "v_mfma_f32_16x16x32_bf16 v[172:175], a[194:197], a[122:125], v[172:175]\n"
    "ds_read_b64_tr_b16 a[244:245], v73 offset:30720\n"
    "v_pk_max_i16 v211, v211, 0\n"
    "v_mfma_f32_16x16x32_bf16 v[172:175], a[198:201], a[126:129], v[172:175]\n"
    "ds_read_b64_tr_b16 a[246:247], v73 offset:32768\n"
    "v_mfma_f32_16x16x32_bf16 v[172:175], a[202:205], a[130:133], v[172:175]\n"
    "ds_read_b64_tr_b16 a[248:249], v73 offset:34816\n"
    "v_mfma_f32_16x16x32_bf16 v[172:175], a[206:209], a[134:137], v[172:175]\n"
    "ds_read_b64_tr_b16 a[250:251], v73 offset:36864\n"
    "buffer_load_dwordx4 v28, s[28:31], s55 offen offset:1024 sc1 lds\n"
    "v_mfma_f32_16x16x32_bf16 v[172:175], a[210:213], a[138:141], v[172:175]\n"
    "ds_read_b64_tr_b16 a[252:253], v73 offset:38912\n"
    "v_mfma_f32_16x16x32_bf16 v[176:179], a[214:217], v[92:95], 0\n"
    "ds_read_b64_tr_b16 a[190:191], v73 offset:40960\n"
    "s_waitcnt lgkmcnt(7)\n"
    "v_mfma_f32_16x16x32_bf16 v[176:179], a[218:221], v[96:99], v[176:179]\n"
    "ds_read_b64_tr_b16 a[192:193], v73 offset:43008\n"
    "v_mfma_f32_16x16x32_bf16 v[176:179], a[222:225], v[100:103], v[176:179]\n"
    "ds_read_b64_tr_b16 a[194:195], v73 offset:45056\n"
    "s_waitcnt vmcnt(18) lgkmcnt(7)\n"
    "v_mfma_f32_16x16x32_bf16 v[176:179], a[226:229], v[104:107], v[176:179]\n"
    "s_barrier\n"
    "ds_read_b64_tr_b16 a[196:197], v73 offset:47104\n"
    "v_mfma_f32_16x16x32_bf16 v[176:179], a[230:233], v[108:111], v[176:179]\n"
    "ds_read_b64_tr_b16 a[198:199], v67 offset:49152\n"
    "buffer_load_dwordx4 v28, s[28:31], s55 offen offset:2048 sc1 lds\n"
    "v_mfma_f32_16x16x32_bf16 v[176:179], a[234:237], v[112:115], v[176:179]\n"
    "ds_read_b64_tr_b16 a[200:201], v67 offset:51200\n"
    "v_mfma_f32_16x16x32_bf16 v[180:183], a[214:217], a[118:121], 0\n"
    "ds_read_b64_tr_b16 a[202:203], v67 offset:53248\n"
    "v_mfma_f32_16x16x32_bf16 v[180:183], a[218:221], a[122:125], v[180:183]\n"
    "ds_read_b64_tr_b16 a[204:205], v67 offset:55296\n"
    "v_mfma_f32_16x16x32_bf16 v[180:183], a[222:225], a[126:129], v[180:183]\n"
    "ds_read_b64_tr_b16 a[206:207], v67 offset:57344\n"
    "buffer_load_dwordx4 v28, s[28:31], s55 offen offset:3072 sc1 lds\n"
    "v_mfma_f32_16x16x32_bf16 v[180:183], a[226:229], a[130:133], v[180:183]\n"
    "ds_read_b64_tr_b16 a[208:209], v67 offset:59392\n"
    "s_add_u32 m0, s96, 4096\n"
    "v_mfma_f32_16x16x32_bf16 v[180:183], a[230:233], a[134:137], v[180:183]\n"
    "ds_read_b64_tr_b16 a[210:211], v67 offset:61440\n"
    "v_mfma_f32_16x16x32_bf16 v[180:183], a[234:237], a[138:141], v[180:183]\n"
    "ds_read_b64_tr_b16 a[212:213], v67 offset:63488\n"
    "v_mfma_f32_16x16x32_bf16 v[184:187], a[238:241], v[92:95], 0\n"
    "ds_read_b64_tr_b16 a[214:215], v68 offset:0\n"
    "v_mfma_f32_16x16x32_bf16 v[184:187], a[242:245], v[96:99], v[184:187]\n"
    "ds_read_b64_tr_b16 a[216:217], v68 offset:2048\n"
    "s_waitcnt lgkmcnt(7)\n"
    "v_mfma_f32_16x16x32_bf16 v[184:187], a[246:249], v[100:103], v[184:187]\n"
    "ds_read_b64_tr_b16 a[218:219], v68 offset:4096\n"
    "buffer_load_dwordx4 v28, s[28:31], s81 offen offset:0 sc1 lds\n"
    "v_mfma_f32_16x16x32_bf16 v[184:187], a[250:253], v[104:107], v[184:187]\n"
    "ds_read_b64_tr_b16 a[220:221], v68 offset:6144\n"
    "v_mfma_f32_16x16x32_bf16 v[184:187], a[190:193], v[108:111], v[184:187]\n"
    "ds_read_b64_tr_b16 a[222:223], v69 offset:49152\n"
    "s_add_u32 m0, s86, 0\n"
    "v_mfma_f32_16x16x32_bf16 v[184:187], a[194:197], v[112:115], v[184:187]\n"
    "ds_read_b64_tr_b16 a[224:225], v69 offset:51200\n"
    "buffer_load_dwordx4 v56, s[20:23], s84 offen offset:0 lds\n"
    "v_mfma_f32_16x16x32_bf16 v[30:33], a[238:241], a[118:121], 0\n"
    "ds_read_b64_tr_b16 a[226:227], v69 offset:53248\n"
    "v_mfma_f32_16x16x32_bf16 v[30:33], a[242:245], a[122:125], v[30:33]\n"
    "ds_read_b64_tr_b16 a[228:229], v69 offset:55296\n"
    "v_mfma_f32_16x16x32_bf16 v[30:33], a[246:249], a[126:129], v[30:33]\n"
    "ds_read_b64_tr_b16 a[230:231], v69 offset:57344\n"
    "buffer_load_dwordx4 v[212:215], v18, s[28:31], s80 offen offset:0 sc1\n"
    "v_mfma_f32_16x16x32_bf16 v[30:33], a[250:253], a[130:133], v[30:33]\n"
    ".p2align 12\n"
    "L_pg6_%=:\n"
    "s_cbranch_execz L_tramp_%=\n"
    "ds_read_b64_tr_b16 a[232:233], v69 offset:59392\n"
    "v_mfma_f32_16x16x32_bf16 v[30:33], a[190:193], a[134:137], v[30:33]\n"
    "ds_read_b64_tr_b16 a[234:235], v69 offset:61440\n"
    "v_mfma_f32_16x16x32_bf16 v[30:33], a[194:197], a[138:141], v[30:33]\n"
    "ds_read_b64_tr_b16 a[236:237], v69 offset:63488\n"
    "buffer_load_dwordx2 v[220:221], v29, s[28:31], s80 offen offset:0 sc1\n"
    "v_mfma_f32_16x16x32_bf16 v[188:191], a[198:201], v[116:119], v[188:191]\n"
    "ds_read_b64_tr_b16 a[238:239], v70 offset:0\n"
    "s_waitcnt lgkmcnt(7)\n"
    "v_mfma_f32_16x16x32_bf16 v[188:191], a[202:205], v[120:123], v[188:191]\n"
    "ds_read_b64_tr_b16 a[240:241], v70 offset:2048\n"
    "v_mfma_f32_16x16x32_bf16 v[188:191], a[206:209], v[124:127], v[188:191]\n"
    "ds_read_b64_tr_b16 a[242:243], v70 offset:4096\n"
    "buffer_load_dwordx4 v57, s[20:23], s84 offen offset:1024 lds\n"
    "v_mfma_f32_16x16x32_bf16 v[188:191], a[210:213], v[128:131], v[188:191]\n"
    "ds_read_b64_tr_b16 a[244:245], v70 offset:6144\n"
    "v_mfma_f32_16x16x32_bf16 v[188:191], a[214:217], v[132:135], v[188:191]\n"
    "ds_read_b64_tr_b16 a[246:247], v71 offset:49152\n"
    "v_mfma_f32_16x16x32_bf16 v[188:191], a[218:221], v[136:139], v[188:191]\n"
    "ds_read_b64_tr_b16 a[248:249], v71 offset:51200\n"
    "buffer_load_dwordx4 v24, s[20:23], s84 offen offset:2048 lds\n"
    "v_mfma_f32_16x16x32_bf16 v[164:167], a[198:201], a[142:145], v[164:167]\n"
    "ds_read_b64_tr_b16 a[250:251], v71 offset:53248\n"
    "v_mfma_f32_16x16x32_bf16 v[164:167], a[202:205], a[146:149], v[164:167]\n"
    "ds_read_b64_tr_b16 a[252:253], v71 offset:55296\n"
    "v_mfma_f32_16x16x32_bf16 v[164:167], a[206:209], a[150:153], v[164:167]\n"
    "ds_read_b64_tr_b16 a[190:191], v71 offset:57344\n"
    "buffer_load_dwordx4 v75, s[20:23], s84 offen offset:3072 lds\n"
    "v_mfma_f32_16x16x32_bf16 v[164:167], a[210:213], a[154:157], v[164:167]\n"
    "ds_read_b64_tr_b16 a[192:193], v71 offset:59392\n"
    "s_add_u32 m0, s86, 4096\n"
    "v_mfma_f32_16x16x32_bf16 v[164:167], a[214:217], a[158:161], v[164:167]\n"
    "ds_read_b64_tr_b16 a[194:195], v71 offset:61440\n"
    "v_mfma_f32_16x16x32_bf16 v[164:167], a[218:221], a[162:165], v[164:167]\n"
    "ds_read_b64_tr_b16 a[196:197], v71 offset:63488\n"
    "v_mfma_f32_16x16x32_bf16 v[168:171], a[222:225], v[116:119], v[168:171]\n"
    "ds_read_b64_tr_b16 a[198:199], v72 offset:0\n"
    "s_waitcnt lgkmcnt(7)\n"
    "v_mfma_f32_16x16x32_bf16 v[168:171], a[226:229], v[120:123], v[168:171]\n"
    "ds_read_b64_tr_b16 a[200:201], v72 offset:2048\n"
    "buffer_load_dwordx4 v56, s[20:23], s89 offen offset:0 lds\n"
    "v_mfma_f32_16x16x32_bf16 v[168:171], a[230:233], v[124:127], v[168:171]\n"
    "ds_read_b64_tr_b16 a[202:203], v72 offset:4096\n"
    "v_mfma_f32_16x16x32_bf16 v[168:171], a[234:237], v[128:131], v[168:171]\n"
    "ds_read_b64_tr_b16 a[204:205], v72 offset:6144\n"
    "v_mfma_f32_16x16x32_bf16 v[168:171], a[238:241], v[132:135], v[168:171]\n"
    "ds_read_b64_tr_b16 a[206:207], v73 offset:49152\n"
    "buffer_load_dword v43, v25, s[28:31], s99 offen offset:128 sc1\n"
    "v_mfma_f32_16x16x32_bf16 v[168:171], a[242:245], v[136:139], v[168:171]\n"
    "ds_read_b64_tr_b16 a[208:209], v73 offset:51200\n"
    "v_mfma_f32_16x16x32_bf16 v[172:175], a[222:225], a[142:145], v[172:175]\n"
    "ds_read_b64_tr_b16 a[210:211], v73 offset:53248\n"
    "buffer_load_dwordx4 v[216:219], v18, s[28:31], s80 offen offset:1280 sc1\n"
    "v_mfma_f32_16x16x32_bf16 v[172:175], a[226:229], a[146:149], v[172:175]\n"
    "ds_read_b64_tr_b16 a[212:213], v73 offset:55296\n"
    "v_add_u32 v18, 102400, v18\n"
    "v_mfma_f32_16x16x32_bf16 v[172:175], a[230:233], a[150:153], v[172:175]\n"
    "ds_read_b64_tr_b16 a[214:215], v73 offset:57344\n"
    "v_mfma_f32_16x16x32_bf16 v[172:175], a[234:237], a[154:157], v[172:175]\n"
    "buffer_store_dwordx4 v[20:23], v28, s[28:31], s98 offen offset:2048 sc1\n"
    "ds_read_b64_tr_b16 a[216:217], v73 offset:59392\n"
    "v_mfma_f32_16x16x32_bf16 v[172:175], a[238:241], a[158:161], v[172:175]\n"
    "ds_read_b64_tr_b16 a[218:219], v73 offset:61440\n"
    "v_mfma_f32_16x16x32_bf16 v[172:175], a[242:245], a[162:165], v[172:175]\n"
    "ds_read_b64_tr_b16 a[220:221], v73 offset:63488\n"
    "v_mfma_f32_16x16x32_bf16 v[176:179], a[246:249], v[116:119], v[176:179]\n"
    "ds_read_b64_tr_b16 a[222:223], v74 offset:0\n"
    "s_waitcnt lgkmcnt(6)\n"
    "v_mfma_f32_16x16x32_bf16 v[176:179], a[250:253], v[120:123], v[176:179]\n"
    "ds_read_b64_tr_b16 a[224:225], v74 offset:2048\n"
    "buffer_load_dwordx2 v[232:233], v29, s[28:31], s80 offen offset:1280 sc1\n"
    "v_mfma_f32_16x16x32_bf16 v[176:179], a[190:193], v[124:127], v[176:179]\n"
    "ds_read_b64_tr_b16 a[226:227], v74 offset:4096\n"
    "s_waitcnt vmcnt(22) lgkmcnt(6)\n"
    "v_mfma_f32_16x16x32_bf16 v[176:179], a[194:197], v[128:131], v[176:179]\n"
    "s_barrier\n"
    "ds_read_b64_tr_b16 a[228:229], v74 offset:6144\n"
    "v_mfma_f32_16x16x32_bf16 v[176:179], a[198:201], v[132:135], v[176:179]\n"
    "ds_read_b64_tr_b16 a[230:231], v68 offset:8192\n"
    "s_lshr_b32 s80, s78, 1\n"
    "s_lshl_b32 s80, s80, 1\n"
    "s_add_u32 s80, s80, s76\n"
    "s_cmp_lt_u32 s80, 2\n"
    "s_cselect_b32 s80, 0xf149, 0\n"
    "v_mfma_f32_16x16x32_bf16 v[176:179], a[202:205], v[136:139], v[176:179]\n"
    "ds_read_b64_tr_b16 a[232:233], v68 offset:10240\n"
    "v_mov_b32 v16, s80\n"
    "v_mfma_f32_16x16x32_bf16 v[180:183], a[246:249], a[142:145], v[180:183]\n"
    "ds_read_b64_tr_b16 a[234:235], v68 offset:12288\n"
    "s_cmp_eq_u32 s76, 0\n"
    "s_cselect_b32 s80, s80, 0\n"
    "v_mfma_f32_16x16x32_bf16 v[180:183], a[250:253], a[146:149], v[180:183]\n"
    "ds_read_b64_tr_b16 a[236:237], v68 offset:14336\n"
    "v_mov_b32 v17, s80\n"
    "v_mfma_f32_16x16x32_bf16 v[180:183], a[190:193], a[150:153], v[180:183]\n"
    "ds_read_b64_tr_b16 a[238:239], v68 offset:16384\n"
    "v_mfma_f32_16x16x32_bf16 v[180:183], a[194:197], a[154:157], v[180:183]\n"
    "ds_read_b64_tr_b16 a[240:241], v68 offset:18432\n"
    "v_mfma_f32_16x16x32_bf16 v[180:183], a[198:201], a[158:161], v[180:183]\n"
    "ds_read_b64_tr_b16 a[242:243], v68 offset:20480\n"
    "v_mfma_f32_16x16x32_bf16 v[180:183], a[202:205], a[162:165], v[180:183]\n"
    "ds_read_b64_tr_b16 a[244:245], v68 offset:22528\n"
    "v_mfma_f32_16x16x32_bf16 v[184:187], a[206:209], v[116:119], v[184:187]\n"
    "ds_read_b64_tr_b16 a[246:247], v68 offset:24576\n"
    "v_mfma_f32_16x16x32_bf16 v[184:187], a[210:213], v[120:123], v[184:187]\n"
    "ds_read_b64_tr_b16 a[248:249], v68 offset:26624\n"
    "s_waitcnt lgkmcnt(7)\n"
    "v_mfma_f32_16x16x32_bf16 v[184:187], a[214:217], v[124:127], v[184:187]\n"
    "ds_read_b64_tr_b16 a[250:251], v68 offset:28672\n"
    "v_mfma_f32_16x16x32_bf16 v[184:187], a[218:221], v[128:131], v[184:187]\n"
    "ds_read_b64_tr_b16 a[252:253], v68 offset:30720\n"
    "v_mfma_f32_16x16x32_bf16 v[184:187], a[222:225], v[132:135], v[184:187]\n"
    "ds_read_b64_tr_b16 a[190:191], v70 offset:8192\n"
    "v_mfma_f32_16x16x32_bf16 v[184:187], a[226:229], v[136:139], v[184:187]\n"
    "ds_read_b64_tr_b16 a[192:193], v70 offset:10240\n"
    "v_mfma_f32_16x16x32_bf16 v[30:33], a[206:209], a[142:145], v[30:33]\n"
    "buffer_store_dwordx4 v[192:195], v28, s[28:31], s98 offen offset:3072 sc1\n"
    "ds_read_b64_tr_b16 a[194:195], v70 offset:12288\n"
    "v_mfma_f32_16x16x32_bf16 v[30:33], a[210:213], a[146:149], v[30:33]\n"
    "ds_read_b64_tr_b16 a[196:197], v70 offset:14336\n"
    "s_add_u32 s98, s58, 8192\n"
    "v_mfma_f32_16x16x32_bf16 v[30:33], a[214:217], a[150:153], v[30:33]\n"
    "ds_read_b64_tr_b16 a[198:199], v70 offset:16384\n"
    "v_mfma_f32_16x16x32_bf16 v[30:33], a[218:221], a[154:157], v[30:33]\n"
    "ds_read_b64_tr_b16 a[200:201], v70 offset:18432\n"
    "v_mfma_f32_16x16x32_bf16 v[30:33], a[222:225], a[158:161], v[30:33]\n"
    "ds_read_b64_tr_b16 a[202:203], v70 offset:20480\n"
    "v_mfma_f32_16x16x32_bf16 v[30:33], a[226:229], a[162:165], v[30:33]\n"
    "ds_read_b64_tr_b16 a[204:205], v70 offset:22528\n"
    "v_mfma_f32_16x16x32_bf16 v[188:191], a[230:233], v[140:143], v[188:191]\n"
    "ds_read_b64_tr_b16 a[206:207], v70 offset:24576\n"
    "s_waitcnt lgkmcnt(7)\n"
    "v_mfma_f32_16x16x32_bf16 v[188:191], a[234:237], v[144:147], v[188:191]\n"
    "ds_read_b64_tr_b16 a[208:209], v70 offset:26624\n"
    "v_mfma_f32_16x16x32_bf16 v[188:191], a[238:241], v[148:151], v[188:191]\n"
    "ds_read_b64_tr_b16 a[210:211], v70 offset:28672\n"
    "v_mfma_f32_16x16x32_bf16 v[188:191], a[242:245], v[152:155], v[188:191]\n"
    "ds_read_b64_tr_b16 a[212:213], v70 offset:30720\n"
    "v_mfma_f32_16x16x32_bf16 v[188:191], a[246:249], v[156:159], v[188:191]\n"
    "ds_read_b64_tr_b16 a[214:215], v72 offset:8192\n"
    "v_mfma_f32_16x16x32_bf16 v[188:191], a[250:253], v[160:163], v[188:191]\n"
    "ds_read_b64_tr_b16 a[216:217], v72 offset:10240\n"
    "v_mfma_f32_16x16x32_bf16 v[164:167], a[230:233], a[166:169], v[164:167]\n"
    "ds_read_b64_tr_b16 a[218:219], v72 offset:12288\n"
    "v_mfma_f32_16x16x32_bf16 v[164:167], a[234:237], a[170:173], v[164:167]\n"
    "ds_read_b64_tr_b16 a[220:221], v72 offset:14336\n"
    "v_mfma_f32_16x16x32_bf16 v[164:167], a[238:241], a[174:177], v[164:167]\n"
    "ds_read_b64_tr_b16 a[222:223], v72 offset:16384\n"
    "v_mfma_f32_16x16x32_bf16 v[164:167], a[242:245], a[178:181], v[164:167]\n"
    "ds_read_b64_tr_b16 a[224:225], v72 offset:18432\n"
    "v_cvt_pk_bf16_f32 v252, v188, v189\n"
    "v_mfma_f32_16x16x32_bf16 v[164:167], a[246:249], a[182:185], v[164:167]\n"
    "ds_read_b64_tr_b16 a[226:227], v72 offset:20480\n"
    "v_cvt_pk_bf16_f32 v253, v190, v191\n"
    "v_mfma_f32_16x16x32_bf16 v[164:167], a[250:253], a[186:189], v[164:167]\n"
    "ds_read_b64_tr_b16 a[228:229], v72 offset:22528\n"
    "v_pk_max_i16 v252, v252, 0\n"
    "v_mfma_f32_16x16x32_bf16 v[168:171], a[190:193], v[140:143], v[168:171]\n"
    "ds_read_b64_tr_b16 a[230:231], v72 offset:24576\n"
    "s_waitcnt lgkmcnt(7)\n"
    "v_mfma_f32_16x16x32_bf16 v[168:171], a[194:197], v[144:147], v[168:171]\n"
    "ds_read_b64_tr_b16 a[232:233], v72 offset:26624\n"
    "v_pk_max_i16 v253, v253, 0\n"
    "v_mfma_f32_16x16x32_bf16 v[168:171], a[198:201], v[148:151], v[168:171]\n"
    "ds_read_b64_tr_b16 a[234:235], v72 offset:28672\n"
    "v_cvt_pk_bf16_f32 v2, v164, v165\n"
    "v_mfma_f32_16x16x32_bf16 v[168:171], a[202:205], v[152:155], v[168:171]\n"
    "ds_read_b64_tr_b16 a[236:237], v72 offset:30720\n"
    "v_cvt_pk_bf16_f32 v3, v166, v167\n"
    "v_mfma_f32_16x16x32_bf16 v[168:171], a[206:209], v[156:159], v[168:171]\n"
    "buffer_store_dwordx4 v[196:199], v28, s[28:31], s98 offen offset:0 sc1\n"
    "ds_read_b64_tr_b16 a[238:239], v74 offset:8192\n"
    "v_mfma_f32_16x16x32_bf16 v[168:171], a[210:213], v[160:163], v[168:171]\n"
    "ds_read_b64_tr_b16 a[240:241], v74 offset:10240\n"
    "s_waitcnt vmcnt(13) lgkmcnt(6)\n"
    "v_mfma_f32_16x16x32_bf16 v[172:175], a[190:193], a[166:169], v[172:175]\n"
    "ds_read_b64_tr_b16 a[242:243], v74 offset:12288\n"
    "v_pk_max_i16 v2, v2, 0\n"
    "v_mfma_f32_16x16x32_bf16 v[172:175], a[194:197], a[170:173], v[172:175]\n"
    "ds_read_b64_tr_b16 a[244:245], v74 offset:14336\n"
    "v_pk_max_i16 v3, v3, 0\n"
    "v_mfma_f32_16x16x32_bf16 v[172:175], a[198:201], a[174:177], v[172:175]\n"
    "ds_read_b64_tr_b16 a[246:247], v74 offset:16384\n"
    "v_cvt_pk_bf16_f32 v254, v168, v169\n"
    "v_mfma_f32_16x16x32_bf16 v[172:175], a[202:205], a[178:181], v[172:175]\n"
    "ds_read_b64_tr_b16 a[248:249], v74 offset:18432\n"
    "v_cvt_pk_bf16_f32 v255, v170, v171\n"
    "v_mfma_f32_16x16x32_bf16 v[172:175], a[206:209], a[182:185], v[172:175]\n"
    "ds_read_b64_tr_b16 a[250:251], v74 offset:20480\n"
    "v_pk_max_i16 v254, v254, 0\n"
    "v_mfma_f32_16x16x32_bf16 v[172:175], a[210:213], a[186:189], v[172:175]\n"
    "ds_read_b64_tr_b16 a[252:253], v74 offset:22528\n"
    "v_pk_max_i16 v255, v255, 0\n"
    "v_mfma_f32_16x16x32_bf16 v[176:179], a[214:217], v[140:143], v[176:179]\n"
    "ds_read_b64_tr_b16 a[190:191], v74 offset:24576\n"
    "ds_write_b16 v86, v16 offset:1038\n"
    "v_mfma_f32_16x16x32_bf16 v[176:179], a[218:221], v[144:147], v[176:179]\n"
    "ds_read_b64_tr_b16 a[192:193], v74 offset:26624\n"
    "v_mfma_f32_16x16x32_bf16 v[176:179], a[222:225], v[148:151], v[176:179]\n"
    "ds_read_b64_tr_b16 a[194:195], v74 offset:28672\n"
    "v_cvt_pk_bf16_f32 v4, v172, v173\n"
    "v_mfma_f32_16x16x32_bf16 v[176:179], a[226:229], v[152:155], v[176:179]\n"
    "ds_read_b64_tr_b16 a[196:197], v74 offset:30720\n"
    "s_waitcnt lgkmcnt(8)\n"
    "v_mfma_f32_16x16x32_bf16 v[176:179], a[230:233], v[156:159], v[176:179]\n"
    "ds_write_b16 v86, v17 offset:11278\n"
    "v_mfma_f32_16x16x32_bf16 v[176:179], a[234:237], v[160:163], v[176:179]\n"
    "s_waitcnt vmcnt(11) lgkmcnt(7)\n"
    "v_mov_b32 v222, v214\n"
    "v_mfma_f32_16x16x32_bf16 v[180:183], a[214:217], a[166:169], v[180:183]\n"
    "v_mov_b32 v223, v215\n"
    "v_cndmask_b32 v215, 0, 1.0, s[72:73]\n"
    "v_mfma_f32_16x16x32_bf16 v[180:183], a[218:221], a[170:173], v[180:183]\n"
    "s_waitcnt vmcnt(10) lgkmcnt(4)\n"
    "v_mov_b32 v214, v220\n"
    "v_mfma_f32_16x16x32_bf16 v[180:183], a[222:225], a[174:177], v[180:183]\n"
    "v_mov_b32 v220, 0\n"
    "v_or_b32 v221, v221, v215\n"
    "v_mfma_f32_16x16x32_bf16 v[180:183], a[226:229], a[178:181], v[180:183]\n"
    "s_waitcnt vmcnt(4) lgkmcnt(2)\n"
    "v_mov_b32 v234, v218\n"
    "v_mfma_f32_16x16x32_bf16 v[180:183], a[230:233], a[182:185], v[180:183]\n"
    "v_mov_b32 v235, v219\n"
    "v_cndmask_b32 v219, 0, 1.0, s[72:73]\n"
    "v_mfma_f32_16x16x32_bf16 v[180:183], a[234:237], a[186:189], v[180:183]\n"
    "s_waitcnt vmcnt(2) lgkmcnt(0)\n"
    "v_mov_b32 v218, v232\n"
    "v_mfma_f32_16x16x32_bf16 v[184:187], a[238:241], v[140:143], v[184:187]\n"
    "s_barrier\n"
    "ds_read_b64 a[70:71], v18 offset:0\n"
    "v_mfma_f32_16x16x32_bf16 v[184:187], a[242:245], v[144:147], v[184:187]\n"
    "ds_read_b64 a[72:73], v44 offset:0\n"
    "v_cmp_eq_u32 vcc, 0, v43\n"
    "v_mfma_f32_16x16x32_bf16 v[184:187], a[246:249], v[148:151], v[184:187]\n"
    "buffer_store_dwordx4 v[200:203], v28, s[28:31], s98 offen offset:1024 sc1\n"
    "ds_read_b64 a[74:75], v18 offset:8\n"
    "v_mfma_f32_16x16x32_bf16 v[184:187], a[250:253], v[152:155], v[184:187]\n"
    "ds_read_b64 a[76:77], v18 offset:1280\n"
    "s_cbranch_vccnz L_h_%=\n"
    "L_h_ret_%=:\n"
    "v_mfma_f32_16x16x32_bf16 v[184:187], a[190:193], v[156:159], v[184:187]\n"
    "ds_read_b64 a[78:79], v44 offset:1280\n"
    "v_mov_b32 v232, 0\n"
    "v_mfma_f32_16x16x32_bf16 v[184:187], a[194:197], v[160:163], v[184:187]\n"
    "ds_read_b64 a[80:81], v18 offset:1288\n"
    "v_or_b32 v233, v233, v219\n"
    "v_mfma_f32_16x16x32_bf16 v[30:33], a[238:241], a[166:169], v[30:33]\n"
    "ds_read_b64_tr_b16 a[94:95], v45 offset:0\n"
    "v_cvt_pk_bf16_f32 v5, v174, v175\n"
    "v_mfma_f32_16x16x32_bf16 v[30:33], a[242:245], a[170:173], v[30:33]\n"
    "ds_read_b64_tr_b16 a[96:97], v45 offset:1280\n"
    "v_pk_max_i16 v4, v4, 0\n"
    "v_mfma_f32_16x16x32_bf16 v[30:33], a[246:249], a[174:177], v[30:33]\n"
    "ds_read_b64_tr_b16 a[98:99], v85 offset:0\n"
    "v_pk_max_i16 v5, v5, 0\n"
    "v_mfma_f32_16x16x32_bf16 v[30:33], a[250:253], a[178:181], v[30:33]\n"
    "ds_read_b64_tr_b16 a[100:101], v85 offset:1280\n"
    "v_cvt_pk_bf16_f32 v6, v176, v177\n"
    "v_mfma_f32_16x16x32_bf16 v[30:33], a[190:193], a[182:185], v[30:33]\n"
    "v_cvt_pk_bf16_f32 v7, v178, v179\n"
    "v_pk_max_i16 v6, v6, 0\n"
    "v_mfma_f32_16x16x32_bf16 v[30:33], a[194:197], a[186:189], v[30:33]\n"
    "v_pk_max_i16 v7, v7, 0\n"
    "v_cvt_pk_bf16_f32 v38, v180, v181\n"
    "v_cvt_pk_bf16_f32 v39, v182, v183\n"
    "v_pk_max_i16 v38, v38, 0\n"
    "v_pk_max_i16 v39, v39, 0\n"
    "L_att_%=:\n"
    "s_waitcnt lgkmcnt(4)\n"
    "v_mfma_f32_16x16x32_bf16 v[92:95], a[70:73], v[212:215], v[236:239]\n"
    "ds_read_b64 a[82:83], v18 offset:2560\n"
    "s_add_u32 s98, s58, 8192\n"
    "v_mov_b32 v120, 0\n"
    "v_mov_b32 v121, 0\n"
    "s_add_u32 s84, s37, s32\n"
    "s_add_u32 s84, s84, 147456\n"
    "v_mfma_f32_16x16x32_bf16 v[96:99], a[76:79], v[212:215], 0\n"
    "ds_read_b64 a[84:85], v44 offset:2560\n"
    "v_exp_f32 v92, v92\n"
    "v_exp_f32 v93, v93\n"
    "v_exp_f32 v94, v94\n"
    "v_mfma_f32_16x16x32_bf16 v[104:107], a[76:79], v[216:219], v[236:239]\n"
    "ds_read_b64_tr_b16 a[102:103], v45 offset:8\n"
    "s_add_u32 m0, s86, 20480\n"
    "v_exp_f32 v95, v95\n"
    "v_cvt_pk_bf16_f32 v116, v92, v93\n"
    "v_cvt_pk_bf16_f32 v117, v94, v95\n"
    "v_exp_f32 v96, v96\n"
    "v_exp_f32 v97, v97\n"
    "v_exp_f32 v98, v98\n"
    "v_exp_f32 v99, v99\n"
    "v_cvt_pk_bf16_f32 v118, v96, v97\n"
    "v_cvt_pk_bf16_f32 v119, v98, v99\n"
    "v_mfma_f32_16x16x32_bf16 v[108:111], a[72:75], v[220:223], v[236:239]\n"
    "ds_read_b64_tr_b16 a[104:105], v45 offset:1288\n"
    "buffer_load_dwordx4 v56, s[20:23], s84 offen offset:0 lds\n"
    "v_exp_f32 v104, v104\n"
    "v_exp_f32 v105, v105\n"
    "s_waitcnt lgkmcnt(2)\n"
    "v_mfma_f32_16x16x32_bf16 v[124:127], a[94:97], v[116:119], 0\n"
    "ds_read_b64 a[88:89], v18 offset:3840\n"
    "v_exp_f32 v106, v106\n"
    "v_exp_f32 v107, v107\n"
    "v_cvt_pk_bf16_f32 v122, v104, v105\n"
    "v_mfma_f32_16x16x32_bf16 v[112:115], a[78:81], v[220:223], 0\n"
    "buffer_store_dwordx4 v[204:207], v28, s[28:31], s98 offen offset:2048 sc1\n"
    "ds_read_b64 a[90:91], v44 offset:3840\n"
    "v_cvt_pk_bf16_f32 v123, v106, v107\n"
    "v_mfma_f32_16x16x32_bf16 v[140:143], a[98:101], v[116:119], 0\n"
    "ds_read_b64_tr_b16 a[106:107], v85 offset:8\n"
    "v_exp_f32 v108, v108\n"
    "v_exp_f32 v109, v109\n"
    "v_exp_f32 v110, v110\n"
    "v_mfma_f32_16x16x32_bf16 v[128:131], a[94:97], v[120:123], 0\n"
    "ds_read_b64_tr_b16 a[108:109], v85 offset:1288\n"
    "v_exp_f32 v111, v111\n"
    "v_cvt_pk_bf16_f32 v156, v108, v109\n"
    "v_cvt_pk_bf16_f32 v157, v110, v111\n"
    "v_exp_f32 v112, v112\n"
    "v_mfma_f32_16x16x32_bf16 v[96:99], a[78:81], v[232:235], v[236:239]\n"
    "ds_read_b64 a[86:87], v18 offset:2568\n"
    "buffer_load_dwordx4 v57, s[20:23], s84 offen offset:1024 lds\n"
    "v_exp_f32 v113, v113\n"
    "v_exp_f32 v114, v114\n"
    "v_mfma_f32_16x16x32_bf16 v[144:147], a[98:101], v[120:123], 0\n"
    "ds_read_b64_tr_b16 a[116:117], v45 offset:2560\n"
    "v_exp_f32 v115, v115\n"
    "v_cvt_pk_bf16_f32 v158, v112, v113\n"
    "v_cvt_pk_bf16_f32 v159, v114, v115\n"
    "v_mov_b32 v116, 0\n"
    "v_mfma_f32_16x16x32_bf16 v[100:103], a[82:85], v[212:215], 0\n"
    "ds_read_b64_tr_b16 a[118:119], v45 offset:3840\n"
    "v_mov_b32 v117, 0\n"
    "v_exp_f32 v96, v96\n"
    "v_exp_f32 v97, v97\n"
    "s_waitcnt lgkmcnt(3)\n"
    "v_mfma_f32_16x16x32_bf16 v[132:135], a[102:105], v[156:159], 0\n"
    "ds_read_b64 a[92:93], v18 offset:3848\n"
    "v_exp_f32 v98, v98\n"
    "v_exp_f32 v99, v99\n"
    "v_cvt_pk_bf16_f32 v118, v96, v97\n"
    "v_mfma_f32_16x16x32_bf16 v[104:107], a[88:91], v[212:215], 0\n"
    "buffer_store_dwordx4 v[208:211], v28, s[28:31], s98 offen offset:3072 sc1\n"
    "ds_read_b64_tr_b16 a[120:121], v85 offset:2560\n"
    "v_mfma_f32_16x16x32_bf16 v[148:151], a[106:109], v[156:159], 0\n"
    "ds_read_b64_tr_b16 a[122:123], v85 offset:3840\n"
    "v_cvt_pk_bf16_f32 v119, v98, v99\n"
    "v_exp_f32 v100, v100\n"
    "v_exp_f32 v101, v101\n"
    "v_mfma_f32_16x16x32_bf16 v[108:111], a[82:85], v[216:219], 0\n"
    "ds_read_b64 a[110:111], v18 offset:5120\n"
    "v_exp_f32 v102, v102\n"
    "v_exp_f32 v103, v103\n"
    "v_cvt_pk_bf16_f32 v120, v100, v101\n"
    "v_mfma_f32_16x16x32_bf16 v[136:139], a[102:105], v[116:119], 0\n"
    "ds_read_b64 a[112:113], v44 offset:5120\n"
    "buffer_load_dwordx4 v24, s[20:23], s84 offen offset:2048 lds\n"
    "s_add_u32 s98, s58, 12288\n"
    "v_cvt_pk_bf16_f32 v121, v102, v103\n"
    "v_exp_f32 v104, v104\n"
    "v_mfma_f32_16x16x32_bf16 v[112:115], a[88:91], v[216:219], 0\n"
    "ds_read_b64_tr_b16 a[124:125], v45 offset:2568\n"
    "v_exp_f32 v105, v105\n"
    "v_exp_f32 v106, v106\n"
    "v_exp_f32 v107, v107\n"
    "v_mfma_f32_16x16x32_bf16 v[152:155], a[106:109], v[116:119], 0\n"
    "ds_read_b64_tr_b16 a[126:127], v45 offset:3848\n"
    "buffer_load_dwordx4 v75, s[20:23], s84 offen offset:3072 lds\n"
    "v_cvt_pk_bf16_f32 v122, v104, v105\n"
    "v_cvt_pk_bf16_f32 v123, v106, v107\n"
    "v_exp_f32 v108, v108\n"
    "s_waitcnt lgkmcnt(3)\n"
    "v_mfma_f32_16x16x32_bf16 v[92:95], a[84:87], v[220:223], 0\n"
    "ds_read_b64 a[34:35], v18 offset:6400\n"
    "v_exp_f32 v109, v109\n"
    "v_exp_f32 v110, v110\n"
    "v_exp_f32 v111, v111\n"
    "v_mfma_f32_16x16x32_bf16 v[124:127], a[116:119], v[120:123], v[124:127]\n"
    "ds_read_b64 a[36:37], v44 offset:6400\n"
    "v_cvt_pk_bf16_f32 v156, v108, v109\n"
    "v_cvt_pk_bf16_f32 v157, v110, v111\n"
    "v_exp_f32 v112, v112\n"
    "v_exp_f32 v113, v113\n"
    "v_mfma_f32_16x16x32_bf16 v[96:99], a[90:93], v[220:223], 0\n"
    "buffer_store_dwordx4 v[252:255], v28, s[28:31], s98 offen offset:0 sc1\n"
    "ds_read_b64_tr_b16 a[128:129], v85 offset:2568\n"
    "v_mfma_f32_16x16x32_bf16 v[140:143], a[120:123], v[120:123], v[140:143]\n"
    "ds_read_b64_tr_b16 a[130:131], v85 offset:3848\n"
    "s_add_u32 s89, s41, s32\n"
    "s_add_u32 m0, s86, 24576\n"
    "v_exp_f32 v114, v114\n"
    "v_exp_f32 v115, v115\n"
    "v_cvt_pk_bf16_f32 v158, v112, v113\n"
    "v_cvt_pk_bf16_f32 v159, v114, v115\n"
    "v_mfma_f32_16x16x32_bf16 v[100:103], a[84:87], v[232:235], 0\n"
    "ds_read_b64 a[114:115], v18 offset:5128\n"
    "buffer_load_dwordx4 v56, s[12:15], s89 offen offset:0 lds\n"
    "v_exp_f32 v92, v92\n"
    "v_exp_f32 v93, v93\n"
    "v_mfma_f32_16x16x32_bf16 v[128:131], a[116:119], v[156:159], v[128:131]\n"
    "ds_read_b64_tr_b16 a[94:95], v45 offset:5120\n"
    "v_exp_f32 v94, v94\n"
    "v_exp_f32 v95, v95\n"
    "v_cvt_pk_bf16_f32 v116, v92, v93\n"
    "v_mfma_f32_16x16x32_bf16 v[104:107], a[90:93], v[232:235], 0\n"
    "ds_read_b64_tr_b16 a[96:97], v45 offset:6400\n"
    "v_cvt_pk_bf16_f32 v117, v94, v95\n"
    "v_exp_f32 v96, v96\n"
    "v_exp_f32 v97, v97\n"
    "v_mfma_f32_16x16x32_bf16 v[144:147], a[120:123], v[156:159], v[144:147]\n"
    "ds_read_b64 a[38:39], v18 offset:6408\n"
    "v_exp_f32 v98, v98\n"
    "v_exp_f32 v99, v99\n"
    "v_cvt_pk_bf16_f32 v118, v96, v97\n"
    "v_cvt_pk_bf16_f32 v119, v98, v99\n"
    "s_waitcnt lgkmcnt(3)\n"
    "v_mfma_f32_16x16x32_bf16 v[108:111], a[110:113], v[212:215], 0\n"
    "ds_read_b64_tr_b16 a[98:99], v85 offset:5120\n"
    "v_exp_f32 v100, v100\n"
    "v_exp_f32 v101, v101\n"
    "v_exp_f32 v102, v102\n"
    "v_mfma_f32_16x16x32_bf16 v[132:135], a[124:127], v[116:119], v[132:135]\n"
    "ds_read_b64_tr_b16 a[100:101], v85 offset:6400\n"
    "v_exp_f32 v103, v103\n"
    "v_cvt_pk_bf16_f32 v120, v100, v101\n"
    "v_cvt_pk_bf16_f32 v121, v102, v103\n"
    "v_exp_f32 v104, v104\n"
    "v_mfma_f32_16x16x32_bf16 v[112:115], a[34:37], v[212:215], 0\n"
    "buffer_store_dwordx4 v[2:5], v28, s[28:31], s98 offen offset:1024 sc1\n"
    "ds_read_b64 a[70:71], v18 offset:7680\n"
    "v_mfma_f32_16x16x32_bf16 v[148:151], a[128:131], v[116:119], v[148:151]\n"
    "ds_read_b64 a[72:73], v44 offset:7680\n"
    "v_exp_f32 v105, v105\n"
    "v_exp_f32 v106, v106\n"
    "v_exp_f32 v107, v107\n"
    "v_cvt_pk_bf16_f32 v122, v104, v105\n"
    "v_cvt_pk_bf16_f32 v123, v106, v107\n"
    "v_mfma_f32_16x16x32_bf16 v[92:95], a[110:113], v[216:219], 0\n"
    "ds_read_b64_tr_b16 a[102:103], v45 offset:5128\n"
    "v_exp_f32 v108, v108\n"
    "v_exp_f32 v109, v109\n"
    "v_exp_f32 v110, v110\n"
    "v_mfma_f32_16x16x32_bf16 v[136:139], a[124:127], v[120:123], v[136:139]\n"
    "ds_read_b64_tr_b16 a[104:105], v45 offset:6408\n"
    "v_exp_f32 v111, v111\n"
    "v_cvt_pk_bf16_f32 v156, v108, v109\n"
    "v_cvt_pk_bf16_f32 v157, v110, v111\n"
    "v_exp_f32 v112, v112\n"
    "v_mfma_f32_16x16x32_bf16 v[96:99], a[34:37], v[216:219], 0\n"
    "ds_read_b64 a[76:77], v18 offset:8960\n"
    "v_exp_f32 v113, v113\n"
    "v_exp_f32 v114, v114\n"
    "v_exp_f32 v115, v115\n"
    "v_mfma_f32_16x16x32_bf16 v[152:155], a[128:131], v[120:123], v[152:155]\n"
    "ds_read_b64 a[78:79], v44 offset:8960\n"
    "s_add_u32 s84, s37, s43\n"
    "v_cvt_pk_bf16_f32 v158, v112, v113\n"
    "v_cvt_pk_bf16_f32 v159, v114, v115\n"
    "v_exp_f32 v92, v92\n"
    "v_mfma_f32_16x16x32_bf16 v[100:103], a[112:115], v[220:223], 0\n"
    "ds_read_b64_tr_b16 a[106:107], v85 offset:5128\n"
    "s_add_u32 m0, s86, 40960\n"
    "v_exp_f32 v93, v93\n"
    "v_exp_f32 v94, v94\n"
    "s_waitcnt lgkmcnt(3)\n"
    "v_mfma_f32_16x16x32_bf16 v[124:127], a[94:97], v[156:159], v[124:127]\n"
    "ds_read_b64_tr_b16 a[108:109], v85 offset:6408\n"
    "buffer_load_dwordx4 v56, s[20:23], s84 offen offset:0 lds\n"
    "v_exp_f32 v95, v95\n"
    "v_cvt_pk_bf16_f32 v116, v92, v93\n"
    "v_cvt_pk_bf16_f32 v117, v94, v95\n"
    "v_mfma_f32_16x16x32_bf16 v[104:107], a[36:39], v[220:223], 0\n"
    "ds_read_b64 a[74:75], v18 offset:7688\n"
    "v_exp_f32 v96, v96\n"
    "v_exp_f32 v97, v97\n"
    "v_exp_f32 v98, v98\n"
    "v_mfma_f32_16x16x32_bf16 v[140:143], a[98:101], v[156:159], v[140:143]\n"
    "ds_read_b64_tr_b16 a[116:117], v45 offset:7680\n"
    "v_exp_f32 v99, v99\n"
    "v_cvt_pk_bf16_f32 v118, v96, v97\n"
    "v_cvt_pk_bf16_f32 v119, v98, v99\n"
    "v_cvt_pk_bf16_f32 v8, v184, v185\n"
    "v_mfma_f32_16x16x32_bf16 v[108:111], a[112:115], v[232:235], 0\n"
    "ds_read_b64_tr_b16 a[118:119], v45 offset:8960\n"
    "v_cvt_pk_bf16_f32 v9, v186, v187\n"
    "v_pk_max_i16 v8, v8, 0\n"
    "v_pk_max_i16 v9, v9, 0\n"
    "v_exp_f32 v100, v100\n"
    "v_mfma_f32_16x16x32_bf16 v[128:131], a[94:97], v[116:119], v[128:131]\n"
    "buffer_store_dwordx4 v[6:9], v28, s[28:31], s98 offen offset:2048 sc1\n"
    "ds_read_b64 a[80:81], v18 offset:8968\n"
    "v_mfma_f32_16x16x32_bf16 v[112:115], a[36:39], v[232:235], 0\n"
    "ds_read_b64_tr_b16 a[120:121], v85 offset:7680\n"
    "v_exp_f32 v101, v101\n"
    "v_exp_f32 v102, v102\n"
    "v_exp_f32 v103, v103\n"
    "v_mfma_f32_16x16x32_bf16 v[144:147], a[98:101], v[116:119], v[144:147]\n"
    "ds_read_b64_tr_b16 a[122:123], v85 offset:8960\n"
    "v_cvt_pk_bf16_f32 v120, v100, v101\n"
    "v_cvt_pk_bf16_f32 v121, v102, v103\n"
    "v_exp_f32 v104, v104\n"
    "v_exp_f32 v105, v105\n"
    "v_exp_f32 v106, v106\n"
    "v_exp_f32 v107, v107\n"
    "v_cvt_pk_bf16_f32 v122, v104, v105\n"
    "v_cvt_pk_bf16_f32 v123, v106, v107\n"
    "v_mfma_f32_16x16x32_bf16 v[92:95], a[70:73], v[212:215], 0\n"
    "ds_read_b64 a[82:83], v18 offset:10240\n"
    "buffer_load_dwordx4 v57, s[20:23], s84 offen offset:1024 lds\n"
    "v_exp_f32 v108, v108\n"
    "v_exp_f32 v109, v109\n"
    "v_mfma_f32_16x16x32_bf16 v[132:135], a[102:105], v[120:123], v[132:135]\n"
    "ds_read_b64 a[84:85], v44 offset:10240\n"
    "v_exp_f32 v110, v110\n"
    "v_exp_f32 v111, v111\n"
    "s_waitcnt lgkmcnt(2)\n"
    "v_mfma_f32_16x16x32_bf16 v[96:99], a[76:79], v[212:215], 0\n"
    "ds_read_b64_tr_b16 a[124:125], v45 offset:7688\n"
    "buffer_load_dwordx4 v24, s[20:23], s84 offen offset:2048 lds\n"
    "v_cvt_pk_bf16_f32 v156, v108, v109\n"
    "v_cvt_pk_bf16_f32 v157, v110, v111\n"
    "v_exp_f32 v112, v112\n"
    "v_mfma_f32_16x16x32_bf16 v[148:151], a[106:109], v[120:123], v[148:151]\n"
    "ds_read_b64_tr_b16 a[126:127], v45 offset:8968\n"
    "v_exp_f32 v113, v113\n"
    "v_exp_f32 v114, v114\n"
    "v_exp_f32 v115, v115\n"
    "v_cvt_pk_bf16_f32 v158, v112, v113\n"
    "v_cvt_pk_bf16_f32 v159, v114, v115\n"
    "v_mfma_f32_16x16x32_bf16 v[100:103], a[70:73], v[216:219], 0\n"
    "ds_read_b64 a[88:89], v18 offset:11520\n"
    "v_cvt_pk_bf16_f32 v40, v30, v31\n"
    "v_cvt_pk_bf16_f32 v41, v32, v33\n"
    "v_pk_max_i16 v40, v40, 0\n"
    "v_pk_max_i16 v41, v41, 0\n"
    "v_exp_f32 v92, v92\n"
    "v_mfma_f32_16x16x32_bf16 v[136:139], a[102:105], v[156:159], v[136:139]\n"
    "buffer_store_dwordx4 v[38:41], v28, s[28:31], s98 offen offset:3072 sc1\n"
    "ds_read_b64 a[90:91], v44 offset:11520\n"
    "v_mfma_f32_16x16x32_bf16 v[104:107], a[76:79], v[216:219], 0\n"
    "ds_read_b64_tr_b16 a[128:129], v85 offset:7688\n"
    "s_add_u32 s83, s50, s36\n"
    "v_exp_f32 v93, v93\n"
    "v_exp_f32 v94, v94\n"
    "v_mfma_f32_16x16x32_bf16 v[152:155], a[106:109], v[156:159], v[152:155]\n"
    "ds_read_b64_tr_b16 a[130:131], v85 offset:8968\n"
    "v_exp_f32 v95, v95\n"
    "v_cvt_pk_bf16_f32 v116, v92, v93\n"
    "v_cvt_pk_bf16_f32 v117, v94, v95\n"
    "v_exp_f32 v96, v96\n"
    "v_exp_f32 v97, v97\n"
    "v_exp_f32 v98, v98\n"
    "v_exp_f32 v99, v99\n"
    "v_cvt_pk_bf16_f32 v118, v96, v97\n"
    "v_cvt_pk_bf16_f32 v119, v98, v99\n"
    "v_mfma_f32_16x16x32_bf16 v[108:111], a[72:75], v[220:223], 0\n"
    "ds_read_b64 a[86:87], v18 offset:10248\n"
    "buffer_load_dwordx4 v75, s[20:23], s84 offen offset:3072 lds\n"
    "v_exp_f32 v100, v100\n"
    "v_exp_f32 v101, v101\n"
    "v_mfma_f32_16x16x32_bf16 v[124:127], a[116:119], v[116:119], v[124:127]\n"
    "ds_read_b64_tr_b16 a[94:95], v45 offset:10240\n"
    "v_exp_f32 v102, v102\n"
    "v_exp_f32 v103, v103\n"
    "v_cvt_pk_bf16_f32 v120, v100, v101\n"
    "v_mfma_f32_16x16x32_bf16 v[112:115], a[78:81], v[220:223], 0\n"
    "buffer_store_dwordx4 a[42:45], v28, s[28:31], s83 offen offset:0 sc1\n"
    "ds_read_b64_tr_b16 a[96:97], v45 offset:11520\n"
    "v_mfma_f32_16x16x32_bf16 v[140:143], a[120:123], v[116:119], v[140:143]\n"
    "ds_read_b64 a[92:93], v18 offset:11528\n"
    "s_add_u32 s89, s39, s43\n"
    "s_add_u32 m0, s86, 45056\n"
    "v_cvt_pk_bf16_f32 v121, v102, v103\n"
    "v_exp_f32 v104, v104\n"
    "v_exp_f32 v105, v105\n"
    "v_exp_f32 v106, v106\n"
    "v_exp_f32 v107, v107\n"
    "v_cvt_pk_bf16_f32 v122, v104, v105\n"
    "v_cvt_pk_bf16_f32 v123, v106, v107\n"
    "v_mfma_f32_16x16x32_bf16 v[92:95], a[72:75], v[232:235], 0\n"
    "ds_read_b64_tr_b16 a[98:99], v85 offset:10240\n"
    "buffer_load_dwordx4 v56, s[20:23], s89 offen offset:0 lds\n"
    "v_exp_f32 v108, v108\n"
    "v_exp_f32 v109, v109\n"
    "v_mfma_f32_16x16x32_bf16 v[128:131], a[116:119], v[120:123], v[128:131]\n"
    "ds_read_b64_tr_b16 a[100:101], v85 offset:11520\n"
    "v_exp_f32 v110, v110\n"
    "v_exp_f32 v111, v111\n"
    "v_cvt_pk_bf16_f32 v156, v108, v109\n"
    "v_mfma_f32_16x16x32_bf16 v[96:99], a[78:81], v[232:235], 0\n"
    "ds_read_b64 a[110:111], v18 offset:12800\n"
    "v_cvt_pk_bf16_f32 v157, v110, v111\n"
    "v_exp_f32 v112, v112\n"
    "v_exp_f32 v113, v113\n"
    ".p2align 12\n"
    "L_pg7_%=:\n"
    "s_cbranch_execz L_tramp_%=\n"
    "v_mfma_f32_16x16x32_bf16 v[144:147], a[120:123], v[120:123], v[144:147]\n"
    "ds_read_b64 a[112:113], v44 offset:12800\n"
    "v_exp_f32 v114, v114\n"
    "v_exp_f32 v115, v115\n"
    "v_cvt_pk_bf16_f32 v158, v112, v113\n"
    "v_cvt_pk_bf16_f32 v159, v114, v115\n"
    "s_waitcnt lgkmcnt(3)\n"
    "v_mfma_f32_16x16x32_bf16 v[100:103], a[82:85], v[212:215], 0\n"
    "ds_read_b64_tr_b16 a[102:103], v45 offset:10248\n"
    "v_exp_f32 v92, v92\n"
    "v_exp_f32 v93, v93\n"
    "v_exp_f32 v94, v94\n"
    "v_mfma_f32_16x16x32_bf16 v[132:135], a[124:127], v[156:159], v[132:135]\n"
    "ds_read_b64_tr_b16 a[104:105], v45 offset:11528\n"
    "v_exp_f32 v95, v95\n"
    "v_cvt_pk_bf16_f32 v116, v92, v93\n"
    "v_cvt_pk_bf16_f32 v117, v94, v95\n"
    "v_exp_f32 v96, v96\n"
    "v_mfma_f32_16x16x32_bf16 v[104:107], a[88:91], v[212:215], 0\n"
    "buffer_store_dwordx4 a[42:45], v28, s[28:31], s83 offen offset:1024 sc1\n"
    "ds_read_b64 a[34:35], v18 offset:14080\n"
    "v_mfma_f32_16x16x32_bf16 v[148:151], a[128:131], v[156:159], v[148:151]\n"
    "ds_read_b64 a[36:37], v44 offset:14080\n"
    "v_exp_f32 v97, v97\n"
    "v_exp_f32 v98, v98\n"
    "v_exp_f32 v99, v99\n"
    "v_cvt_pk_bf16_f32 v118, v96, v97\n"
    "v_cvt_pk_bf16_f32 v119, v98, v99\n"
    "v_mfma_f32_16x16x32_bf16 v[108:111], a[82:85], v[216:219], 0\n"
    "ds_read_b64_tr_b16 a[106:107], v85 offset:10248\n"
    "v_exp_f32 v100, v100\n"
    "v_exp_f32 v101, v101\n"
    "v_exp_f32 v102, v102\n"
    "v_mfma_f32_16x16x32_bf16 v[136:139], a[124:127], v[116:119], v[136:139]\n"
    "ds_read_b64_tr_b16 a[108:109], v85 offset:11528\n"
    "s_sub_u32 s82, s46, 30720\n"
    "v_exp_f32 v103, v103\n"
    "v_cvt_pk_bf16_f32 v120, v100, v101\n"
    "v_cvt_pk_bf16_f32 v121, v102, v103\n"
    "v_mfma_f32_16x16x32_bf16 v[112:115], a[88:91], v[216:219], 0\n"
    "ds_read_b64 a[114:115], v18 offset:12808\n"
    "v_exp_f32 v104, v104\n"
    "v_exp_f32 v105, v105\n"
    "v_exp_f32 v106, v106\n"
    "v_mfma_f32_16x16x32_bf16 v[152:155], a[128:131], v[116:119], v[152:155]\n"
    "ds_read_b64_tr_b16 a[116:117], v45 offset:12800\n"
    "s_add_u32 s84, s37, s43\n"
    "s_add_u32 s84, s84, 147456\n"
    "v_exp_f32 v107, v107\n"
    "v_cvt_pk_bf16_f32 v122, v104, v105\n"
    "v_cvt_pk_bf16_f32 v123, v106, v107\n"
    "v_mfma_f32_16x16x32_bf16 v[92:95], a[84:87], v[220:223], 0\n"
    "ds_read_b64_tr_b16 a[118:119], v45 offset:14080\n"
    "s_add_u32 m0, s86, 61440\n"
    "v_exp_f32 v108, v108\n"
    "v_exp_f32 v109, v109\n"
    "v_mfma_f32_16x16x32_bf16 v[124:127], a[94:97], v[120:123], v[124:127]\n"
    "ds_read_b64 a[38:39], v18 offset:14088\n"
    "buffer_load_dwordx4 v56, s[20:23], s84 offen offset:0 lds\n"
    "v_exp_f32 v110, v110\n"
    "v_exp_f32 v111, v111\n"
    "v_mfma_f32_16x16x32_bf16 v[96:99], a[90:93], v[220:223], 0\n"
    "ds_read_b64_tr_b16 a[120:121], v85 offset:12800\n"
    "v_cvt_pk_bf16_f32 v156, v108, v109\n"
    "v_cvt_pk_bf16_f32 v157, v110, v111\n"
    "v_exp_f32 v112, v112\n"
    "s_waitcnt lgkmcnt(3)\n"
    "v_mfma_f32_16x16x32_bf16 v[140:143], a[98:101], v[120:123], v[140:143]\n"
    "ds_read_b64_tr_b16 a[122:123], v85 offset:14080\n"
    "v_exp_f32 v113, v113\n"
    "v_exp_f32 v114, v114\n"
    "v_exp_f32 v115, v115\n"
    "v_cvt_pk_bf16_f32 v158, v112, v113\n"
    "v_cvt_pk_bf16_f32 v159, v114, v115\n"
    "v_mfma_f32_16x16x32_bf16 v[100:103], a[84:87], v[232:235], 0\n"
    "ds_read_b64 a[70:71], v18 offset:15360\n"
    "s_bitcmp1_b32 s77, 0\n"
    "s_cselect_b64 exec, -1, 0\n"
    "buffer_load_dwordx4 v[224:227], v54, s[28:31], s46 offen offset:0 sc1\n"
    "buffer_load_dwordx4 v[228:231], v54, s[28:31], s46 offen offset:1280 sc1\n"
    "s_mov_b64 exec, -1\n"
    "v_mfma_f32_16x16x32_bf16 v[128:131], a[94:97], v[156:159], v[128:131]\n"
    "ds_read_b64 a[72:73], v44 offset:15360\n"
    "buffer_load_dwordx4 v57, s[20:23], s84 offen offset:1024 lds\n"
    "v_exp_f32 v92, v92\n"
    "v_exp_f32 v93, v93\n"
    "v_mfma_f32_16x16x32_bf16 v[104:107], a[90:93], v[232:235], 0\n"
    "ds_read_b64_tr_b16 a[124:125], v45 offset:12808\n"
    "v_exp_f32 v94, v94\n"
    "v_exp_f32 v95, v95\n"
    "v_cvt_pk_bf16_f32 v116, v92, v93\n"
    "v_mfma_f32_16x16x32_bf16 v[144:147], a[98:101], v[156:159], v[144:147]\n"
    "ds_read_b64_tr_b16 a[126:127], v45 offset:14088\n"
    "v_cvt_pk_bf16_f32 v117, v94, v95\n"
    "v_exp_f32 v96, v96\n"
    "v_exp_f32 v97, v97\n"
    "v_exp_f32 v98, v98\n"
    "v_exp_f32 v99, v99\n"
    "v_cvt_pk_bf16_f32 v118, v96, v97\n"
    "v_cvt_pk_bf16_f32 v119, v98, v99\n"
    "v_mfma_f32_16x16x32_bf16 v[108:111], a[110:113], v[212:215], 0\n"
    "ds_read_b64 a[76:77], v18 offset:16640\n"
    "v_exp_f32 v100, v100\n"
    "v_exp_f32 v101, v101\n"
    "v_exp_f32 v102, v102\n"
    "v_mfma_f32_16x16x32_bf16 v[132:135], a[102:105], v[116:119], v[132:135]\n"
    "ds_read_b64 a[78:79], v44 offset:16640\n"
    "s_bitcmp1_b32 s77, 0\n"
    "s_cselect_b64 exec, -1, 0\n"
    "buffer_load_dwordx4 v[240:243], v54, s[28:31], s82 offen offset:0 sc1\n"
    "buffer_load_dwordx4 v[244:247], v54, s[28:31], s82 offen offset:1280 sc1\n"
    "s_mov_b64 exec, -1\n"
    "v_mfma_f32_16x16x32_bf16 v[112:115], a[34:37], v[212:215], 0\n"
    "ds_read_b64_tr_b16 a[128:129], v85 offset:12808\n"
    "buffer_load_dwordx4 v24, s[20:23], s84 offen offset:2048 lds\n"
    "v_exp_f32 v103, v103\n"
    "v_cvt_pk_bf16_f32 v120, v100, v101\n"
    "v_cvt_pk_bf16_f32 v121, v102, v103\n"
    "v_mfma_f32_16x16x32_bf16 v[148:151], a[106:109], v[116:119], v[148:151]\n"
    "ds_read_b64_tr_b16 a[130:131], v85 offset:14088\n"
    "v_exp_f32 v104, v104\n"
    "v_exp_f32 v105, v105\n"
    "v_exp_f32 v106, v106\n"
    "v_exp_f32 v107, v107\n"
    "v_cvt_pk_bf16_f32 v122, v104, v105\n"
    "v_cvt_pk_bf16_f32 v123, v106, v107\n"
    "v_mfma_f32_16x16x32_bf16 v[92:95], a[110:113], v[216:219], 0\n"
    "ds_read_b64 a[74:75], v18 offset:15368\n"
    "v_exp_f32 v108, v108\n"
    "v_exp_f32 v109, v109\n"
    "v_exp_f32 v110, v110\n"
    "v_mfma_f32_16x16x32_bf16 v[136:139], a[102:105], v[120:123], v[136:139]\n"
    "ds_read_b64_tr_b16 a[94:95], v45 offset:15360\n"
    "s_add_u32 s81, s59, 0\n"
    "v_exp_f32 v111, v111\n"
    "v_cvt_pk_bf16_f32 v156, v108, v109\n"
    "v_cvt_pk_bf16_f32 v157, v110, v111\n"
    "v_mfma_f32_16x16x32_bf16 v[96:99], a[34:37], v[216:219], 0\n"
    "ds_read_b64_tr_b16 a[96:97], v45 offset:16640\n"
    "v_exp_f32 v112, v112\n"
    "v_exp_f32 v113, v113\n"
    "v_exp_f32 v114, v114\n"
    "v_mfma_f32_16x16x32_bf16 v[152:155], a[106:109], v[120:123], v[152:155]\n"
    "ds_read_b64 a[80:81], v18 offset:16648\n"
    "buffer_load_dwordx4 v75, s[20:23], s84 offen offset:3072 lds\n"
    "v_exp_f32 v115, v115\n"
    "v_cvt_pk_bf16_f32 v158, v112, v113\n"
    "v_cvt_pk_bf16_f32 v159, v114, v115\n"
    "v_mfma_f32_16x16x32_bf16 v[100:103], a[112:115], v[220:223], 0\n"
    "ds_read_b64_tr_b16 a[98:99], v85 offset:15360\n"
    "v_exp_f32 v92, v92\n"
    "v_exp_f32 v93, v93\n"
    "s_waitcnt lgkmcnt(3)\n"
    "v_mfma_f32_16x16x32_bf16 v[124:127], a[116:119], v[156:159], v[124:127]\n"
    "ds_read_b64_tr_b16 a[100:101], v85 offset:16640\n"
    "buffer_load_dwordx4 a[2:5], v28, s[28:31], s81 offen offset:0 sc1\n"
    "v_exp_f32 v94, v94\n"
    "v_exp_f32 v95, v95\n"
    "v_mfma_f32_16x16x32_bf16 v[104:107], a[36:39], v[220:223], 0\n"
    "ds_read_b64 a[82:83], v18 offset:17920\n"
    "v_cvt_pk_bf16_f32 v116, v92, v93\n"
    "v_cvt_pk_bf16_f32 v117, v94, v95\n"
    "v_exp_f32 v96, v96\n"
    "v_exp_f32 v97, v97\n"
    "v_mfma_f32_16x16x32_bf16 v[140:143], a[120:123], v[156:159], v[140:143]\n"
    "ds_read_b64 a[84:85], v44 offset:17920\n"
    "s_add_u32 s89, s41, s43\n"
    "s_add_u32 m0, s86, 65536\n"
    "v_exp_f32 v98, v98\n"
    "v_exp_f32 v99, v99\n"
    "v_cvt_pk_bf16_f32 v118, v96, v97\n"
    "v_cvt_pk_bf16_f32 v119, v98, v99\n"
    "v_mfma_f32_16x16x32_bf16 v[108:111], a[112:115], v[232:235], 0\n"
    "ds_read_b64_tr_b16 a[102:103], v45 offset:15368\n"
    "buffer_load_dwordx4 v56, s[12:15], s89 offen offset:0 lds\n"
    "v_exp_f32 v100, v100\n"
    "v_exp_f32 v101, v101\n"
    "v_mfma_f32_16x16x32_bf16 v[128:131], a[116:119], v[116:119], v[128:131]\n"
    "ds_read_b64_tr_b16 a[104:105], v45 offset:16648\n"
    "v_exp_f32 v102, v102\n"
    "v_exp_f32 v103, v103\n"
    "v_cvt_pk_bf16_f32 v120, v100, v101\n"
    "v_mfma_f32_16x16x32_bf16 v[112:115], a[36:39], v[232:235], 0\n"
    "ds_read_b64 a[88:89], v18 offset:19200\n"
    "v_cvt_pk_bf16_f32 v121, v102, v103\n"
    "v_exp_f32 v104, v104\n"
    "v_exp_f32 v105, v105\n"
    "v_mfma_f32_16x16x32_bf16 v[144:147], a[120:123], v[116:119], v[144:147]\n"
    "ds_read_b64 a[90:91], v44 offset:19200\n"
    "buffer_load_dwordx4 a[20:23], v28, s[28:31], s81 offen offset:1024 sc1\n"
    "v_exp_f32 v106, v106\n"
    "v_exp_f32 v107, v107\n"
    "v_cvt_pk_bf16_f32 v122, v104, v105\n"
    "v_cvt_pk_bf16_f32 v123, v106, v107\n"
    "v_mfma_f32_16x16x32_bf16 v[92:95], a[70:73], v[212:215], 0\n"
    "ds_read_b64_tr_b16 a[106:107], v85 offset:15368\n"
    "v_exp_f32 v108, v108\n"
    "v_exp_f32 v109, v109\n"
    "v_exp_f32 v110, v110\n"
    "v_mfma_f32_16x16x32_bf16 v[132:135], a[124:127], v[120:123], v[132:135]\n"
    "ds_read_b64_tr_b16 a[108:109], v85 offset:16648\n"
    "v_exp_f32 v111, v111\n"
    "v_cvt_pk_bf16_f32 v156, v108, v109\n"
    "v_cvt_pk_bf16_f32 v157, v110, v111\n"
    "v_exp_f32 v112, v112\n"
    "v_mfma_f32_16x16x32_bf16 v[96:99], a[76:79], v[212:215], 0\n"
    "ds_read_b64 a[86:87], v18 offset:17928\n"
    "s_waitcnt vmcnt(7) lgkmcnt(3)\n"
    "buffer_store_dword a42, v19, s[28:31], s90 offen offset:256 sc1\n"
    "v_exp_f32 v113, v113\n"
    "v_mfma_f32_16x16x32_bf16 v[148:151], a[128:131], v[120:123], v[148:151]\n"
    "ds_read_b64_tr_b16 a[116:117], v45 offset:17920\n"
    "v_exp_f32 v114, v114\n"
    "v_exp_f32 v115, v115\n"
    "v_cvt_pk_bf16_f32 v158, v112, v113\n"
    "v_cvt_pk_bf16_f32 v159, v114, v115\n"
    "v_mfma_f32_16x16x32_bf16 v[100:103], a[70:73], v[216:219], 0\n"
    "ds_read_b64_tr_b16 a[118:119], v45 offset:19200\n"
    "v_exp_f32 v92, v92\n"
    "v_exp_f32 v93, v93\n"
    "v_exp_f32 v94, v94\n"
    "v_mfma_f32_16x16x32_bf16 v[136:139], a[124:127], v[156:159], v[136:139]\n"
    "ds_read_b64 a[92:93], v18 offset:19208\n"
    "v_exp_f32 v95, v95\n"
    "v_cvt_pk_bf16_f32 v116, v92, v93\n"
    "v_cvt_pk_bf16_f32 v117, v94, v95\n"
    "v_exp_f32 v96, v96\n"
    "v_mfma_f32_16x16x32_bf16 v[104:107], a[76:79], v[216:219], 0\n"
    "ds_read_b64_tr_b16 a[120:121], v85 offset:17920\n"
    "buffer_load_dwordx4 a[132:135], v28, s[28:31], s81 offen offset:2048 sc1\n"
    "v_exp_f32 v97, v97\n"
    "v_exp_f32 v98, v98\n"
    "v_mfma_f32_16x16x32_bf16 v[152:155], a[128:131], v[156:159], v[152:155]\n"
    "ds_read_b64_tr_b16 a[122:123], v85 offset:19200\n"
    "s_add_u32 s84, s37, s33\n"
    "v_exp_f32 v99, v99\n"
    "v_cvt_pk_bf16_f32 v118, v96, v97\n"
    "v_cvt_pk_bf16_f32 v119, v98, v99\n"
    "v_mfma_f32_16x16x32_bf16 v[108:111], a[72:75], v[220:223], 0\n"
    "ds_read_b64 a[110:111], v18 offset:20480\n"
    "s_add_u32 m0, s86, 81920\n"
    "v_exp_f32 v100, v100\n"
    "v_exp_f32 v101, v101\n"
    "v_mfma_f32_16x16x32_bf16 v[124:127], a[94:97], v[116:119], v[124:127]\n"
    "ds_read_b64 a[112:113], v44 offset:20480\n"
    "buffer_load_dwordx4 v56, s[20:23], s84 offen offset:0 lds\n"
    "v_exp_f32 v102, v102\n"
    "v_exp_f32 v103, v103\n"
    "v_mfma_f32_16x16x32_bf16 v[112:115], a[78:81], v[220:223], 0\n"
    "ds_read_b64_tr_b16 a[124:125], v45 offset:17928\n"
    "v_cvt_pk_bf16_f32 v120, v100, v101\n"
    "v_cvt_pk_bf16_f32 v121, v102, v103\n"
    "v_exp_f32 v104, v104\n"
    "v_exp_f32 v105, v105\n"
    "v_mfma_f32_16x16x32_bf16 v[140:143], a[98:101], v[116:119], v[140:143]\n"
    "ds_read_b64_tr_b16 a[126:127], v45 offset:19208\n"
    "v_exp_f32 v106, v106\n"
    "v_exp_f32 v107, v107\n"
    "v_cvt_pk_bf16_f32 v122, v104, v105\n"
    "v_cvt_pk_bf16_f32 v123, v106, v107\n"
    "v_mfma_f32_16x16x32_bf16 v[92:95], a[72:75], v[232:235], 0\n"
    "ds_read_b64_tr_b16 a[128:129], v85 offset:17928\n"
    "v_exp_f32 v108, v108\n"
    "v_exp_f32 v109, v109\n"
    "v_exp_f32 v110, v110\n"
    "v_mfma_f32_16x16x32_bf16 v[128:131], a[94:97], v[120:123], v[128:131]\n"
    "ds_read_b64_tr_b16 a[130:131], v85 offset:19208\n"
    "buffer_load_dwordx4 a[136:139], v28, s[28:31], s81 offen offset:3072 sc1\n"
    "v_exp_f32 v111, v111\n"
    "v_cvt_pk_bf16_f32 v156, v108, v109\n"
    "v_cvt_pk_bf16_f32 v157, v110, v111\n"
    "v_mfma_f32_16x16x32_bf16 v[96:99], a[78:81], v[232:235], 0\n"
    "ds_read_b64 a[34:35], v18 offset:21760\n"
    "v_exp_f32 v112, v112\n"
    "v_exp_f32 v113, v113\n"
    "v_exp_f32 v114, v114\n"
    "v_mfma_f32_16x16x32_bf16 v[144:147], a[98:101], v[120:123], v[144:147]\n"
    "ds_read_b64 a[36:37], v44 offset:21760\n"
    "buffer_load_dwordx4 v57, s[20:23], s84 offen offset:1024 lds\n"
    "v_exp_f32 v115, v115\n"
    "v_cvt_pk_bf16_f32 v158, v112, v113\n"
    "v_cvt_pk_bf16_f32 v159, v114, v115\n"
    "v_mfma_f32_16x16x32_bf16 v[100:103], a[82:85], v[212:215], 0\n"
    "ds_read_b64 a[114:115], v18 offset:20488\n"
    "v_exp_f32 v92, v92\n"
    "v_exp_f32 v93, v93\n"
    "v_exp_f32 v94, v94\n"
    "v_mfma_f32_16x16x32_bf16 v[132:135], a[102:105], v[156:159], v[132:135]\n"
    "ds_read_b64_tr_b16 a[94:95], v45 offset:20480\n"
    "s_add_u32 s81, s59, 4096\n"
    "v_exp_f32 v95, v95\n"
    "v_cvt_pk_bf16_f32 v116, v92, v93\n"
    "v_cvt_pk_bf16_f32 v117, v94, v95\n"
    "v_mfma_f32_16x16x32_bf16 v[104:107], a[88:91], v[212:215], 0\n"
    "ds_read_b64_tr_b16 a[96:97], v45 offset:21760\n"
    "buffer_load_dwordx4 v24, s[20:23], s84 offen offset:2048 lds\n"
    "v_exp_f32 v96, v96\n"
    "v_exp_f32 v97, v97\n"
    "s_waitcnt lgkmcnt(3)\n"
    "v_mfma_f32_16x16x32_bf16 v[148:151], a[106:109], v[156:159], v[148:151]\n"
    "ds_read_b64_tr_b16 a[98:99], v85 offset:20480\n"
    "v_exp_f32 v98, v98\n"
    "v_exp_f32 v99, v99\n"
    "v_cvt_pk_bf16_f32 v118, v96, v97\n"
    "v_cvt_pk_bf16_f32 v119, v98, v99\n"
    "v_mfma_f32_16x16x32_bf16 v[108:111], a[82:85], v[216:219], 0\n"
    "ds_read_b64_tr_b16 a[100:101], v85 offset:21760\n"
    "buffer_load_dwordx4 a[140:143], v28, s[28:31], s81 offen offset:0 sc1\n"
    "v_exp_f32 v100, v100\n"
    "v_exp_f32 v101, v101\n"
    "v_mfma_f32_16x16x32_bf16 v[136:139], a[102:105], v[116:119], v[136:139]\n"
    "ds_read_b64 a[38:39], v18 offset:21768\n"
    "v_exp_f32 v102, v102\n"
    "v_exp_f32 v103, v103\n"
    "v_cvt_pk_bf16_f32 v120, v100, v101\n"
    "v_mfma_f32_16x16x32_bf16 v[112:115], a[88:91], v[216:219], 0\n"
    "ds_read_b64_tr_b16 a[102:103], v45 offset:20488\n"
    "v_cvt_pk_bf16_f32 v121, v102, v103\n"
    "v_exp_f32 v104, v104\n"
    "v_exp_f32 v105, v105\n"
    "v_mfma_f32_16x16x32_bf16 v[152:155], a[106:109], v[116:119], v[152:155]\n"
    "ds_read_b64_tr_b16 a[104:105], v45 offset:21768\n"
    "buffer_load_dwordx4 v75, s[20:23], s84 offen offset:3072 lds\n"
    "v_exp_f32 v106, v106\n"
    "v_exp_f32 v107, v107\n"
    "v_cvt_pk_bf16_f32 v122, v104, v105\n"
    "v_cvt_pk_bf16_f32 v123, v106, v107\n"
    "v_mfma_f32_16x16x32_bf16 v[92:95], a[84:87], v[220:223], 0\n"
    "ds_read_b64_tr_b16 a[106:107], v85 offset:20488\n"
    "v_exp_f32 v108, v108\n"
    "v_exp_f32 v109, v109\n"
    "v_exp_f32 v110, v110\n"
    "v_mfma_f32_16x16x32_bf16 v[124:127], a[116:119], v[120:123], v[124:127]\n"
    "ds_read_b64_tr_b16 a[108:109], v85 offset:21768\n"
    "v_exp_f32 v111, v111\n"
    "v_cvt_pk_bf16_f32 v156, v108, v109\n"
    "v_cvt_pk_bf16_f32 v157, v110, v111\n"
    "v_exp_f32 v112, v112\n"
    "v_mfma_f32_16x16x32_bf16 v[96:99], a[90:93], v[220:223], 0\n"
    "v_exp_f32 v113, v113\n"
    "v_exp_f32 v114, v114\n"
    "v_exp_f32 v115, v115\n"
    "v_mfma_f32_16x16x32_bf16 v[140:143], a[120:123], v[120:123], v[140:143]\n"
    "buffer_load_dwordx4 a[144:147], v28, s[28:31], s81 offen offset:1024 sc1\n"
    "s_add_u32 s89, s39, s33\n"
    "s_add_u32 m0, s86, 86016\n"
    "v_cvt_pk_bf16_f32 v158, v112, v113\n"
    "v_cvt_pk_bf16_f32 v159, v114, v115\n"
    "v_exp_f32 v92, v92\n"
    "v_mfma_f32_16x16x32_bf16 v[100:103], a[84:87], v[232:235], 0\n"
    "v_exp_f32 v93, v93\n"
    "v_exp_f32 v94, v94\n"
    "v_exp_f32 v95, v95\n"
    "v_mfma_f32_16x16x32_bf16 v[128:131], a[116:119], v[156:159], v[128:131]\n"
    "buffer_load_dwordx4 v56, s[20:23], s89 offen offset:0 lds\n"
    "v_cvt_pk_bf16_f32 v116, v92, v93\n"
    "v_cvt_pk_bf16_f32 v117, v94, v95\n"
    "v_exp_f32 v96, v96\n"
    "v_exp_f32 v97, v97\n"
    "v_mfma_f32_16x16x32_bf16 v[104:107], a[90:93], v[232:235], 0\n"
    "ds_read_b64_tr_b16 a[198:199], v42 offset:0\n"
    "v_exp_f32 v98, v98\n"
    "v_exp_f32 v99, v99\n"
    "v_cvt_pk_bf16_f32 v118, v96, v97\n"
    "v_mfma_f32_16x16x32_bf16 v[144:147], a[120:123], v[156:159], v[144:147]\n"
    "ds_read_b64_tr_b16 a[200:201], v42 offset:10240\n"
    "v_cvt_pk_bf16_f32 v119, v98, v99\n"
    "v_exp_f32 v100, v100\n"
    "v_exp_f32 v101, v101\n"
    "v_mfma_f32_16x16x32_bf16 v[108:111], a[110:113], v[212:215], v[248:251]\n"
    "ds_read_b64_tr_b16 a[202:203], v42 offset:1024\n"
    "v_exp_f32 v102, v102\n"
    "v_exp_f32 v103, v103\n"
    "v_cvt_pk_bf16_f32 v120, v100, v101\n"
    "v_mfma_f32_16x16x32_bf16 v[132:135], a[124:127], v[116:119], v[132:135]\n"
    "ds_read_b64_tr_b16 a[204:205], v42 offset:11264\n"
    "v_cvt_pk_bf16_f32 v121, v102, v103\n"
    "v_exp_f32 v104, v104\n"
    "v_exp_f32 v105, v105\n"
    "v_mfma_f32_16x16x32_bf16 v[148:151], a[128:131], v[116:119], v[148:151]\n"
    "ds_read_b64_tr_b16 a[206:207], v42 offset:2048\n"
    "buffer_load_dword v43, v25, s[28:31], s99 offen offset:256 sc1\n"
    "v_exp_f32 v106, v106\n"
    "v_exp_f32 v107, v107\n"
    "v_cvt_pk_bf16_f32 v122, v104, v105\n"
    "v_cvt_pk_bf16_f32 v123, v106, v107\n"
    "v_mfma_f32_16x16x32_bf16 v[92:95], a[110:113], v[216:219], 0\n"
    "ds_read_b64_tr_b16 a[208:209], v42 offset:12288\n"
    "v_exp_f32 v108, v108\n"
    "v_exp_f32 v109, v109\n"
    "v_exp_f32 v110, v110\n"
    "v_mfma_f32_16x16x32_bf16 v[136:139], a[124:127], v[120:123], v[136:139]\n"
    "ds_read_b64_tr_b16 a[210:211], v42 offset:3072\n"
    "v_exp_f32 v111, v111\n"
    "v_cvt_pk_bf16_f32 v156, v108, v109\n"
    "v_cvt_pk_bf16_f32 v157, v110, v111\n"
    "v_mov_b32 v158, 0\n"
    "v_mfma_f32_16x16x32_bf16 v[96:99], a[34:37], v[216:219], v[248:251]\n"
    "ds_read_b64_tr_b16 a[212:213], v42 offset:13312\n"
    "v_mov_b32 v159, 0\n"
    "v_exp_f32 v92, v92\n"
    "v_exp_f32 v93, v93\n"
    "v_mfma_f32_16x16x32_bf16 v[152:155], a[128:131], v[120:123], v[152:155]\n"
    "ds_read_b64_tr_b16 a[214:215], v42 offset:4096\n"
    "v_exp_f32 v94, v94\n"
    "v_exp_f32 v95, v95\n"
    "s_waitcnt lgkmcnt(3)\n"
    "v_mfma_f32_16x16x32_bf16 v[100:103], a[112:115], v[220:223], v[248:251]\n"
    "ds_read_b64_tr_b16 a[216:217], v42 offset:14336\n"
    "v_cvt_pk_bf16_f32 v116, v92, v93\n"
    "v_cvt_pk_bf16_f32 v117, v94, v95\n"
    "v_exp_f32 v96, v96\n"
    "v_exp_f32 v97, v97\n"
    "v_mfma_f32_16x16x32_bf16 v[124:127], a[94:97], v[156:159], v[124:127]\n"
    "ds_read_b64_tr_b16 a[218:219], v87 offset:0\n"
    "v_exp_f32 v98, v98\n"
    "v_exp_f32 v99, v99\n"
    "v_cvt_pk_bf16_f32 v118, v96, v97\n"
    "v_mfma_f32_16x16x32_bf16 v[140:143], a[98:101], v[156:159], v[140:143]\n"
    "ds_read_b64_tr_b16 a[220:221], v87 offset:10240\n"
    "v_cvt_pk_bf16_f32 v119, v98, v99\n"
    "v_exp_f32 v100, v100\n"
    "v_exp_f32 v101, v101\n"
    "v_mfma_f32_16x16x32_bf16 v[108:111], a[112:115], v[232:235], 0\n"
    "ds_read_b64_tr_b16 a[222:223], v87 offset:1024\n"
    "s_waitcnt vmcnt(13) lgkmcnt(4)\n"
    "v_exp_f32 v102, v102\n"
    "v_exp_f32 v103, v103\n"
    "v_mfma_f32_16x16x32_bf16 v[128:131], a[94:97], v[116:119], v[128:131]\n"
    "ds_read_b64_tr_b16 a[224:225], v87 offset:11264\n"
    "v_cvt_pk_bf16_f32 v120, v100, v101\n"
    "v_cvt_pk_bf16_f32 v121, v102, v103\n"
    "v_mov_b32 v122, 0\n"
    "v_mov_b32 v123, 0\n"
    "v_exp_f32 v108, v108\n"
    "v_mfma_f32_16x16x32_bf16 v[112:115], a[36:39], v[232:235], v[248:251]\n"
    "ds_read_b64_tr_b16 a[226:227], v87 offset:2048\n"
    "v_exp_f32 v109, v109\n"
    "v_exp_f32 v110, v110\n"
    "v_exp_f32 v111, v111\n"
    "v_mfma_f32_16x16x32_bf16 v[144:147], a[98:101], v[116:119], v[144:147]\n"
    "ds_read_b64_tr_b16 a[228:229], v87 offset:12288\n"
    "v_cvt_pk_bf16_f32 v156, v108, v109\n"
    "v_cvt_pk_bf16_f32 v157, v110, v111\n"
    "v_exp_f32 v112, v112\n"
    "v_exp_f32 v113, v113\n"
    "v_mfma_f32_16x16x32_bf16 v[132:135], a[102:105], v[120:123], v[132:135]\n"
    "ds_read_b64_tr_b16 a[230:231], v87 offset:3072\n"
    "v_exp_f32 v114, v114\n"
    "v_exp_f32 v115, v115\n"
    "v_cvt_pk_bf16_f32 v158, v112, v113\n"
    "v_cvt_pk_bf16_f32 v159, v114, v115\n"
    "v_mfma_f32_16x16x32_bf16 v[148:151], a[106:109], v[120:123], v[148:151]\n"
    "ds_read_b64_tr_b16 a[232:233], v87 offset:13312\n"
    "v_subrev_u32 v15, 0x21800000, v142\n"
    "v_subrev_u32 v16, 0x21800000, v146\n"
    "v_mul_f32 v143, v142, v84\n"
    "v_rcp_f32 v143, v143\n"
    "v_mfma_f32_16x16x32_bf16 v[136:139], a[102:105], v[156:159], v[136:139]\n"
    "ds_read_b64_tr_b16 a[234:235], v87 offset:4096\n"
    "v_mul_f32 v124, v124, v143\n"
    "v_subrev_u32 v17, 0x21800000, v150\n"
    "v_max3_u32 v16, v15, v16, v17\n"
    "v_mul_f32 v125, v125, v143\n"
    "v_mul_f32 v126, v126, v143\n"
    "v_mfma_f32_16x16x32_bf16 v[152:155], a[106:109], v[156:159], v[152:155]\n"
    "ds_read_b64_tr_b16 a[236:237], v87 offset:14336\n"
    "v_mul_f32 v127, v127, v143\n"
    "v_cvt_pk_bf16_f32 v160, v124, v125\n"
    "v_cvt_pk_bf16_f32 v161, v126, v127\n"
    "v_mul_f32 v151, v150, v84\n"
    "v_rcp_f32 v151, v151\n"
    "v_mfma_f32_16x16x32_bf16 v[92:95], a[198:201], v[34:37], 0\n"
    "ds_read_b64_tr_b16 a[238:239], v89 offset:0\n"
    "v_subrev_u32 v17, 0x21800000, v154\n"
    "v_mfma_f32_16x16x32_bf16 v[92:95], a[202:205], v[50:53], v[92:95]\n"
    "ds_read_b64_tr_b16 a[240:241], v89 offset:10240\n"
    "v_max_u32 v16, v16, v17\n"
    "v_mfma_f32_16x16x32_bf16 v[92:95], a[206:209], v[80:83], v[92:95]\n"
    "ds_read_b64_tr_b16 a[242:243], v89 offset:1024\n"
    "v_cmp_lt_u32 vcc, 0x46000000, v16\n"
    "v_mfma_f32_16x16x32_bf16 v[92:95], a[210:213], v[20:23], v[92:95]\n"
    "ds_read_b64_tr_b16 a[244:245], v89 offset:11264\n"
    "s_waitcnt lgkmcnt(5)\n"
    "v_mfma_f32_16x16x32_bf16 v[92:95], a[214:217], v[224:227], v[92:95]\n"
    "ds_read_b64_tr_b16 a[246:247], v89 offset:2048\n"
    "s_cbranch_vccnz L_fb_%=\n"
    "L_fb_ret_%=:\n"
    "v_mfma_f32_16x16x32_bf16 v[108:111], a[198:201], v[46:49], 0\n"
    "ds_read_b64_tr_b16 a[248:249], v89 offset:12288\n"
    "v_mul_f32 v132, v132, v151\n"
    "v_mfma_f32_16x16x32_bf16 v[108:111], a[202:205], v[76:79], v[108:111]\n"
    "ds_read_b64_tr_b16 a[250:251], v89 offset:3072\n"
    "v_mul_f32 v133, v133, v151\n"
    "v_mfma_f32_16x16x32_bf16 v[108:111], a[206:209], v[10:13], v[108:111]\n"
    "ds_read_b64_tr_b16 a[252:253], v89 offset:13312\n"
    "v_mul_f32 v134, v134, v151\n"
    "v_mfma_f32_16x16x32_bf16 v[108:111], a[210:213], v[192:195], v[108:111]\n"
    "ds_read_b64_tr_b16 a[190:191], v89 offset:4096\n"
    "v_mul_f32 v135, v135, v151\n"
    "v_mfma_f32_16x16x32_bf16 v[108:111], a[214:217], v[228:231], v[108:111]\n"
    "ds_read_b64_tr_b16 a[192:193], v89 offset:14336\n"
    "v_cvt_pk_bf16_f32 v162, v132, v133\n"
    "v_mfma_f32_16x16x32_bf16 v[96:99], a[218:221], v[34:37], 0\n"
    "ds_read_b64_tr_b16 a[194:195], v90 offset:0\n"
    "v_cvt_pk_bf16_f32 v163, v134, v135\n"
    "v_mfma_f32_16x16x32_bf16 v[96:99], a[222:225], v[50:53], v[96:99]\n"
    "ds_read_b64_tr_b16 a[196:197], v90 offset:10240\n"
    "s_add_u32 s98, s58, 16384\n"
    "v_mfma_f32_16x16x32_bf16 v[96:99], a[226:229], v[80:83], v[96:99]\n"
    "buffer_store_dwordx4 v[160:163], v28, s[28:31], s98 offen offset:2048 sc1\n"
    "ds_read_b64_tr_b16 a[198:199], v90 offset:1024\n"
    "v_mfma_f32_16x16x32_bf16 v[96:99], a[230:233], v[20:23], v[96:99]\n"
    "ds_read_b64_tr_b16 a[200:201], v90 offset:11264\n"
    "s_waitcnt lgkmcnt(6)\n"
    "v_mfma_f32_16x16x32_bf16 v[96:99], a[234:237], v[224:227], v[96:99]\n"
    "ds_read_b64_tr_b16 a[202:203], v90 offset:2048\n"
    "v_mul_f32 v147, v146, v88\n"
    "v_mfma_f32_16x16x32_bf16 v[112:115], a[218:221], v[46:49], 0\n"
    "ds_read_b64_tr_b16 a[204:205], v90 offset:12288\n"
    "v_rcp_f32 v147, v147\n"
    "v_mfma_f32_16x16x32_bf16 v[112:115], a[222:225], v[76:79], v[112:115]\n"
    "ds_read_b64_tr_b16 a[206:207], v90 offset:3072\n"
    "v_mul_f32 v128, v128, v147\n"
    "v_mfma_f32_16x16x32_bf16 v[112:115], a[226:229], v[10:13], v[112:115]\n"
    "ds_read_b64_tr_b16 a[208:209], v90 offset:13312\n"
    "v_mul_f32 v129, v129, v147\n"
    "v_mfma_f32_16x16x32_bf16 v[112:115], a[230:233], v[192:195], v[112:115]\n"
    "ds_read_b64_tr_b16 a[210:211], v90 offset:4096\n"
    "s_waitcnt vmcnt(13) lgkmcnt(7)\n"
    "v_mfma_f32_16x16x32_bf16 v[112:115], a[234:237], v[228:231], v[112:115]\n"
    "s_barrier\n"
    "ds_read_b64_tr_b16 a[212:213], v90 offset:14336\n"
    "v_mfma_f32_16x16x32_bf16 v[100:103], a[238:241], v[34:37], 0\n"
    "ds_read_b64_tr_b16 a[214:215], v42 offset:20480\n"
    "v_mul_f32 v130, v130, v147\n"
    "v_mfma_f32_16x16x32_bf16 v[100:103], a[242:245], v[50:53], v[100:103]\n"
    "ds_read_b64_tr_b16 a[216:217], v42 offset:30720\n"
    "v_mul_f32 v131, v131, v147\n"
    "v_mfma_f32_16x16x32_bf16 v[100:103], a[246:249], v[80:83], v[100:103]\n"
    "ds_read_b64_tr_b16 a[218:219], v42 offset:21504\n"
    "v_cvt_pk_bf16_f32 v66, v128, v129\n"
    "v_mfma_f32_16x16x32_bf16 v[100:103], a[250:253], v[20:23], v[100:103]\n"
    "ds_read_b64_tr_b16 a[220:221], v42 offset:31744\n"
    "v_cvt_pk_bf16_f32 v67, v130, v131\n"
    "v_mfma_f32_16x16x32_bf16 v[100:103], a[190:193], v[224:227], v[100:103]\n"
    "ds_read_b64_tr_b16 a[222:223], v42 offset:22528\n"
    "v_mul_f32 v155, v154, v88\n"
    "v_mfma_f32_16x16x32_bf16 v[116:119], a[238:241], v[46:49], 0\n"
    "ds_read_b64_tr_b16 a[224:225], v42 offset:32768\n"
    "v_rcp_f32 v155, v155\n"
    "v_mfma_f32_16x16x32_bf16 v[116:119], a[242:245], v[76:79], v[116:119]\n"
    "ds_read_b64_tr_b16 a[226:227], v42 offset:23552\n"
    "s_waitcnt vmcnt(1) lgkmcnt(7)\n"
    "v_cmp_eq_u32 vcc, 0, v43\n"
    "v_mfma_f32_16x16x32_bf16 v[116:119], a[246:249], v[10:13], v[116:119]\n"
    "ds_read_b64_tr_b16 a[228:229], v42 offset:33792\n"
    "v_mul_f32 v136, v136, v155\n"
    "v_mul_f32 v137, v137, v155\n"
    "v_mul_f32 v138, v138, v155\n"
    "v_mul_f32 v139, v139, v155\n"
    "v_cvt_pk_bf16_f32 v68, v136, v137\n"
    "v_cvt_pk_bf16_f32 v69, v138, v139\n"
    "v_mfma_f32_16x16x32_bf16 v[116:119], a[250:253], v[192:195], v[116:119]\n"
    "buffer_store_dwordx4 v[66:69], v28, s[28:31], s98 offen offset:3072 sc1\n"
    "ds_read_b64_tr_b16 a[230:231], v42 offset:24576\n"
    "s_add_u32 s84, s38, s32\n"
    "s_add_u32 m0, s86, 102400\n"
    "v_mfma_f32_16x16x32_bf16 v[116:119], a[190:193], v[228:231], v[116:119]\n"
    "ds_read_b64_tr_b16 a[232:233], v42 offset:34816\n"
    "s_cbranch_vccnz L_h2_%=\n"
    "L_h2_ret_%=:\n"
    "v_mfma_f32_16x16x32_bf16 v[104:107], a[194:197], v[34:37], 0\n"
    "ds_read_b64_tr_b16 a[234:235], v87 offset:20480\n"
    "v_mul_f32 v140, v140, v143\n"
    "v_mfma_f32_16x16x32_bf16 v[104:107], a[198:201], v[50:53], v[104:107]\n"
    "ds_read_b64_tr_b16 a[236:237], v87 offset:30720\n"
    "buffer_load_dwordx4 v56, s[20:23], s84 offen offset:0 lds\n"
    "v_mfma_f32_16x16x32_bf16 v[104:107], a[202:205], v[80:83], v[104:107]\n"
    "ds_read_b64_tr_b16 a[238:239], v87 offset:21504\n"
    "v_mul_f32 v141, v141, v143\n"
    "v_mfma_f32_16x16x32_bf16 v[104:107], a[206:209], v[20:23], v[104:107]\n"
    "ds_read_b64_tr_b16 a[240:241], v87 offset:31744\n"
    "v_cvt_pk_bf16_f32 v70, v140, v141\n"
    "v_mfma_f32_16x16x32_bf16 v[104:107], a[210:213], v[224:227], v[104:107]\n"
    "ds_read_b64_tr_b16 a[242:243], v87 offset:22528\n"
    "buffer_load_dwordx4 a[148:151], v28, s[28:31], s81 offen offset:2048 sc1\n"
    "v_mfma_f32_16x16x32_bf16 v[120:123], a[194:197], v[46:49], 0\n"
    "ds_read_b64_tr_b16 a[244:245], v87 offset:32768\n"
    "v_mul_f32 v148, v148, v151\n"
    "v_mfma_f32_16x16x32_bf16 v[120:123], a[198:201], v[76:79], v[120:123]\n"
    "ds_read_b64_tr_b16 a[246:247], v87 offset:23552\n"
    "v_mul_f32 v149, v149, v151\n"
    "v_cvt_pk_bf16_f32 v71, v148, v149\n"
    "v_mul_f32 v144, v144, v147\n"
    "v_mul_f32 v145, v145, v147\n"
    "v_cvt_pk_bf16_f32 v72, v144, v145\n"
    "v_mul_f32 v152, v152, v155\n"
    "v_mul_f32 v153, v153, v155\n"
    "v_cvt_pk_bf16_f32 v73, v152, v153\n"
    "v_accvgpr_read_b32 v74, a33\n"
    "s_add_u32 s98, s58, 20480\n"
    "v_mfma_f32_16x16x32_bf16 v[120:123], a[202:205], v[10:13], v[120:123]\n"
    "buffer_store_dwordx4 v[70:73], v74, s[28:31], s98 offen offset:0 sc1\n"
    "ds_read_b64_tr_b16 a[248:249], v87 offset:33792\n"
    "v_mfma_f32_16x16x32_bf16 v[120:123], a[206:209], v[192:195], v[120:123]\n"
    "ds_read_b64_tr_b16 a[250:251], v87 offset:24576\n"
    "s_add_u32 s89, s40, s32\n"
    "v_mfma_f32_16x16x32_bf16 v[120:123], a[210:213], v[228:231], v[120:123]\n"
    "ds_read_b64_tr_b16 a[252:253], v87 offset:34816\n"
    "s_waitcnt lgkmcnt(4)\n"
    "v_mfma_f32_16x16x32_bf16 v[92:95], a[214:217], v[196:199], v[92:95]\n"
    "ds_read_b64_tr_b16 a[190:191], v89 offset:20480\n"
    "v_add_u32 v91, 65536, v42\n"
    "v_mfma_f32_16x16x32_bf16 v[92:95], a[218:221], v[204:207], v[92:95]\n"
    "ds_read_b64_tr_b16 a[192:193], v89 offset:30720\n"
    "buffer_load_dwordx4 v57, s[20:23], s84 offen offset:1024 lds\n"
    "v_mfma_f32_16x16x32_bf16 v[92:95], a[222:225], v[252:255], v[92:95]\n"
    "ds_read_b64_tr_b16 a[194:195], v89 offset:21504\n"
    "v_add_u32 v86, 65536, v87\n"
    "v_mfma_f32_16x16x32_bf16 v[92:95], a[226:229], v[6:9], v[92:95]\n"
    "ds_read_b64_tr_b16 a[196:197], v89 offset:31744\n"
    "v_add_u32 v27, 65536, v89\n"
    "v_mfma_f32_16x16x32_bf16 v[92:95], a[230:233], v[160:163], v[92:95]\n"
    "ds_read_b64_tr_b16 a[198:199], v89 offset:22528\n"
    "buffer_load_dwordx4 v24, s[20:23], s84 offen offset:2048 lds\n"
    "v_mfma_f32_16x16x32_bf16 v[108:111], a[214:217], v[200:203], v[108:111]\n"
    ".p2align 12\n"
    "L_pg8_%=:\n"
    "s_cbranch_execz L_tramp_%=\n"
    "ds_read_b64_tr_b16 a[200:201], v89 offset:32768\n"
    "v_add_u32 v64, 65536, v90\n"
    "v_mfma_f32_16x16x32_bf16 v[108:111], a[218:221], v[208:211], v[108:111]\n"
    "ds_read_b64_tr_b16 a[202:203], v89 offset:23552\n"
    "v_accvgpr_read_b32 v54, a27\n"
    "v_mfma_f32_16x16x32_bf16 v[108:111], a[222:225], v[2:5], v[108:111]\n"
    "ds_read_b64_tr_b16 a[204:205], v89 offset:33792\n"
    "buffer_load_dwordx4 a[152:155], v28, s[28:31], s81 offen offset:3072 sc1\n"
    "v_mfma_f32_16x16x32_bf16 v[108:111], a[226:229], v[38:41], v[108:111]\n"
    "ds_read_b64_tr_b16 a[206:207], v89 offset:24576\n"
    "s_add_u32 s81, s59, 8192\n"
    "v_mfma_f32_16x16x32_bf16 v[108:111], a[230:233], v[66:69], v[108:111]\n"
    "ds_read_b64_tr_b16 a[208:209], v89 offset:34816\n"
    "s_add_u32 s83, s52, 16384\n"
    "v_mfma_f32_16x16x32_bf16 v[96:99], a[234:237], v[196:199], v[96:99]\n"
    "ds_read_b64_tr_b16 a[210:211], v90 offset:20480\n"
    "buffer_load_dwordx4 v75, s[20:23], s84 offen offset:3072 lds\n"
    "v_mfma_f32_16x16x32_bf16 v[96:99], a[238:241], v[204:207], v[96:99]\n"
    "ds_read_b64_tr_b16 a[212:213], v90 offset:30720\n"
    "s_add_u32 m0, s86, 106496\n"
    "v_mfma_f32_16x16x32_bf16 v[96:99], a[242:245], v[252:255], v[96:99]\n"
    "ds_read_b64_tr_b16 a[214:215], v90 offset:21504\n"
    "s_waitcnt lgkmcnt(7)\n"
    "v_mfma_f32_16x16x32_bf16 v[96:99], a[246:249], v[6:9], v[96:99]\n"
    "ds_read_b64_tr_b16 a[216:217], v90 offset:31744\n"
    "s_add_u32 s84, s37, s33\n"
    "v_mfma_f32_16x16x32_bf16 v[96:99], a[250:253], v[160:163], v[96:99]\n"
    "ds_read_b64_tr_b16 a[218:219], v90 offset:22528\n"
    "buffer_load_dwordx4 v56, s[20:23], s89 offen offset:0 lds\n"
    "v_mfma_f32_16x16x32_bf16 v[112:115], a[234:237], v[200:203], v[112:115]\n"
    "ds_read_b64_tr_b16 a[220:221], v90 offset:32768\n"
    "s_add_u32 s84, s84, 147456\n"
    "v_mfma_f32_16x16x32_bf16 v[112:115], a[238:241], v[208:211], v[112:115]\n"
    "ds_read_b64_tr_b16 a[222:223], v90 offset:23552\n"
    "s_add_u32 s89, s41, s33\n"
    "v_mfma_f32_16x16x32_bf16 v[112:115], a[242:245], v[2:5], v[112:115]\n"
    "ds_read_b64_tr_b16 a[224:225], v90 offset:33792\n"
    "v_and_b32 v74, 255, v28\n"
    "v_mfma_f32_16x16x32_bf16 v[112:115], a[246:249], v[38:41], v[112:115]\n"
    "ds_read_b64_tr_b16 a[226:227], v90 offset:24576\n"
    "v_add_u32 v29, 131072, v42\n"
    "v_mfma_f32_16x16x32_bf16 v[112:115], a[250:253], v[66:69], v[112:115]\n"
    "ds_read_b64_tr_b16 a[228:229], v90 offset:34816\n"
    "v_add_u32 v26, 131072, v87\n"
    "v_mfma_f32_16x16x32_bf16 v[100:103], a[190:193], v[196:199], v[100:103]\n"
    "ds_read_b64_tr_b16 a[230:231], v42 offset:40960\n"
    "v_add_u32 v63, 131072, v89\n"
    "v_mfma_f32_16x16x32_bf16 v[100:103], a[194:197], v[204:207], v[100:103]\n"
    "ds_read_b64_tr_b16 a[232:233], v42 offset:51200\n"
    "v_add_u32 v65, 131072, v90\n"
    "v_mfma_f32_16x16x32_bf16 v[100:103], a[198:201], v[252:255], v[100:103]\n"
    "ds_read_b64_tr_b16 a[234:235], v42 offset:41984\n"
    "s_waitcnt lgkmcnt(7)\n"
    "v_mfma_f32_16x16x32_bf16 v[100:103], a[202:205], v[6:9], v[100:103]\n"
    "ds_read_b64_tr_b16 a[236:237], v42 offset:52224\n"
    "buffer_load_dwordx4 a[156:159], v28, s[28:31], s81 offen offset:0 sc1\n"
    "v_mfma_f32_16x16x32_bf16 v[100:103], a[206:209], v[160:163], v[100:103]\n"
    "ds_read_b64_tr_b16 a[238:239], v42 offset:43008\n"
    "s_add_u32 m0, s86, 122880\n"
    "v_mfma_f32_16x16x32_bf16 v[116:119], a[190:193], v[200:203], v[116:119]\n"
    "ds_read_b64_tr_b16 a[240:241], v42 offset:53248\n"
    "v_accvgpr_read_b32 v18, a41\n"
    "v_mfma_f32_16x16x32_bf16 v[116:119], a[194:197], v[208:211], v[116:119]\n"
    "ds_read_b64_tr_b16 a[242:243], v42 offset:44032\n"
    "buffer_load_dwordx4 v56, s[20:23], s84 offen offset:0 lds\n"
    "v_mfma_f32_16x16x32_bf16 v[116:119], a[198:201], v[2:5], v[116:119]\n"
    "ds_read_b64_tr_b16 a[244:245], v42 offset:54272\n"
    "v_accvgpr_read_b32 v55, a40\n"
    "v_mfma_f32_16x16x32_bf16 v[116:119], a[202:205], v[38:41], v[116:119]\n"
    "ds_read_b64_tr_b16 a[246:247], v42 offset:45056\n"
    "v_accvgpr_read_b32 v44, a24\n"
    "v_mfma_f32_16x16x32_bf16 v[116:119], a[206:209], v[66:69], v[116:119]\n"
    "ds_read_b64_tr_b16 a[248:249], v42 offset:55296\n"
    "v_accvgpr_read_b32 v45, a29\n"
    "v_mfma_f32_16x16x32_bf16 v[104:107], a[210:213], v[196:199], v[104:107]\n"
    "ds_read_b64_tr_b16 a[250:251], v87 offset:40960\n"
    "buffer_load_dwordx4 v57, s[20:23], s84 offen offset:1024 lds\n"
    "v_mfma_f32_16x16x32_bf16 v[104:107], a[214:217], v[204:207], v[104:107]\n"
    "ds_read_b64_tr_b16 a[252:253], v87 offset:51200\n"
    "v_add3_u32 v45, v45, v44, s44\n"
    "v_mfma_f32_16x16x32_bf16 v[104:107], a[218:221], v[252:255], v[104:107]\n"
    "ds_read_b64_tr_b16 a[190:191], v87 offset:41984\n"
    "s_waitcnt lgkmcnt(7)\n"
    "v_mfma_f32_16x16x32_bf16 v[104:107], a[222:225], v[6:9], v[104:107]\n"
    "ds_read_b64_tr_b16 a[192:193], v87 offset:52224\n"
    "v_accvgpr_read_b32 v156, a6\n"
    "v_mfma_f32_16x16x32_bf16 v[104:107], a[226:229], v[160:163], v[104:107]\n"
    "ds_read_b64_tr_b16 a[194:195], v87 offset:43008\n"
    "v_accvgpr_read_b32 v157, a7\n"
    "v_mfma_f32_16x16x32_bf16 v[120:123], a[210:213], v[200:203], v[120:123]\n"
    "ds_read_b64_tr_b16 a[196:197], v87 offset:53248\n"
    "v_accvgpr_read_b32 v158, a8\n"
    "v_mfma_f32_16x16x32_bf16 v[120:123], a[214:217], v[208:211], v[120:123]\n"
    "ds_read_b64_tr_b16 a[198:199], v87 offset:44032\n"
    "v_accvgpr_read_b32 v159, a9\n"
    "v_mfma_f32_16x16x32_bf16 v[120:123], a[218:221], v[2:5], v[120:123]\n"
    "ds_read_b64_tr_b16 a[200:201], v87 offset:54272\n"
    "v_accvgpr_read_b32 v212, a10\n"
    "v_mfma_f32_16x16x32_bf16 v[120:123], a[222:225], v[38:41], v[120:123]\n"
    "ds_read_b64_tr_b16 a[202:203], v87 offset:45056\n"
    "buffer_load_dwordx4 a[160:163], v28, s[28:31], s81 offen offset:1024 sc1\n"
    "v_mfma_f32_16x16x32_bf16 v[120:123], a[226:229], v[66:69], v[120:123]\n"
    "ds_read_b64_tr_b16 a[204:205], v87 offset:55296\n"
    "v_accvgpr_read_b32 v213, a11\n"
    "v_mfma_f32_16x16x32_bf16 v[124:127], a[230:233], v[34:37], 0\n"
    "ds_read_b64_tr_b16 a[206:207], v89 offset:40960\n"
    "v_accvgpr_read_b32 v214, a12\n"
    "v_mfma_f32_16x16x32_bf16 v[124:127], a[234:237], v[50:53], v[124:127]\n"
    "ds_read_b64_tr_b16 a[208:209], v89 offset:51200\n"
    "buffer_load_dwordx4 v24, s[20:23], s84 offen offset:2048 lds\n"
    "v_mfma_f32_16x16x32_bf16 v[124:127], a[238:241], v[80:83], v[124:127]\n"
    "ds_read_b64_tr_b16 a[210:211], v89 offset:41984\n"
    "s_waitcnt lgkmcnt(7)\n"
    "v_mfma_f32_16x16x32_bf16 v[124:127], a[242:245], v[20:23], v[124:127]\n"
    "ds_read_b64_tr_b16 a[212:213], v89 offset:52224\n"
    "v_accvgpr_read_b32 v215, a13\n"
    "v_mfma_f32_16x16x32_bf16 v[124:127], a[246:249], v[224:227], v[124:127]\n"
    "ds_read_b64_tr_b16 a[214:215], v89 offset:43008\n"
    "v_accvgpr_read_b32 v216, a46\n"
    "v_mfma_f32_16x16x32_bf16 v[140:143], a[230:233], v[46:49], 0\n"
    "ds_read_b64_tr_b16 a[216:217], v89 offset:53248\n"
    "v_accvgpr_read_b32 v217, a47\n"
    "v_mfma_f32_16x16x32_bf16 v[140:143], a[234:237], v[76:79], v[140:143]\n"
    "ds_read_b64_tr_b16 a[218:219], v89 offset:44032\n"
    "buffer_load_dwordx4 v75, s[20:23], s84 offen offset:3072 lds\n"
    "v_mfma_f32_16x16x32_bf16 v[140:143], a[238:241], v[10:13], v[140:143]\n"
    "ds_read_b64_tr_b16 a[220:221], v89 offset:54272\n"
    "s_add_u32 m0, s86, 126976\n"
    "v_mfma_f32_16x16x32_bf16 v[140:143], a[242:245], v[192:195], v[140:143]\n"
    "ds_read_b64_tr_b16 a[222:223], v89 offset:45056\n"
    "s_add_u32 s84, s38, s32\n"
    "v_mfma_f32_16x16x32_bf16 v[140:143], a[246:249], v[228:231], v[140:143]\n"
    "ds_read_b64_tr_b16 a[224:225], v89 offset:55296\n"
    "s_add_u32 s84, s84, 147456\n"
    "v_mfma_f32_16x16x32_bf16 v[128:131], a[250:253], v[34:37], 0\n"
    "ds_read_b64_tr_b16 a[226:227], v90 offset:40960\n"
    "v_accvgpr_read_b32 v218, a48\n"
    "v_mfma_f32_16x16x32_bf16 v[128:131], a[190:193], v[50:53], v[128:131]\n"
    "ds_read_b64_tr_b16 a[228:229], v90 offset:51200\n"
    "v_accvgpr_read_b32 v219, a49\n"
    "v_mfma_f32_16x16x32_bf16 v[128:131], a[194:197], v[80:83], v[128:131]\n"
    "ds_read_b64_tr_b16 a[230:231], v90 offset:41984\n"
    "s_waitcnt lgkmcnt(7)\n"
    "v_mfma_f32_16x16x32_bf16 v[128:131], a[198:201], v[20:23], v[128:131]\n"
    "ds_read_b64_tr_b16 a[232:233], v90 offset:52224\n"
    "v_mfma_f32_16x16x32_bf16 v[128:131], a[202:205], v[224:227], v[128:131]\n"
    "ds_read_b64_tr_b16 a[234:235], v90 offset:43008\n"
    "buffer_load_dwordx4 a[164:167], v28, s[28:31], s81 offen offset:2048 sc1\n"
    "v_mfma_f32_16x16x32_bf16 v[144:147], a[250:253], v[46:49], 0\n"
    "ds_read_b64_tr_b16 a[236:237], v90 offset:53248\n"
    "v_mfma_f32_16x16x32_bf16 v[144:147], a[190:193], v[76:79], v[144:147]\n"
    "ds_read_b64_tr_b16 a[238:239], v90 offset:44032\n"
    "s_waitcnt vmcnt(12) lgkmcnt(7)\n"
    "v_mfma_f32_16x16x32_bf16 v[144:147], a[194:197], v[10:13], v[144:147]\n"
    "ds_read_b64_tr_b16 a[240:241], v90 offset:54272\n"
    "buffer_store_dword a42, v19, s[28:31], s90 offen offset:384 sc1\n"
    "v_mfma_f32_16x16x32_bf16 v[144:147], a[198:201], v[192:195], v[144:147]\n"
    "ds_read_b64_tr_b16 a[242:243], v90 offset:45056\n"
    "v_mfma_f32_16x16x32_bf16 v[144:147], a[202:205], v[228:231], v[144:147]\n"
    "ds_read_b64_tr_b16 a[244:245], v90 offset:55296\n"
    "buffer_load_dwordx4 v56, s[12:15], s89 offen offset:0 lds\n"
    "v_mfma_f32_16x16x32_bf16 v[132:135], a[206:209], v[34:37], 0\n"
    "s_add_u32 s89, s42, s32\n"
    "v_mfma_f32_16x16x32_bf16 v[132:135], a[210:213], v[50:53], v[132:135]\n"
    "v_mfma_f32_16x16x32_bf16 v[132:135], a[214:217], v[80:83], v[132:135]\n"
    "ds_read_b64_tr_b16 a[246:247], v42 offset:61440\n"
    "s_add_u32 m0, s86, 143360\n"
    "v_mfma_f32_16x16x32_bf16 v[132:135], a[218:221], v[20:23], v[132:135]\n"
    "ds_read_b64_tr_b16 a[248:249], v91 offset:6144\n"
    "buffer_load_dwordx4 v56, s[20:23], s84 offen offset:0 lds\n"
    "v_mfma_f32_16x16x32_bf16 v[132:135], a[222:225], v[224:227], v[132:135]\n"
    "ds_read_b64_tr_b16 a[250:251], v42 offset:62464\n"
    "v_accvgpr_read_b32 v85, a28\n"
    "v_mfma_f32_16x16x32_bf16 v[148:151], a[206:209], v[46:49], 0\n"
    "ds_read_b64_tr_b16 a[252:253], v91 offset:7168\n"
    "s_add_u32 s80, s54, 4096\n"
    "v_mfma_f32_16x16x32_bf16 v[148:151], a[210:213], v[76:79], v[148:151]\n"
    "ds_read_b64_tr_b16 a[190:191], v42 offset:63488\n"
    "v_mfma_f32_16x16x32_bf16 v[148:151], a[214:217], v[10:13], v[148:151]\n"
    "ds_read_b64_tr_b16 a[192:193], v91 offset:8192\n"
    "v_mfma_f32_16x16x32_bf16 v[148:151], a[218:221], v[192:195], v[148:151]\n"
    "ds_read_b64_tr_b16 a[194:195], v42 offset:64512\n"
    "v_mfma_f32_16x16x32_bf16 v[148:151], a[222:225], v[228:231], v[148:151]\n"
    "ds_read_b64_tr_b16 a[196:197], v91 offset:9216\n"
    "s_waitcnt lgkmcnt(7)\n"
    "v_mfma_f32_16x16x32_bf16 v[136:139], a[226:229], v[34:37], 0\n"
    "ds_read_b64_tr_b16 a[198:199], v91 offset:0\n"
    "buffer_load_dwordx4 a[168:171], v28, s[28:31], s81 offen offset:3072 sc1\n"
    "v_mfma_f32_16x16x32_bf16 v[136:139], a[230:233], v[50:53], v[136:139]\n"
    "ds_read_b64_tr_b16 a[200:201], v91 offset:10240\n"
    "s_add_u32 s81, s59, 12288\n"
    "v_mfma_f32_16x16x32_bf16 v[136:139], a[234:237], v[80:83], v[136:139]\n"
    "ds_read_b64_tr_b16 a[202:203], v87 offset:61440\n"
    "v_mfma_f32_16x16x32_bf16 v[136:139], a[238:241], v[20:23], v[136:139]\n"
    "ds_read_b64_tr_b16 a[204:205], v86 offset:6144\n"
    "buffer_load_dwordx4 v57, s[20:23], s84 offen offset:1024 lds\n"
    "v_mfma_f32_16x16x32_bf16 v[136:139], a[242:245], v[224:227], v[136:139]\n"
    "ds_read_b64_tr_b16 a[206:207], v87 offset:62464\n"
    "v_mfma_f32_16x16x32_bf16 v[152:155], a[226:229], v[46:49], 0\n"
    "ds_read_b64_tr_b16 a[208:209], v86 offset:7168\n"
    "v_mfma_f32_16x16x32_bf16 v[152:155], a[230:233], v[76:79], v[152:155]\n"
    "ds_read_b64_tr_b16 a[210:211], v87 offset:63488\n"
    "v_mfma_f32_16x16x32_bf16 v[152:155], a[234:237], v[10:13], v[152:155]\n"
    "ds_read_b64_tr_b16 a[212:213], v86 offset:8192\n"
    "v_mfma_f32_16x16x32_bf16 v[152:155], a[238:241], v[192:195], v[152:155]\n"
    "ds_read_b64_tr_b16 a[214:215], v87 offset:64512\n"
    "buffer_load_dwordx4 v24, s[20:23], s84 offen offset:2048 lds\n"
    "v_mfma_f32_16x16x32_bf16 v[152:155], a[242:245], v[228:231], v[152:155]\n"
    "ds_read_b64_tr_b16 a[216:217], v86 offset:9216\n"
    "v_mfma_f32_16x16x4_4b_bf16 v[124:139], a[50:51], v[70:71], v[124:139]\n"
    "ds_read_b64_tr_b16 a[218:219], v86 offset:0\n"
    "s_nop 2\n"
    "v_mfma_f32_16x16x4_4b_bf16 v[140:155], a[50:51], v[72:73], v[140:155]\n"
    "ds_read_b64_tr_b16 a[220:221], v86 offset:10240\n"
    "s_waitcnt lgkmcnt(6)\n"
    "s_nop 2\n"
    "v_mfma_f32_16x16x32_bf16 v[124:127], a[246:249], v[196:199], v[124:127]\n"
    "ds_read_b64_tr_b16 a[222:223], v89 offset:61440\n"
    "v_mfma_f32_16x16x32_bf16 v[124:127], a[250:253], v[204:207], v[124:127]\n"
    "ds_read_b64_tr_b16 a[224:225], v27 offset:6144\n"
    "v_mfma_f32_16x16x32_bf16 v[124:127], a[190:193], v[252:255], v[124:127]\n"
    "ds_read_b64_tr_b16 a[226:227], v89 offset:62464\n"
    "v_mfma_f32_16x16x32_bf16 v[124:127], a[194:197], v[6:9], v[124:127]\n"
    "ds_read_b64_tr_b16 a[228:229], v27 offset:7168\n"
    "v_mfma_f32_16x16x32_bf16 v[124:127], a[198:201], v[160:163], v[124:127]\n"
    "ds_read_b64_tr_b16 a[230:231], v89 offset:63488\n"
    "buffer_load_dwordx4 a[172:175], v28, s[28:31], s81 offen offset:0 sc1\n"
    "v_mfma_f32_16x16x32_bf16 v[140:143], a[246:249], v[200:203], v[140:143]\n"
    "ds_read_b64_tr_b16 a[232:233], v27 offset:8192\n"
    "v_mfma_f32_16x16x32_bf16 v[140:143], a[250:253], v[208:211], v[140:143]\n"
    "ds_read_b64_tr_b16 a[234:235], v89 offset:64512\n"
    "v_mfma_f32_16x16x32_bf16 v[140:143], a[190:193], v[2:5], v[140:143]\n"
    "ds_read_b64_tr_b16 a[236:237], v27 offset:9216\n"
    "buffer_load_dwordx4 v75, s[20:23], s84 offen offset:3072 lds\n"
    "v_mfma_f32_16x16x32_bf16 v[140:143], a[194:197], v[38:41], v[140:143]\n"
    "ds_read_b64_tr_b16 a[238:239], v27 offset:0\n"
    "s_add_u32 m0, s86, 147456\n"
    "v_mfma_f32_16x16x32_bf16 v[140:143], a[198:201], v[66:69], v[140:143]\n"
    "ds_read_b64_tr_b16 a[240:241], v27 offset:10240\n"
    "s_add_u32 s84, s38, s33\n"
    "v_mfma_f32_16x16x32_bf16 v[128:131], a[202:205], v[196:199], v[128:131]\n"
    "ds_read_b64_tr_b16 a[242:243], v90 offset:61440\n"
    "v_mul_f32 v124, v124, v84\n"
    "v_mfma_f32_16x16x32_bf16 v[128:131], a[206:209], v[204:207], v[128:131]\n"
    "ds_read_b64_tr_b16 a[244:245], v64 offset:6144\n"
    "s_waitcnt lgkmcnt(7)\n"
    "v_mfma_f32_16x16x32_bf16 v[128:131], a[210:213], v[252:255], v[128:131]\n"
    "ds_read_b64_tr_b16 a[246:247], v90 offset:62464\n"
    "buffer_load_dwordx4 v56, s[12:15], s89 offen offset:0 lds\n"
    "v_mfma_f32_16x16x32_bf16 v[128:131], a[214:217], v[6:9], v[128:131]\n"
    "ds_read_b64_tr_b16 a[248:249], v64 offset:7168\n"
    "v_mul_f32 v125, v125, v84\n"
    "v_mfma_f32_16x16x32_bf16 v[128:131], a[218:221], v[160:163], v[128:131]\n"
    "ds_read_b64_tr_b16 a[250:251], v90 offset:63488\n"
    "v_mul_f32 v126, v126, v84\n"
    "v_mfma_f32_16x16x32_bf16 v[144:147], a[202:205], v[200:203], v[144:147]\n"
    "ds_read_b64_tr_b16 a[252:253], v64 offset:8192\n"
    "v_mul_f32 v127, v127, v84\n"
    "v_mfma_f32_16x16x32_bf16 v[144:147], a[206:209], v[208:211], v[144:147]\n"
    "ds_read_b64_tr_b16 a[190:191], v90 offset:64512\n"
    "v_cvt_pk_bf16_f32 v188, v124, v125\n"
    "v_mfma_f32_16x16x32_bf16 v[144:147], a[210:213], v[2:5], v[144:147]\n"
    "ds_read_b64_tr_b16 a[192:193], v64 offset:9216\n"
    "v_cvt_pk_bf16_f32 v189, v126, v127\n"
    "v_mfma_f32_16x16x32_bf16 v[144:147], a[214:217], v[38:41], v[144:147]\n"
    "ds_read_b64_tr_b16 a[194:195], v64 offset:0\n"
    "s_waitcnt vmcnt(16) lgkmcnt(7)\n"
    "v_mfma_f32_16x16x32_bf16 v[144:147], a[218:221], v[66:69], v[144:147]\n"
    "s_barrier\n"
    "ds_read_b64_tr_b16 a[196:197], v64 offset:10240\n"
    "v_mfma_f32_16x16x32_bf16 v[132:135], a[222:225], v[196:199], v[132:135]\n"
    "ds_read_b64_tr_b16 a[198:199], v91 offset:16384\n"
    "buffer_load_dwordx4 a[176:179], v28, s[28:31], s81 offen offset:1024 sc1\n"
    "v_mfma_f32_16x16x32_bf16 v[132:135], a[226:229], v[204:207], v[132:135]\n"
    "ds_read_b64_tr_b16 a[200:201], v91 offset:26624\n"
    "v_mul_f32 v128, v128, v84\n"
    "v_mfma_f32_16x16x32_bf16 v[132:135], a[230:233], v[252:255], v[132:135]\n"
    "ds_read_b64_tr_b16 a[202:203], v91 offset:17408\n"
    "s_add_u32 m0, s86, 0\n"
    "v_mfma_f32_16x16x32_bf16 v[132:135], a[234:237], v[6:9], v[132:135]\n"
    "ds_read_b64_tr_b16 a[204:205], v91 offset:27648\n"
    "buffer_load_dwordx4 v56, s[20:23], s84 offen offset:0 lds\n"
    "v_mfma_f32_16x16x32_bf16 v[132:135], a[238:241], v[160:163], v[132:135]\n"
    "ds_read_b64_tr_b16 a[206:207], v91 offset:18432\n"
    "v_mul_f32 v129, v129, v84\n"
    "v_mfma_f32_16x16x32_bf16 v[148:151], a[222:225], v[200:203], v[148:151]\n"
    "ds_read_b64_tr_b16 a[208:209], v91 offset:28672\n"
    "v_mul_f32 v130, v130, v84\n"
    "v_mfma_f32_16x16x32_bf16 v[148:151], a[226:229], v[208:211], v[148:151]\n"
    "ds_read_b64_tr_b16 a[210:211], v91 offset:19456\n"
    "v_mul_f32 v131, v131, v84\n"
    "v_mfma_f32_16x16x32_bf16 v[148:151], a[230:233], v[2:5], v[148:151]\n"
    "ds_read_b64_tr_b16 a[212:213], v91 offset:29696\n"
    "v_cvt_pk_bf16_f32 v190, v128, v129\n"
    "v_mfma_f32_16x16x32_bf16 v[148:151], a[234:237], v[38:41], v[148:151]\n"
    "ds_read_b64_tr_b16 a[214:215], v91 offset:20480\n"
    "v_cvt_pk_bf16_f32 v191, v130, v131\n"
    "v_mfma_f32_16x16x32_bf16 v[148:151], a[238:241], v[66:69], v[148:151]\n"
    "ds_read_b64_tr_b16 a[216:217], v91 offset:30720\n"
    "v_mul_f32 v140, v140, v88\n"
    "v_mfma_f32_16x16x32_bf16 v[136:139], a[242:245], v[196:199], v[136:139]\n"
    "ds_read_b64_tr_b16 a[218:219], v86 offset:16384\n"
    "s_waitcnt lgkmcnt(7)\n"
    "v_mfma_f32_16x16x32_bf16 v[136:139], a[246:249], v[204:207], v[136:139]\n"
    "ds_read_b64_tr_b16 a[220:221], v86 offset:26624\n"
    "buffer_load_dwordx4 v57, s[20:23], s84 offen offset:1024 lds\n"
    "v_mfma_f32_16x16x32_bf16 v[136:139], a[250:253], v[252:255], v[136:139]\n"
    "ds_read_b64_tr_b16 a[222:223], v86 offset:17408\n"
    "v_mul_f32 v141, v141, v88\n"
    "v_mfma_f32_16x16x32_bf16 v[136:139], a[190:193], v[6:9], v[136:139]\n"
    "ds_read_b64_tr_b16 a[224:225], v86 offset:27648\n"
    "v_mul_f32 v142, v142, v88\n"
    "v_mfma_f32_16x16x32_bf16 v[136:139], a[194:197], v[160:163], v[136:139]\n"
    "ds_read_b64_tr_b16 a[226:227], v86 offset:18432\n"
    "v_mul_f32 v143, v143, v88\n"
    "v_mfma_f32_16x16x32_bf16 v[152:155], a[242:245], v[200:203], v[152:155]\n"
    "ds_read_b64_tr_b16 a[228:229], v86 offset:28672\n"
    "v_cvt_pk_bf16_f32 v168, v140, v141\n"
    "v_mfma_f32_16x16x32_bf16 v[152:155], a[246:249], v[208:211], v[152:155]\n"
    "buffer_store_dwordx4 v[188:191], v54, s[28:31], s52 offen offset:0 sc1\n"
    "ds_read_b64_tr_b16 a[230:231], v86 offset:19456\n"
    "v_mfma_f32_16x16x32_bf16 v[152:155], a[250:253], v[2:5], v[152:155]\n"
    "ds_read_b64_tr_b16 a[232:233], v86 offset:29696\n"
    "v_cvt_pk_bf16_f32 v169, v142, v143\n"
    "v_mfma_f32_16x16x32_bf16 v[152:155], a[190:193], v[38:41], v[152:155]\n"
    "ds_read_b64_tr_b16 a[234:235], v86 offset:20480\n"
    "buffer_load_dword v43, v25, s[28:31], s99 offen offset:384 sc1\n"
    "v_mfma_f32_16x16x32_bf16 v[152:155], a[194:197], v[66:69], v[152:155]\n"
    "ds_read_b64_tr_b16 a[236:237], v86 offset:30720\n"
    "v_mul_f32 v144, v144, v88\n"
    "v_mfma_f32_16x16x32_bf16 v[124:127], a[198:201], v[34:37], 0\n"
    "ds_read_b64_tr_b16 a[238:239], v27 offset:16384\n"
    "buffer_load_dwordx4 a[180:183], v28, s[28:31], s81 offen offset:2048 sc1\n"
    "v_mfma_f32_16x16x32_bf16 v[124:127], a[202:205], v[50:53], v[124:127]\n"
    "ds_read_b64_tr_b16 a[240:241], v27 offset:26624\n"
    "s_waitcnt lgkmcnt(6)\n"
    "v_mfma_f32_16x16x32_bf16 v[124:127], a[206:209], v[80:83], v[124:127]\n"
    "ds_read_b64_tr_b16 a[242:243], v27 offset:17408\n"
    "v_mul_f32 v145, v145, v88\n"
    "v_mfma_f32_16x16x32_bf16 v[124:127], a[210:213], v[20:23], v[124:127]\n"
    "ds_read_b64_tr_b16 a[244:245], v27 offset:27648\n"
    "buffer_load_dwordx4 v24, s[20:23], s84 offen offset:2048 lds\n"
    "v_mfma_f32_16x16x32_bf16 v[124:127], a[214:217], v[224:227], v[124:127]\n"
    "ds_read_b64_tr_b16 a[246:247], v27 offset:18432\n"
    "v_mul_f32 v146, v146, v88\n"
    "v_mfma_f32_16x16x32_bf16 v[140:143], a[198:201], v[46:49], 0\n"
    "ds_read_b64_tr_b16 a[248:249], v27 offset:28672\n"
    "v_mul_f32 v147, v147, v88\n"
    "v_mfma_f32_16x16x32_bf16 v[140:143], a[202:205], v[76:79], v[140:143]\n"
    "ds_read_b64_tr_b16 a[250:251], v27 offset:19456\n"
    "buffer_load_dwordx4 v75, s[20:23], s84 offen offset:3072 lds\n"
    "v_mfma_f32_16x16x32_bf16 v[140:143], a[206:209], v[10:13], v[140:143]\n"
    "ds_read_b64_tr_b16 a[252:253], v27 offset:29696\n"
    "v_cvt_pk_bf16_f32 v170, v144, v145\n"
    "v_mfma_f32_16x16x32_bf16 v[140:143], a[210:213], v[192:195], v[140:143]\n"
    "ds_read_b64_tr_b16 a[190:191], v27 offset:20480\n"
    "v_cvt_pk_bf16_f32 v171, v146, v147\n"
    "v_mfma_f32_16x16x32_bf16 v[140:143], a[214:217], v[228:231], v[140:143]\n"
    "ds_read_b64_tr_b16 a[192:193], v27 offset:30720\n"
    "v_mul_f32 v132, v132, v84\n"
    "v_mfma_f32_16x16x32_bf16 v[128:131], a[218:221], v[34:37], 0\n"
    "ds_read_b64_tr_b16 a[194:195], v64 offset:16384\n"
    "v_mul_f32 v133, v133, v84\n"
    "v_mfma_f32_16x16x32_bf16 v[128:131], a[222:225], v[50:53], v[128:131]\n"
    "ds_read_b64_tr_b16 a[196:197], v64 offset:26624\n"
    "v_mul_f32 v134, v134, v84\n"
    "v_mfma_f32_16x16x32_bf16 v[128:131], a[226:229], v[80:83], v[128:131]\n"
    "buffer_store_dwordx4 v[168:171], v54, s[28:31], s83 offen offset:0 sc1\n"
    "ds_read_b64_tr_b16 a[198:199], v64 offset:17408\n"
    "s_waitcnt lgkmcnt(6)\n"
    "v_mfma_f32_16x16x32_bf16 v[128:131], a[230:233], v[20:23], v[128:131]\n"
    "ds_read_b64_tr_b16 a[200:201], v64 offset:27648\n"
    "v_mul_f32 v135, v135, v84\n"
    "v_mfma_f32_16x16x32_bf16 v[128:131], a[234:237], v[224:227], v[128:131]\n"
    "ds_read_b64_tr_b16 a[202:203], v64 offset:18432\n"
    "v_cvt_pk_bf16_f32 v172, v132, v133\n"
    "v_mfma_f32_16x16x32_bf16 v[144:147], a[218:221], v[46:49], 0\n"
    "ds_read_b64_tr_b16 a[204:205], v64 offset:28672\n"
    "v_cvt_pk_bf16_f32 v173, v134, v135\n"
    "v_mfma_f32_16x16x32_bf16 v[144:147], a[222:225], v[76:79], v[144:147]\n"
    "ds_read_b64_tr_b16 a[206:207], v64 offset:19456\n"
    "buffer_load_dwordx4 a[184:187], v28, s[28:31], s81 offen offset:3072 sc1\n"
    "v_mfma_f32_16x16x32_bf16 v[144:147], a[226:229], v[10:13], v[144:147]\n"
    "ds_read_b64_tr_b16 a[208:209], v64 offset:29696\n"
    "s_add_u32 s89, s40, s33\n"
    "v_mfma_f32_16x16x32_bf16 v[144:147], a[230:233], v[192:195], v[144:147]\n"
    "ds_read_b64_tr_b16 a[210:211], v64 offset:20480\n"
    "s_add_u32 m0, s86, 4096\n"
    "v_mfma_f32_16x16x32_bf16 v[144:147], a[234:237], v[228:231], v[144:147]\n"
    "ds_read_b64_tr_b16 a[212:213], v64 offset:30720\n"
    "buffer_load_dwordx4 v56, s[20:23], s89 offen offset:0 lds\n"
    "v_mfma_f32_16x16x32_bf16 v[132:135], a[238:241], v[34:37], 0\n"
    "ds_read_b64_tr_b16 a[214:215], v91 offset:36864\n"
    "v_mul_f32 v148, v148, v88\n"
    "v_mfma_f32_16x16x32_bf16 v[132:135], a[242:245], v[50:53], v[132:135]\n"
    "ds_read_b64_tr_b16 a[216:217], v91 offset:47104\n"
    "v_mul_f32 v149, v149, v88\n"
    "v_mfma_f32_16x16x32_bf16 v[132:135], a[246:249], v[80:83], v[132:135]\n"
    "ds_read_b64_tr_b16 a[218:219], v91 offset:37888\n"
    "s_waitcnt lgkmcnt(7)\n"
    "v_mfma_f32_16x16x32_bf16 v[132:135], a[250:253], v[20:23], v[132:135]\n"
    "ds_read_b64_tr_b16 a[220:221], v91 offset:48128\n"
    "v_mul_f32 v150, v150, v88\n"
    "v_mfma_f32_16x16x32_bf16 v[132:135], a[190:193], v[224:227], v[132:135]\n"
    "ds_read_b64_tr_b16 a[222:223], v91 offset:38912\n"
    "v_mul_f32 v151, v151, v88\n"
    "v_cvt_pk_bf16_f32 v58, v148, v149\n"
    "v_cvt_pk_bf16_f32 v59, v150, v151\n"
    "v_mfma_f32_16x16x32_bf16 v[148:151], a[238:241], v[46:49], 0\n"
    "ds_read_b64_tr_b16 a[224:225], v91 offset:49152\n"
    "s_add_u32 s84, s38, s33\n"
    "v_mfma_f32_16x16x32_bf16 v[148:151], a[242:245], v[76:79], v[148:151]\n"
    "ds_read_b64_tr_b16 a[226:227], v91 offset:39936\n"
    "s_add_u32 s84, s84, 147456\n"
    "v_mfma_f32_16x16x32_bf16 v[148:151], a[246:249], v[10:13], v[148:151]\n"
    "ds_read_b64_tr_b16 a[228:229], v91 offset:50176\n"
    "s_add_u32 m0, s86, 20480\n"
    "v_mfma_f32_16x16x32_bf16 v[148:151], a[250:253], v[192:195], v[148:151]\n"
    "ds_read_b64_tr_b16 a[230:231], v91 offset:40960\n"
    "buffer_load_dwordx4 v56, s[20:23], s84 offen offset:0 lds\n"
    "v_mfma_f32_16x16x32_bf16 v[148:151], a[190:193], v[228:231], v[148:151]\n"
    "ds_read_b64_tr_b16 a[232:233], v91 offset:51200\n"
    "v_mul_f32 v136, v136, v84\n"
    "v_mul_f32 v137, v137, v84\n"
    "v_mul_f32 v138, v138, v84\n"
    "v_mul_f32 v139, v139, v84\n"
    "v_cvt_pk_bf16_f32 v174, v136, v137\n"
    "v_cvt_pk_bf16_f32 v175, v138, v139\n"
    "v_mfma_f32_16x16x32_bf16 v[136:139], a[194:197], v[34:37], 0\n"
    "ds_read_b64_tr_b16 a[234:235], v86 offset:36864\n"
    "buffer_load_dwordx4 v57, s[20:23], s84 offen offset:1024 lds\n"
    "v_mfma_f32_16x16x32_bf16 v[136:139], a[198:201], v[50:53], v[136:139]\n"
    "ds_read_b64_tr_b16 a[236:237], v86 offset:47104\n"
    "s_add_u32 s83, s52, s45\n"
    "v_mfma_f32_16x16x32_bf16 v[136:139], a[202:205], v[80:83], v[136:139]\n"
    "ds_read_b64_tr_b16 a[238:239], v86 offset:37888\n"
    "s_waitcnt lgkmcnt(6)\n"
    "v_mfma_f32_16x16x32_bf16 v[136:139], a[206:209], v[20:23], v[136:139]\n"
    "buffer_store_dwordx4 v[172:175], v54, s[28:31], s83 offen offset:0 sc1\n"
    "ds_read_b64_tr_b16 a[240:241], v86 offset:48128\n"
    "v_mfma_f32_16x16x32_bf16 v[136:139], a[210:213], v[224:227], v[136:139]\n"
    "ds_read_b64_tr_b16 a[242:243], v86 offset:38912\n"
    "v_mul_f32 v152, v152, v88\n"
    "v_mul_f32 v153, v153, v88\n"
    "v_mul_f32 v154, v154, v88\n"
    "v_mul_f32 v155, v155, v88\n"
    "v_cvt_pk_bf16_f32 v60, v152, v153\n"
    "v_cvt_pk_bf16_f32 v61, v154, v155\n"
    "v_mfma_f32_16x16x32_bf16 v[152:155], a[194:197], v[46:49], 0\n"
    "ds_read_b64_tr_b16 a[244:245], v86 offset:49152\n"
    "s_add_u32 s83, s83, 16384\n"
    "v_mfma_f32_16x16x32_bf16 v[152:155], a[198:201], v[76:79], v[152:155]\n"
    "ds_read_b64_tr_b16 a[246:247], v86 offset:39936\n"
    "s_add_u32 s81, s59, 20480\n"
    "v_mfma_f32_16x16x32_bf16 v[152:155], a[202:205], v[10:13], v[152:155]\n"
    "ds_read_b64_tr_b16 a[248:249], v86 offset:50176\n"
    "s_add_u32 s89, s42, s33\n"
    "v_mfma_f32_16x16x32_bf16 v[152:155], a[206:209], v[192:195], v[152:155]\n"
    "ds_read_b64_tr_b16 a[250:251], v86 offset:40960\n"
    "buffer_load_dwordx4 v24, s[20:23], s84 offen offset:2048 lds\n"
    "v_mfma_f32_16x16x32_bf16 v[152:155], a[210:213], v[228:231], v[152:155]\n"
    "ds_read_b64_tr_b16 a[252:253], v86 offset:51200\n"
    "v_mfma_f32_16x16x32_bf16 v[92:95], a[214:217], a[2:5], v[92:95]\n"
    "ds_read_b64_tr_b16 a[190:191], v27 offset:36864\n"
    "v_mfma_f32_16x16x32_bf16 v[92:95], a[218:221], a[132:135], v[92:95]\n"
    "ds_read_b64_tr_b16 a[192:193], v27 offset:47104\n"
    "v_mfma_f32_16x16x32_bf16 v[92:95], a[222:225], a[140:143], v[92:95]\n"
    "ds_read_b64_tr_b16 a[194:195], v27 offset:37888\n"
    "s_waitcnt lgkmcnt(7)\n"
    "v_mfma_f32_16x16x32_bf16 v[92:95], a[226:229], a[148:151], v[92:95]\n"
    "ds_read_b64_tr_b16 a[196:197], v27 offset:48128\n"
    "v_mfma_f32_16x16x32_bf16 v[92:95], a[230:233], v[240:243], v[92:95]\n"
    "buffer_store_dwordx4 v[58:61], v54, s[28:31], s83 offen offset:0 sc1\n"
    "ds_read_b64_tr_b16 a[198:199], v27 offset:38912\n"
    "v_mfma_f32_16x16x32_bf16 v[108:111], a[214:217], a[20:23], v[108:111]\n"
    "ds_read_b64_tr_b16 a[200:201], v27 offset:49152\n"
    "s_add_u32 s83, s48, s36\n"
    "v_mfma_f32_16x16x32_bf16 v[108:111], a[218:221], a[136:139], v[108:111]\n"
    "ds_read_b64_tr_b16 a[202:203], v27 offset:39936\n"
    "v_mfma_f32_16x16x32_bf16 v[108:111], a[222:225], a[144:147], v[108:111]\n"
    "ds_read_b64_tr_b16 a[204:205], v27 offset:50176\n"
    "v_mfma_f32_16x16x32_bf16 v[108:111], a[226:229], a[152:155], v[108:111]\n"
    "ds_read_b64_tr_b16 a[206:207], v27 offset:40960\n"
    "buffer_load_dwordx4 v75, s[20:23], s84 offen offset:3072 lds\n"
    "v_mfma_f32_16x16x32_bf16 v[108:111], a[230:233], v[244:247], v[108:111]\n"
    "ds_read_b64_tr_b16 a[208:209], v27 offset:51200\n"
    "s_add_u32 m0, s86, 24576\n"
    "v_mfma_f32_16x16x32_bf16 v[96:99], a[234:237], a[2:5], v[96:99]\n"
    "ds_read_b64_tr_b16 a[210:211], v64 offset:36864\n"
    "s_waitcnt vmcnt(12) lgkmcnt(7)\n"
    "v_cmp_eq_u32 vcc, 0, v43\n"
    "v_mfma_f32_16x16x32_bf16 v[96:99], a[238:241], a[132:135], v[96:99]\n"
    "ds_read_b64_tr_b16 a[212:213], v64 offset:47104\n"
    "s_add_u32 s84, s37, s34\n"
    "v_mfma_f32_16x16x32_bf16 v[96:99], a[242:245], a[140:143], v[96:99]\n"
    "ds_read_b64_tr_b16 a[214:215], v64 offset:37888\n"
    "s_cbranch_vccnz L_o_%=\n"
    "L_o_ret_%=:\n"
    "v_mfma_f32_16x16x32_bf16 v[96:99], a[246:249], a[148:151], v[96:99]\n"
    "ds_read_b64_tr_b16 a[216:217], v64 offset:48128\n"
    "buffer_load_dwordx4 v[164:167], v74, s[28:31], s81 offen offset:0 sc1\n"
    "v_mfma_f32_16x16x32_bf16 v[96:99], a[250:253], v[240:243], v[96:99]\n"
    "ds_read_b64_tr_b16 a[218:219], v64 offset:38912\n"
    "s_add_u32 s81, s59, 16384\n"
    "v_mfma_f32_16x16x32_bf16 v[112:115], a[234:237], a[20:23], v[112:115]\n"
    "ds_read_b64_tr_b16 a[220:221], v64 offset:49152\n"
    "v_mfma_f32_16x16x32_bf16 v[112:115], a[238:241], a[136:139], v[112:115]\n"
    "ds_read_b64_tr_b16 a[222:223], v64 offset:39936\n"
    "buffer_load_dwordx4 v56, s[12:15], s89 offen offset:0 lds\n"
    "v_mfma_f32_16x16x32_bf16 v[112:115], a[242:245], a[144:147], v[112:115]\n"
    "ds_read_b64_tr_b16 a[224:225], v64 offset:50176\n"
    "s_add_u32 s89, s39, s34\n"
    "v_mfma_f32_16x16x32_bf16 v[112:115], a[246:249], a[152:155], v[112:115]\n"
    "s_barrier\n"
    "ds_read_b64_tr_b16 a[226:227], v64 offset:40960\n"
    "v_mfma_f32_16x16x32_bf16 v[112:115], a[250:253], v[244:247], v[112:115]\n"
    "ds_read_b64_tr_b16 a[228:229], v64 offset:51200\n"
    "buffer_load_dwordx4 a[34:37], v28, s[28:31], s81 offen offset:2048 sc1\n"
    "v_mfma_f32_16x16x32_bf16 v[100:103], a[190:193], a[2:5], v[100:103]\n"
    "ds_read_b64_tr_b16 a[230:231], v91 offset:57344\n"
    "v_mfma_f32_16x16x32_bf16 v[100:103], a[194:197], a[132:135], v[100:103]\n"
    "ds_read_b64_tr_b16 a[232:233], v29 offset:2048\n"
    "s_waitcnt lgkmcnt(7)\n"
    "v_mfma_f32_16x16x32_bf16 v[100:103], a[198:201], a[140:143], v[100:103]\n"
    "ds_read_b64_tr_b16 a[234:235], v91 offset:58368\n"
    "buffer_load_dwordx4 a[70:73], v28, s[28:31], s81 offen offset:3072 sc1\n"
    "v_mfma_f32_16x16x32_bf16 v[100:103], a[202:205], a[148:151], v[100:103]\n"
    "ds_read_b64_tr_b16 a[236:237], v29 offset:3072\n"
    "s_add_u32 m0, s86, 40960\n"
    "v_mfma_f32_16x16x32_bf16 v[100:103], a[206:209], v[240:243], v[100:103]\n"
    "ds_read_b64_tr_b16 a[238:239], v91 offset:59392\n"
    "v_mfma_f32_16x16x32_bf16 v[116:119], a[190:193], a[20:23], v[116:119]\n"
    "ds_read_b64_tr_b16 a[240:241], v29 offset:4096\n"
    "buffer_load_dwordx4 v56, s[20:23], s84 offen offset:0 lds\n"
    "v_mfma_f32_16x16x32_bf16 v[116:119], a[194:197], a[136:139], v[116:119]\n"
    "ds_read_b64_tr_b16 a[242:243], v91 offset:60416\n"
    "v_mfma_f32_16x16x32_bf16 v[116:119], a[198:201], a[144:147], v[116:119]\n"
    "ds_read_b64_tr_b16 a[244:245], v29 offset:5120\n"
    "v_mfma_f32_16x16x32_bf16 v[116:119], a[202:205], a[152:155], v[116:119]\n"
    "ds_read_b64_tr_b16 a[246:247], v91 offset:61440\n"
    "v_mfma_f32_16x16x32_bf16 v[116:119], a[206:209], v[244:247], v[116:119]\n"
    "ds_read_b64_tr_b16 a[248:249], v29 offset:6144\n"
    "v_mfma_f32_16x16x32_bf16 v[104:107], a[210:213], a[2:5], v[104:107]\n"
    "ds_read_b64_tr_b16 a[250:251], v86 offset:57344\n"
    "buffer_load_dwordx4 v57, s[20:23], s84 offen offset:1024 lds\n"
    "v_mfma_f32_16x16x32_bf16 v[104:107], a[214:217], a[132:135], v[104:107]\n"
    "ds_read_b64_tr_b16 a[252:253], v26 offset:2048\n"
    ".p2align 12\n"
    "L_pg9_%=:\n"
    "s_cbranch_execz L_tramp_%=\n"
    "s_waitcnt lgkmcnt(7)\n"
    "v_mfma_f32_16x16x32_bf16 v[104:107], a[218:221], a[140:143], v[104:107]\n"
    "ds_read_b64_tr_b16 a[190:191], v86 offset:58368\n"
    "v_mfma_f32_16x16x32_bf16 v[104:107], a[222:225], a[148:151], v[104:107]\n"
    "ds_read_b64_tr_b16 a[192:193], v26 offset:3072\n"
    "v_mfma_f32_16x16x32_bf16 v[104:107], a[226:229], v[240:243], v[104:107]\n"
    "ds_read_b64_tr_b16 a[194:195], v86 offset:59392\n"
    "v_mfma_f32_16x16x32_bf16 v[120:123], a[210:213], a[20:23], v[120:123]\n"
    "ds_read_b64_tr_b16 a[196:197], v26 offset:4096\n"
    "v_mfma_f32_16x16x32_bf16 v[120:123], a[214:217], a[136:139], v[120:123]\n"
    "ds_read_b64_tr_b16 a[198:199], v86 offset:60416\n"
    "v_mfma_f32_16x16x32_bf16 v[120:123], a[218:221], a[144:147], v[120:123]\n"
    "ds_read_b64_tr_b16 a[200:201], v26 offset:5120\n"
    "v_mfma_f32_16x16x32_bf16 v[120:123], a[222:225], a[152:155], v[120:123]\n"
    "ds_read_b64_tr_b16 a[202:203], v86 offset:61440\n"
    "buffer_load_dwordx4 v24, s[20:23], s84 offen offset:2048 lds\n"
    "v_mfma_f32_16x16x32_bf16 v[120:123], a[226:229], v[244:247], v[120:123]\n"
    "ds_read_b64_tr_b16 a[204:205], v26 offset:6144\n"
    "v_mfma_f32_16x16x32_bf16 v[124:127], a[230:233], v[196:199], v[124:127]\n"
    "ds_read_b64_tr_b16 a[206:207], v27 offset:57344\n"
    "v_mfma_f32_16x16x32_bf16 v[124:127], a[234:237], v[204:207], v[124:127]\n"
    "ds_read_b64_tr_b16 a[208:209], v63 offset:2048\n"
    "s_waitcnt lgkmcnt(7)\n"
    "v_mfma_f32_16x16x32_bf16 v[124:127], a[238:241], v[252:255], v[124:127]\n"
    "ds_read_b64_tr_b16 a[210:211], v27 offset:58368\n"
    "v_mfma_f32_16x16x32_bf16 v[124:127], a[242:245], v[6:9], v[124:127]\n"
    "ds_read_b64_tr_b16 a[212:213], v63 offset:3072\n"
    "v_mfma_f32_16x16x32_bf16 v[124:127], a[246:249], v[160:163], v[124:127]\n"
    "ds_read_b64_tr_b16 a[214:215], v27 offset:59392\n"
    "v_mfma_f32_16x16x32_bf16 v[140:143], a[230:233], v[200:203], v[140:143]\n"
    "ds_read_b64_tr_b16 a[216:217], v63 offset:4096\n"
    "v_mfma_f32_16x16x32_bf16 v[140:143], a[234:237], v[208:211], v[140:143]\n"
    "ds_read_b64_tr_b16 a[218:219], v27 offset:60416\n"
    "buffer_load_dwordx4 v75, s[20:23], s84 offen offset:3072 lds\n"
    "v_mfma_f32_16x16x32_bf16 v[140:143], a[238:241], v[2:5], v[140:143]\n"
    "ds_read_b64_tr_b16 a[220:221], v63 offset:5120\n"
    "s_add_u32 m0, s86, 45056\n"
    "v_mfma_f32_16x16x32_bf16 v[140:143], a[242:245], v[38:41], v[140:143]\n"
    "ds_read_b64_tr_b16 a[222:223], v27 offset:61440\n"
    "s_add_u32 s84, s37, s34\n"
    "v_mfma_f32_16x16x32_bf16 v[140:143], a[246:249], v[66:69], v[140:143]\n"
    "ds_read_b64_tr_b16 a[224:225], v63 offset:6144\n"
    "s_add_u32 s84, s84, 147456\n"
    "v_mfma_f32_16x16x32_bf16 v[128:131], a[250:253], v[196:199], v[128:131]\n"
    "ds_read_b64_tr_b16 a[226:227], v64 offset:57344\n"
    "v_mfma_f32_16x16x32_bf16 v[128:131], a[190:193], v[204:207], v[128:131]\n"
    "ds_read_b64_tr_b16 a[228:229], v65 offset:2048\n"
    "s_waitcnt lgkmcnt(7)\n"
    "v_mfma_f32_16x16x32_bf16 v[128:131], a[194:197], v[252:255], v[128:131]\n"
    "ds_read_b64_tr_b16 a[230:231], v64 offset:58368\n"
    "v_mfma_f32_16x16x32_bf16 v[128:131], a[198:201], v[6:9], v[128:131]\n"
    "ds_read_b64_tr_b16 a[232:233], v65 offset:3072\n"
    "v_mfma_f32_16x16x32_bf16 v[128:131], a[202:205], v[160:163], v[128:131]\n"
    "ds_read_b64_tr_b16 a[234:235], v64 offset:59392\n"
    "buffer_load_dwordx4 v56, s[20:23], s89 offen offset:0 lds\n"
    "v_mfma_f32_16x16x32_bf16 v[144:147], a[250:253], v[200:203], v[144:147]\n"
    "ds_read_b64_tr_b16 a[236:237], v65 offset:4096\n"
    "s_add_u32 s89, s41, s34\n"
    "v_mfma_f32_16x16x32_bf16 v[144:147], a[190:193], v[208:211], v[144:147]\n"
    "ds_read_b64_tr_b16 a[238:239], v64 offset:60416\n"
    "v_mfma_f32_16x16x32_bf16 v[144:147], a[194:197], v[2:5], v[144:147]\n"
    "ds_read_b64_tr_b16 a[240:241], v65 offset:5120\n"
    "v_mfma_f32_16x16x32_bf16 v[144:147], a[198:201], v[38:41], v[144:147]\n"
    "ds_read_b64_tr_b16 a[242:243], v64 offset:61440\n"
    "v_mfma_f32_16x16x32_bf16 v[144:147], a[202:205], v[66:69], v[144:147]\n"
    "ds_read_b64_tr_b16 a[244:245], v65 offset:6144\n"
    "v_mfma_f32_16x16x32_bf16 v[132:135], a[206:209], v[196:199], v[132:135]\n"
    "v_mfma_f32_16x16x32_bf16 v[132:135], a[210:213], v[204:207], v[132:135]\n"
    "s_waitcnt lgkmcnt(5)\n"
    "v_mfma_f32_16x16x32_bf16 v[132:135], a[214:217], v[252:255], v[132:135]\n"
    "s_add_u32 m0, s86, 61440\n"
    "v_mfma_f32_16x16x32_bf16 v[132:135], a[218:221], v[6:9], v[132:135]\n"
    "buffer_load_dwordx4 v56, s[20:23], s84 offen offset:0 lds\n"
    "v_mfma_f32_16x16x32_bf16 v[132:135], a[222:225], v[160:163], v[132:135]\n"
    "ds_read_b64_tr_b16 a[246:247], v29 offset:12288\n"
    "v_mfma_f32_16x16x32_bf16 v[148:151], a[206:209], v[200:203], v[148:151]\n"
    "ds_read_b64_tr_b16 a[248:249], v29 offset:22528\n"
    "v_mfma_f32_16x16x32_bf16 v[148:151], a[210:213], v[208:211], v[148:151]\n"
    "ds_read_b64_tr_b16 a[250:251], v29 offset:13312\n"
    "v_mfma_f32_16x16x32_bf16 v[148:151], a[214:217], v[2:5], v[148:151]\n"
    "ds_read_b64_tr_b16 a[252:253], v29 offset:23552\n"
    "v_mfma_f32_16x16x32_bf16 v[148:151], a[218:221], v[38:41], v[148:151]\n"
    "ds_read_b64_tr_b16 a[190:191], v29 offset:14336\n"
    "v_mfma_f32_16x16x32_bf16 v[148:151], a[222:225], v[66:69], v[148:151]\n"
    "ds_read_b64_tr_b16 a[192:193], v29 offset:24576\n"
    "s_waitcnt vmcnt(11) lgkmcnt(6)\n"
    "v_mfma_f32_16x16x32_bf16 v[136:139], a[226:229], v[196:199], v[136:139]\n"
    "ds_read_b64_tr_b16 a[194:195], v29 offset:15360\n"
    "buffer_store_dword a42, v19, s[28:31], s90 offen offset:512 sc1\n"
    "v_mfma_f32_16x16x32_bf16 v[136:139], a[230:233], v[204:207], v[136:139]\n"
    "ds_read_b64_tr_b16 a[196:197], v29 offset:25600\n"
    "v_mfma_f32_16x16x32_bf16 v[136:139], a[234:237], v[252:255], v[136:139]\n"
    "ds_read_b64_tr_b16 a[198:199], v29 offset:16384\n"
    "buffer_load_dwordx4 v57, s[20:23], s84 offen offset:1024 lds\n"
    "v_mfma_f32_16x16x32_bf16 v[136:139], a[238:241], v[6:9], v[136:139]\n"
    "ds_read_b64_tr_b16 a[200:201], v29 offset:26624\n"
    "v_mfma_f32_16x16x32_bf16 v[136:139], a[242:245], v[160:163], v[136:139]\n"
    "ds_read_b64_tr_b16 a[202:203], v26 offset:12288\n"
    "v_mfma_f32_16x16x32_bf16 v[152:155], a[226:229], v[200:203], v[152:155]\n"
    "ds_read_b64_tr_b16 a[204:205], v26 offset:22528\n"
    "v_mfma_f32_16x16x32_bf16 v[152:155], a[230:233], v[208:211], v[152:155]\n"
    "ds_read_b64_tr_b16 a[206:207], v26 offset:13312\n"
    "v_mfma_f32_16x16x32_bf16 v[152:155], a[234:237], v[2:5], v[152:155]\n"
    "ds_read_b64_tr_b16 a[208:209], v26 offset:23552\n"
    "v_mfma_f32_16x16x32_bf16 v[152:155], a[238:241], v[38:41], v[152:155]\n"
    "ds_read_b64_tr_b16 a[210:211], v26 offset:14336\n"
    "buffer_load_dwordx4 v24, s[20:23], s84 offen offset:2048 lds\n"
    "v_mfma_f32_16x16x32_bf16 v[152:155], a[242:245], v[66:69], v[152:155]\n"
    "ds_read_b64_tr_b16 a[212:213], v26 offset:24576\n"
    "v_mfma_f32_16x16x4_4b_bf16 v[92:107], a[54:55], v[70:71], v[92:107]\n"
    "ds_read_b64_tr_b16 a[214:215], v26 offset:15360\n"
    "v_mfma_f32_16x16x4_4b_bf16 v[108:123], a[54:55], v[72:73], v[108:123]\n"
    "ds_read_b64_tr_b16 a[216:217], v26 offset:25600\n"
    "s_waitcnt vmcnt(12) lgkmcnt(6)\n"
    "v_mfma_f32_16x16x4_4b_bf16 v[92:107], a[56:57], v[164:165], v[92:107]\n"
    "ds_read_b64_tr_b16 a[218:219], v26 offset:16384\n"
    "v_mfma_f32_16x16x4_4b_bf16 v[108:123], a[56:57], v[166:167], v[108:123]\n"
    "ds_read_b64_tr_b16 a[220:221], v26 offset:26624\n"
    "s_nop 6\n"
    "v_mfma_f32_16x16x32_bf16 v[92:95], a[246:249], a[156:159], v[92:95]\n"
    "ds_read_b64_tr_b16 a[222:223], v63 offset:12288\n"
    "v_mfma_f32_16x16x32_bf16 v[92:95], a[250:253], a[164:167], v[92:95]\n"
    "ds_read_b64_tr_b16 a[224:225], v63 offset:22528\n"
    "v_mfma_f32_16x16x32_bf16 v[92:95], a[190:193], a[172:175], v[92:95]\n"
    "ds_read_b64_tr_b16 a[226:227], v63 offset:13312\n"
    "buffer_load_dwordx4 v75, s[20:23], s84 offen offset:3072 lds\n"
    "v_mfma_f32_16x16x32_bf16 v[92:95], a[194:197], a[180:183], v[92:95]\n"
    "ds_read_b64_tr_b16 a[228:229], v63 offset:23552\n"
    "s_waitcnt vmcnt(11) lgkmcnt(5)\n"
    "v_mfma_f32_16x16x32_bf16 v[92:95], a[198:201], a[34:37], v[92:95]\n"
    "ds_read_b64_tr_b16 a[230:231], v63 offset:14336\n"
    "s_add_u32 m0, s86, 65536\n"
    "v_mfma_f32_16x16x32_bf16 v[108:111], a[246:249], a[160:163], v[108:111]\n"
    "ds_read_b64_tr_b16 a[232:233], v63 offset:24576\n"
    "s_add_u32 s84, s38, s34\n"
    "v_mfma_f32_16x16x32_bf16 v[108:111], a[250:253], a[168:171], v[108:111]\n"
    "ds_read_b64_tr_b16 a[234:235], v63 offset:15360\n"
    "v_mfma_f32_16x16x32_bf16 v[108:111], a[190:193], a[176:179], v[108:111]\n"
    "ds_read_b64_tr_b16 a[236:237], v63 offset:25600\n"
    "v_cvt_pk_bf16_f32 v176, v92, v93\n"
    "v_mfma_f32_16x16x32_bf16 v[108:111], a[194:197], a[184:187], v[108:111]\n"
    "ds_read_b64_tr_b16 a[238:239], v63 offset:16384\n"
    "s_waitcnt vmcnt(10) lgkmcnt(7)\n"
    "v_mfma_f32_16x16x32_bf16 v[108:111], a[198:201], a[70:73], v[108:111]\n"
    "ds_read_b64_tr_b16 a[240:241], v63 offset:26624\n"
    "v_cvt_pk_bf16_f32 v177, v94, v95\n"
    "v_mfma_f32_16x16x32_bf16 v[96:99], a[202:205], a[156:159], v[96:99]\n"
    "ds_read_b64_tr_b16 a[242:243], v65 offset:12288\n"
    "buffer_load_dwordx4 v56, s[12:15], s89 offen offset:0 lds\n"
    "v_mfma_f32_16x16x32_bf16 v[96:99], a[206:209], a[164:167], v[96:99]\n"
    "ds_read_b64_tr_b16 a[244:245], v65 offset:22528\n"
    "s_add_u32 s89, s40, s34\n"
    "v_mfma_f32_16x16x32_bf16 v[96:99], a[210:213], a[172:175], v[96:99]\n"
    "ds_read_b64_tr_b16 a[246:247], v65 offset:13312\n"
    "v_cvt_pk_bf16_f32 v180, v108, v109\n"
    "v_mfma_f32_16x16x32_bf16 v[96:99], a[214:217], a[180:183], v[96:99]\n"
    "ds_read_b64_tr_b16 a[248:249], v65 offset:23552\n"
    "v_cvt_pk_bf16_f32 v181, v110, v111\n"
    "v_mfma_f32_16x16x32_bf16 v[96:99], a[218:221], a[34:37], v[96:99]\n"
    "ds_read_b64_tr_b16 a[250:251], v65 offset:14336\n"
    "v_mfma_f32_16x16x32_bf16 v[112:115], a[202:205], a[160:163], v[112:115]\n"
    "ds_read_b64_tr_b16 a[252:253], v65 offset:24576\n"
    "v_mfma_f32_16x16x32_bf16 v[112:115], a[206:209], a[168:171], v[112:115]\n"
    "ds_read_b64_tr_b16 a[190:191], v65 offset:15360\n"
    "v_mfma_f32_16x16x32_bf16 v[112:115], a[210:213], a[176:179], v[112:115]\n"
    "ds_read_b64_tr_b16 a[192:193], v65 offset:25600\n"
    "v_mfma_f32_16x16x32_bf16 v[112:115], a[214:217], a[184:187], v[112:115]\n"
    "s_barrier\n"
    "ds_read_b64_tr_b16 a[194:195], v65 offset:16384\n"
    "v_mfma_f32_16x16x32_bf16 v[112:115], a[218:221], a[70:73], v[112:115]\n"
    "ds_read_b64_tr_b16 a[196:197], v65 offset:26624\n"
    "s_add_u32 m0, s86, 81920\n"
    "v_mfma_f32_16x16x32_bf16 v[100:103], a[222:225], a[156:159], v[100:103]\n"
    "ds_read_b64_tr_b16 a[198:199], v42 offset:0\n"
    "s_waitcnt lgkmcnt(7)\n"
    "v_mfma_f32_16x16x32_bf16 v[100:103], a[226:229], a[164:167], v[100:103]\n"
    "ds_read_b64_tr_b16 a[200:201], v42 offset:10240\n"
    "buffer_load_dwordx4 v56, s[20:23], s84 offen offset:0 lds\n"
    "v_mfma_f32_16x16x32_bf16 v[100:103], a[230:233], a[172:175], v[100:103]\n"
    "ds_read_b64_tr_b16 a[202:203], v42 offset:1024\n"
    "v_cvt_pk_bf16_f32 v178, v96, v97\n"
    "v_mfma_f32_16x16x32_bf16 v[100:103], a[234:237], a[180:183], v[100:103]\n"
    "ds_read_b64_tr_b16 a[204:205], v42 offset:11264\n"
    "v_cvt_pk_bf16_f32 v179, v98, v99\n"
    "v_mfma_f32_16x16x32_bf16 v[100:103], a[238:241], a[34:37], v[100:103]\n"
    "buffer_store_dwordx4 v[176:179], v28, s[28:31], s48 offen offset:0 sc1\n"
    "ds_read_b64_tr_b16 a[206:207], v42 offset:2048\n"
    "v_mfma_f32_16x16x32_bf16 v[116:119], a[222:225], a[160:163], v[116:119]\n"
    "ds_read_b64_tr_b16 a[208:209], v42 offset:12288\n"
    "v_cvt_pk_bf16_f32 v182, v112, v113\n"
    "v_mfma_f32_16x16x32_bf16 v[116:119], a[226:229], a[168:171], v[116:119]\n"
    "ds_read_b64_tr_b16 a[210:211], v42 offset:3072\n"
    "v_cvt_pk_bf16_f32 v183, v114, v115\n"
    "v_mfma_f32_16x16x32_bf16 v[116:119], a[230:233], a[176:179], v[116:119]\n"
    "ds_read_b64_tr_b16 a[212:213], v42 offset:13312\n"
    "v_cvt_pk_bf16_f32 v188, v100, v101\n"
    "v_mfma_f32_16x16x32_bf16 v[116:119], a[234:237], a[184:187], v[116:119]\n"
    "ds_read_b64_tr_b16 a[214:215], v42 offset:4096\n"
    "buffer_load_dwordx4 v57, s[20:23], s84 offen offset:1024 lds\n"
    "v_mfma_f32_16x16x32_bf16 v[116:119], a[238:241], a[70:73], v[116:119]\n"
    "ds_read_b64_tr_b16 a[216:217], v42 offset:14336\n"
    "v_cvt_pk_bf16_f32 v189, v102, v103\n"
    "v_mfma_f32_16x16x32_bf16 v[104:107], a[242:245], a[156:159], v[104:107]\n"
    "ds_read_b64_tr_b16 a[218:219], v87 offset:0\n"
    "v_mfma_f32_16x16x32_bf16 v[104:107], a[246:249], a[164:167], v[104:107]\n"
    "ds_read_b64_tr_b16 a[220:221], v87 offset:10240\n"
    "s_waitcnt lgkmcnt(7)\n"
    "v_mfma_f32_16x16x32_bf16 v[104:107], a[250:253], a[172:175], v[104:107]\n"
    "ds_read_b64_tr_b16 a[222:223], v87 offset:1024\n"
    "v_cvt_pk_bf16_f32 v168, v116, v117\n"
    "v_mfma_f32_16x16x32_bf16 v[104:107], a[190:193], a[180:183], v[104:107]\n"
    "ds_read_b64_tr_b16 a[224:225], v87 offset:11264\n"
    "v_cvt_pk_bf16_f32 v169, v118, v119\n"
    "v_mfma_f32_16x16x32_bf16 v[104:107], a[194:197], a[34:37], v[104:107]\n"
    "ds_read_b64_tr_b16 a[226:227], v87 offset:2048\n"
    "buffer_load_dwordx4 v24, s[20:23], s84 offen offset:2048 lds\n"
    "v_mfma_f32_16x16x32_bf16 v[120:123], a[242:245], a[160:163], v[120:123]\n"
    "ds_read_b64_tr_b16 a[228:229], v87 offset:12288\n"
    "v_mfma_f32_16x16x32_bf16 v[120:123], a[246:249], a[168:171], v[120:123]\n"
    "ds_read_b64_tr_b16 a[230:231], v87 offset:3072\n"
    "v_mfma_f32_16x16x32_bf16 v[120:123], a[250:253], a[176:179], v[120:123]\n"
    "ds_read_b64_tr_b16 a[232:233], v87 offset:13312\n"
    "v_cvt_pk_bf16_f32 v190, v104, v105\n"
    "v_mfma_f32_16x16x32_bf16 v[120:123], a[190:193], a[184:187], v[120:123]\n"
    "ds_read_b64_tr_b16 a[234:235], v87 offset:4096\n"
    "v_cvt_pk_bf16_f32 v191, v106, v107\n"
    "v_mfma_f32_16x16x32_bf16 v[120:123], a[194:197], a[70:73], v[120:123]\n"
    "ds_read_b64_tr_b16 a[236:237], v87 offset:14336\n"
    "v_mfma_f32_16x16x32_bf16 v[124:127], a[198:201], a[2:5], v[124:127]\n"
    "buffer_store_dwordx4 v[180:183], v28, s[28:31], s48 offen offset:1024 sc1\n"
    "ds_read_b64_tr_b16 a[238:239], v89 offset:0\n"
    "v_mfma_f32_16x16x32_bf16 v[124:127], a[202:205], a[132:135], v[124:127]\n"
    "ds_read_b64_tr_b16 a[240:241], v89 offset:10240\n"
    "s_waitcnt lgkmcnt(6)\n"
    "v_mfma_f32_16x16x32_bf16 v[124:127], a[206:209], a[140:143], v[124:127]\n"
    "ds_read_b64_tr_b16 a[242:243], v89 offset:1024\n"
    "v_cvt_pk_bf16_f32 v170, v120, v121\n"
    "v_mfma_f32_16x16x32_bf16 v[124:127], a[210:213], a[148:151], v[124:127]\n"
    "ds_read_b64_tr_b16 a[244:245], v89 offset:11264\n"
    "v_cvt_pk_bf16_f32 v171, v122, v123\n"
    "v_mfma_f32_16x16x32_bf16 v[124:127], a[214:217], v[240:243], v[124:127]\n"
    "ds_read_b64_tr_b16 a[246:247], v89 offset:2048\n"
    "buffer_load_dwordx4 v75, s[20:23], s84 offen offset:3072 lds\n"
    "v_mfma_f32_16x16x32_bf16 v[140:143], a[198:201], a[20:23], v[140:143]\n"
    "ds_read_b64_tr_b16 a[248:249], v89 offset:12288\n"
    "s_add_u32 m0, s86, 86016\n"
    "v_mfma_f32_16x16x32_bf16 v[140:143], a[202:205], a[136:139], v[140:143]\n"
    "ds_read_b64_tr_b16 a[250:251], v89 offset:3072\n"
    "s_add_u32 s84, s38, s34\n"
    "v_mfma_f32_16x16x32_bf16 v[140:143], a[206:209], a[144:147], v[140:143]\n"
    "ds_read_b64_tr_b16 a[252:253], v89 offset:13312\n"
    "s_add_u32 s84, s84, 147456\n"
    "v_mfma_f32_16x16x32_bf16 v[140:143], a[210:213], a[152:155], v[140:143]\n"
    "ds_read_b64_tr_b16 a[190:191], v89 offset:4096\n"
    "v_mfma_f32_16x16x32_bf16 v[140:143], a[214:217], v[244:247], v[140:143]\n"
    "ds_read_b64_tr_b16 a[192:193], v89 offset:14336\n"
    "v_mfma_f32_16x16x32_bf16 v[128:131], a[218:221], a[2:5], v[128:131]\n"
    "ds_read_b64_tr_b16 a[194:195], v90 offset:0\n"
    "buffer_load_dwordx4 v56, s[20:23], s89 offen offset:0 lds\n"
    "v_mfma_f32_16x16x32_bf16 v[128:131], a[222:225], a[132:135], v[128:131]\n"
    "ds_read_b64_tr_b16 a[196:197], v90 offset:10240\n"
    "s_add_u32 s89, s42, s34\n"
    "v_mfma_f32_16x16x32_bf16 v[128:131], a[226:229], a[140:143], v[128:131]\n"
    "ds_read_b64_tr_b16 a[198:199], v90 offset:1024\n"
    "s_waitcnt lgkmcnt(7)\n"
    "v_mfma_f32_16x16x32_bf16 v[128:131], a[230:233], a[148:151], v[128:131]\n"
    "ds_read_b64_tr_b16 a[200:201], v90 offset:11264\n"
    "v_mfma_f32_16x16x32_bf16 v[128:131], a[234:237], v[240:243], v[128:131]\n"
    "ds_read_b64_tr_b16 a[202:203], v90 offset:2048\n"
    "v_mfma_f32_16x16x32_bf16 v[144:147], a[218:221], a[20:23], v[144:147]\n"
    "ds_read_b64_tr_b16 a[204:205], v90 offset:12288\n"
    "v_mfma_f32_16x16x32_bf16 v[144:147], a[222:225], a[136:139], v[144:147]\n"
    "buffer_store_dwordx4 v[188:191], v28, s[28:31], s83 offen offset:0 sc1\n"
    "ds_read_b64_tr_b16 a[206:207], v90 offset:3072\n"
    "v_mfma_f32_16x16x32_bf16 v[144:147], a[226:229], a[144:147], v[144:147]\n"
    "ds_read_b64_tr_b16 a[208:209], v90 offset:13312\n"
    "v_mfma_f32_16x16x32_bf16 v[144:147], a[230:233], a[152:155], v[144:147]\n"
    "ds_read_b64_tr_b16 a[210:211], v90 offset:4096\n"
    "s_add_u32 m0, s86, 102400\n"
    "v_mfma_f32_16x16x32_bf16 v[144:147], a[234:237], v[244:247], v[144:147]\n"
    "ds_read_b64_tr_b16 a[212:213], v90 offset:14336\n"
    "v_mfma_f32_16x16x32_bf16 v[132:135], a[238:241], a[2:5], v[132:135]\n"
    "buffer_load_dwordx4 v56, s[20:23], s84 offen offset:0 lds\n"
    "v_mfma_f32_16x16x32_bf16 v[132:135], a[242:245], a[132:135], v[132:135]\n"
    "v_mfma_f32_16x16x32_bf16 v[132:135], a[246:249], a[140:143], v[132:135]\n"
    "s_waitcnt lgkmcnt(4)\n"
    "v_mfma_f32_16x16x32_bf16 v[132:135], a[250:253], a[148:151], v[132:135]\n"
    "v_mfma_f32_16x16x32_bf16 v[132:135], a[190:193], v[240:243], v[132:135]\n"
    "ds_read_b64_tr_b16 a[214:215], v42 offset:20480\n"
    "v_mfma_f32_16x16x32_bf16 v[148:151], a[238:241], a[20:23], v[148:151]\n"
    "ds_read_b64_tr_b16 a[216:217], v42 offset:30720\n"
    "v_mfma_f32_16x16x32_bf16 v[148:151], a[242:245], a[136:139], v[148:151]\n"
    "ds_read_b64_tr_b16 a[218:219], v42 offset:21504\n"
    "buffer_load_dwordx4 v57, s[20:23], s84 offen offset:1024 lds\n"
    "v_mfma_f32_16x16x32_bf16 v[148:151], a[246:249], a[144:147], v[148:151]\n"
    "ds_read_b64_tr_b16 a[220:221], v42 offset:31744\n"
    "v_mfma_f32_16x16x32_bf16 v[148:151], a[250:253], a[152:155], v[148:151]\n"
    "ds_read_b64_tr_b16 a[222:223], v42 offset:22528\n"
    "v_mfma_f32_16x16x32_bf16 v[148:151], a[190:193], v[244:247], v[148:151]\n"
    "ds_read_b64_tr_b16 a[224:225], v42 offset:32768\n"
    "v_mfma_f32_16x16x32_bf16 v[136:139], a[194:197], a[2:5], v[136:139]\n"
    "ds_read_b64_tr_b16 a[226:227], v42 offset:23552\n"
    "v_mfma_f32_16x16x32_bf16 v[136:139], a[198:201], a[132:135], v[136:139]\n"
    "ds_read_b64_tr_b16 a[228:229], v42 offset:33792\n"
    "v_mfma_f32_16x16x32_bf16 v[136:139], a[202:205], a[140:143], v[136:139]\n"
    "buffer_store_dwordx4 v[168:171], v28, s[28:31], s83 offen offset:1024 sc1\n"
    "ds_read_b64_tr_b16 a[230:231], v42 offset:24576\n"
    "s_waitcnt lgkmcnt(6)\n"
    "v_mfma_f32_16x16x32_bf16 v[136:139], a[206:209], a[148:151], v[136:139]\n"
    "ds_read_b64_tr_b16 a[232:233], v42 offset:34816\n"
    "s_add_u32 s83, s49, s36\n"
    "v_mfma_f32_16x16x32_bf16 v[136:139], a[210:213], v[240:243], v[136:139]\n"
    "ds_read_b64_tr_b16 a[234:235], v87 offset:20480\n"
    "v_mfma_f32_16x16x32_bf16 v[152:155], a[194:197], a[20:23], v[152:155]\n"
    "ds_read_b64_tr_b16 a[236:237], v87 offset:30720\n"
    "v_mfma_f32_16x16x32_bf16 v[152:155], a[198:201], a[136:139], v[152:155]\n"
    "ds_read_b64_tr_b16 a[238:239], v87 offset:21504\n"
    "buffer_load_dwordx4 v24, s[20:23], s84 offen offset:2048 lds\n"
    "v_mfma_f32_16x16x32_bf16 v[152:155], a[202:205], a[144:147], v[152:155]\n"
    "ds_read_b64_tr_b16 a[240:241], v87 offset:31744\n"
    "v_mfma_f32_16x16x32_bf16 v[152:155], a[206:209], a[152:155], v[152:155]\n"
    "ds_read_b64_tr_b16 a[242:243], v87 offset:22528\n"
    "v_mfma_f32_16x16x32_bf16 v[152:155], a[210:213], v[244:247], v[152:155]\n"
    "ds_read_b64_tr_b16 a[244:245], v87 offset:32768\n"
    "v_mfma_f32_16x16x4_4b_bf16 v[124:139], a[58:59], v[70:71], v[124:139]\n"
    "ds_read_b64_tr_b16 a[246:247], v87 offset:23552\n"
    "s_nop 2\n"
    "v_mfma_f32_16x16x4_4b_bf16 v[140:155], a[58:59], v[72:73], v[140:155]\n"
    "ds_read_b64_tr_b16 a[248:249], v87 offset:33792\n"
    "v_mfma_f32_16x16x4_4b_bf16 v[124:139], a[60:61], v[164:165], v[124:139]\n"
    "ds_read_b64_tr_b16 a[250:251], v87 offset:24576\n"
    "buffer_load_dwordx4 v75, s[20:23], s84 offen offset:3072 lds\n"
    "s_add_u32 m0, s86, 106496\n"
    "s_add_u32 s84, s37, s35\n"
    "v_mfma_f32_16x16x4_4b_bf16 v[140:155], a[60:61], v[166:167], v[140:155]\n"
    "ds_read_b64_tr_b16 a[252:253], v87 offset:34816\n"
    "s_nop 3\n"
    "v_mfma_f32_16x16x32_bf16 v[124:127], a[214:217], a[156:159], v[124:127]\n"
    "ds_read_b64_tr_b16 a[190:191], v89 offset:20480\n"
    "s_waitcnt lgkmcnt(4)\n"
    "v_mfma_f32_16x16x32_bf16 v[124:127], a[218:221], a[164:167], v[124:127]\n"
    "ds_read_b64_tr_b16 a[192:193], v89 offset:30720\n"
    "v_mfma_f32_16x16x32_bf16 v[124:127], a[222:225], a[172:175], v[124:127]\n"
    "ds_read_b64_tr_b16 a[194:195], v89 offset:21504\n"
    "v_mfma_f32_16x16x32_bf16 v[124:127], a[226:229], a[180:183], v[124:127]\n"
    "ds_read_b64_tr_b16 a[196:197], v89 offset:31744\n"
    "buffer_load_dwordx4 v56, s[12:15], s89 offen offset:0 lds\n"
    "v_mfma_f32_16x16x32_bf16 v[124:127], a[230:233], a[34:37], v[124:127]\n"
    "ds_read_b64_tr_b16 a[198:199], v89 offset:22528\n"
    "s_add_u32 s89, s39, s35\n"
    "v_mfma_f32_16x16x32_bf16 v[140:143], a[214:217], a[160:163], v[140:143]\n"
    "ds_read_b64_tr_b16 a[200:201], v89 offset:32768\n"
    "v_mfma_f32_16x16x32_bf16 v[140:143], a[218:221], a[168:171], v[140:143]\n"
    "ds_read_b64_tr_b16 a[202:203], v89 offset:23552\n"
    "v_mfma_f32_16x16x32_bf16 v[140:143], a[222:225], a[176:179], v[140:143]\n"
    "ds_read_b64_tr_b16 a[204:205], v89 offset:33792\n"
    "v_cvt_pk_bf16_f32 v172, v124, v125\n"
    "v_mfma_f32_16x16x32_bf16 v[140:143], a[226:229], a[184:187], v[140:143]\n"
    "ds_read_b64_tr_b16 a[206:207], v89 offset:24576\n"
    "v_cvt_pk_bf16_f32 v173, v126, v127\n"
    "v_mfma_f32_16x16x32_bf16 v[140:143], a[230:233], a[70:73], v[140:143]\n"
    "ds_read_b64_tr_b16 a[208:209], v89 offset:34816\n"
    "v_mfma_f32_16x16x32_bf16 v[128:131], a[234:237], a[156:159], v[128:131]\n"
    "ds_read_b64_tr_b16 a[210:211], v90 offset:20480\n"
    "v_mfma_f32_16x16x32_bf16 v[128:131], a[238:241], a[164:167], v[128:131]\n"
    "ds_read_b64_tr_b16 a[212:213], v90 offset:30720\n"
    "v_mfma_f32_16x16x32_bf16 v[128:131], a[242:245], a[172:175], v[128:131]\n"
    "ds_read_b64_tr_b16 a[214:215], v90 offset:21504\n"
    "s_waitcnt lgkmcnt(7)\n"
    "v_mfma_f32_16x16x32_bf16 v[128:131], a[246:249], a[180:183], v[128:131]\n"
    "ds_read_b64_tr_b16 a[216:217], v90 offset:31744\n"
    "v_cvt_pk_bf16_f32 v58, v140, v141\n"
    "v_mfma_f32_16x16x32_bf16 v[128:131], a[250:253], a[34:37], v[128:131]\n"
    "ds_read_b64_tr_b16 a[218:219], v90 offset:22528\n"
    "s_add_u32 m0, s86, 122880\n"
    "v_mfma_f32_16x16x32_bf16 v[144:147], a[234:237], a[160:163], v[144:147]\n"
    "ds_read_b64_tr_b16 a[220:221], v90 offset:32768\n"
    "buffer_load_dwordx4 v56, s[20:23], s84 offen offset:0 lds\n"
    "v_mfma_f32_16x16x32_bf16 v[144:147], a[238:241], a[168:171], v[144:147]\n"
    "ds_read_b64_tr_b16 a[222:223], v90 offset:23552\n"
    "v_cvt_pk_bf16_f32 v59, v142, v143\n"
    "v_mfma_f32_16x16x32_bf16 v[144:147], a[242:245], a[176:179], v[144:147]\n"
    "ds_read_b64_tr_b16 a[224:225], v90 offset:33792\n"
    "v_cvt_pk_bf16_f32 v174, v128, v129\n"
    "v_mfma_f32_16x16x32_bf16 v[144:147], a[246:249], a[184:187], v[144:147]\n"
    "ds_read_b64_tr_b16 a[226:227], v90 offset:24576\n"
    "s_waitcnt vmcnt(15) lgkmcnt(7)\n"
    "v_mfma_f32_16x16x32_bf16 v[144:147], a[250:253], a[70:73], v[144:147]\n"
    "s_barrier\n"
    "ds_read_b64_tr_b16 a[228:229], v90 offset:34816\n"
    "v_mfma_f32_16x16x32_bf16 v[132:135], a[190:193], a[156:159], v[132:135]\n"
    "v_cvt_pk_bf16_f32 v175, v130, v131\n"
    "v_mfma_f32_16x16x32_bf16 v[132:135], a[194:197], a[164:167], v[132:135]\n"
    "v_mfma_f32_16x16x32_bf16 v[132:135], a[198:201], a[172:175], v[132:135]\n"
    "buffer_load_dwordx4 v57, s[20:23], s84 offen offset:1024 lds\n"
    "v_mfma_f32_16x16x32_bf16 v[132:135], a[202:205], a[180:183], v[132:135]\n"
    "v_cvt_pk_bf16_f32 v60, v144, v145\n"
    "v_cvt_pk_bf16_f32 v61, v146, v147\n"
    "v_mfma_f32_16x16x32_bf16 v[132:135], a[206:209], a[34:37], v[132:135]\n"
    "ds_read_b64_tr_b16 a[230:231], v42 offset:40960\n"
    "v_mfma_f32_16x16x32_bf16 v[148:151], a[190:193], a[160:163], v[148:151]\n"
    "buffer_store_dwordx4 v[172:175], v28, s[28:31], s49 offen offset:0 sc1\n"
    "ds_read_b64_tr_b16 a[232:233], v42 offset:51200\n"
    "v_mfma_f32_16x16x32_bf16 v[148:151], a[194:197], a[168:171], v[148:151]\n"
    "ds_read_b64_tr_b16 a[234:235], v42 offset:41984\n"
    "v_mfma_f32_16x16x32_bf16 v[148:151], a[198:201], a[176:179], v[148:151]\n"
    "ds_read_b64_tr_b16 a[236:237], v42 offset:52224\n"
    "v_cvt_pk_bf16_f32 v176, v132, v133\n"
    "v_mfma_f32_16x16x32_bf16 v[148:151], a[202:205], a[184:187], v[148:151]\n"
    "ds_read_b64_tr_b16 a[238:239], v42 offset:43008\n"
    "v_cvt_pk_bf16_f32 v177, v134, v135\n"
    "v_mfma_f32_16x16x32_bf16 v[148:151], a[206:209], a[70:73], v[148:151]\n"
    "ds_read_b64_tr_b16 a[240:241], v42 offset:53248\n"
    "v_mfma_f32_16x16x32_bf16 v[136:139], a[210:213], a[156:159], v[136:139]\n"
    "ds_read_b64_tr_b16 a[242:243], v42 offset:44032\n"
    "s_waitcnt lgkmcnt(6)\n"
    "v_mfma_f32_16x16x32_bf16 v[136:139], a[214:217], a[164:167], v[136:139]\n"
    "ds_read_b64_tr_b16 a[244:245], v42 offset:54272\n"
    "buffer_load_dword v62, v18, s[28:31], 0 offen offset:512 sc1\n"
    "v_mfma_f32_16x16x32_bf16 v[136:139], a[218:221], a[172:175], v[136:139]\n"
    "ds_read_b64_tr_b16 a[246:247], v42 offset:45056\n"
    "v_cvt_pk_bf16_f32 v180, v148, v149\n"
    "v_mfma_f32_16x16x32_bf16 v[136:139], a[222:225], a[180:183], v[136:139]\n"
    "ds_read_b64_tr_b16 a[248:249], v42 offset:55296\n"
    "buffer_load_dwordx4 v24, s[20:23], s84 offen offset:2048 lds\n"
    "v_mfma_f32_16x16x32_bf16 v[136:139], a[226:229], a[34:37], v[136:139]\n"
    "ds_read_b64_tr_b16 a[250:251], v87 offset:40960\n"
    "v_cvt_pk_bf16_f32 v181, v150, v151\n"
    "v_mfma_f32_16x16x32_bf16 v[152:155], a[210:213], a[160:163], v[152:155]\n"
    "ds_read_b64_tr_b16 a[252:253], v87 offset:51200\n"
    "v_mfma_f32_16x16x32_bf16 v[152:155], a[214:217], a[168:171], v[152:155]\n"
    "ds_read_b64_tr_b16 a[190:191], v87 offset:41984\n"
    "v_mfma_f32_16x16x32_bf16 v[152:155], a[218:221], a[176:179], v[152:155]\n"
    "ds_read_b64_tr_b16 a[192:193], v87 offset:52224\n"
    "v_cvt_pk_bf16_f32 v178, v136, v137\n"
    "v_mfma_f32_16x16x32_bf16 v[152:155], a[222:225], a[184:187], v[152:155]\n"
    "ds_read_b64_tr_b16 a[194:195], v87 offset:43008\n"
    "buffer_load_dwordx4 v75, s[20:23], s84 offen offset:3072 lds\n"
    "v_mfma_f32_16x16x32_bf16 v[152:155], a[226:229], a[70:73], v[152:155]\n"
    "ds_read_b64_tr_b16 a[196:197], v87 offset:53248\n"
    "s_add_u32 m0, s86, 126976\n"
    "v_mfma_f32_16x16x4_4b_bf16 v[92:107], a[62:63], v[70:71], 0\n"
    "ds_read_b64_tr_b16 a[198:199], v87 offset:44032\n"
    "buffer_store_dwordx4 v[58:61], v28, s[28:31], s49 offen offset:1024 sc1\n"
    "v_mfma_f32_16x16x4_4b_bf16 v[108:123], a[62:63], v[72:73], 0\n"
    "ds_read_b64_tr_b16 a[200:201], v87 offset:54272\n"
    "s_add_u32 s84, s37, s35\n"
    "s_add_u32 s84, s84, 147456\n"
    "v_cvt_pk_bf16_f32 v179, v138, v139\n"
    "v_cvt_pk_bf16_f32 v182, v152, v153\n"
    "v_cvt_pk_bf16_f32 v183, v154, v155\n"
    "v_mfma_f32_16x16x4_4b_bf16 v[92:107], a[64:65], v[164:165], v[92:107]\n"
    "ds_read_b64_tr_b16 a[202:203], v87 offset:45056\n"
    "v_mfma_f32_16x16x4_4b_bf16 v[108:123], a[64:65], v[166:167], v[108:123]\n"
    "ds_read_b64_tr_b16 a[204:205], v87 offset:55296\n"
    "s_waitcnt lgkmcnt(4)\n"
    "s_nop 5\n"
    "v_mfma_f32_16x16x32_bf16 v[92:95], a[230:233], v[34:37], v[92:95]\n"
    "ds_read_b64_tr_b16 a[206:207], v42 offset:61440\n"
    "v_mfma_f32_16x16x32_bf16 v[92:95], a[234:237], v[50:53], v[92:95]\n"
    "ds_read_b64_tr_b16 a[208:209], v91 offset:6144\n"
    "v_mfma_f32_16x16x32_bf16 v[92:95], a[238:241], v[80:83], v[92:95]\n"
    "ds_read_b64_tr_b16 a[210:211], v42 offset:62464\n"
    "buffer_load_dwordx4 v56, s[20:23], s89 offen offset:0 lds\n"
    "v_mfma_f32_16x16x32_bf16 v[92:95], a[242:245], v[20:23], v[92:95]\n"
    "ds_read_b64_tr_b16 a[212:213], v91 offset:7168\n"
    "s_add_u32 s89, s41, s35\n"
    "v_mfma_f32_16x16x32_bf16 v[92:95], a[246:249], v[224:227], v[92:95]\n"
    "ds_read_b64_tr_b16 a[214:215], v42 offset:63488\n"
    "v_mfma_f32_16x16x32_bf16 v[108:111], a[230:233], v[46:49], v[108:111]\n"
    "ds_read_b64_tr_b16 a[216:217], v91 offset:8192\n"
    "v_mfma_f32_16x16x32_bf16 v[108:111], a[234:237], v[76:79], v[108:111]\n"
    "ds_read_b64_tr_b16 a[218:219], v42 offset:64512\n"
    "v_mfma_f32_16x16x32_bf16 v[108:111], a[238:241], v[10:13], v[108:111]\n"
    "ds_read_b64_tr_b16 a[220:221], v91 offset:9216\n"
    "v_mfma_f32_16x16x32_bf16 v[108:111], a[242:245], v[192:195], v[108:111]\n"
    "ds_read_b64_tr_b16 a[222:223], v91 offset:0\n"
    "v_mfma_f32_16x16x32_bf16 v[108:111], a[246:249], v[228:231], v[108:111]\n"
    "ds_read_b64_tr_b16 a[224:225], v91 offset:10240\n"
    "s_waitcnt vmcnt(11) lgkmcnt(7)\n"
    "v_mfma_f32_16x16x32_bf16 v[96:99], a[250:253], v[34:37], v[96:99]\n"
    "ds_read_b64_tr_b16 a[226:227], v87 offset:61440\n"
    "buffer_store_dword a42, v19, s[28:31], s90 offen offset:640 sc1\n"
    "v_mfma_f32_16x16x32_bf16 v[96:99], a[190:193], v[50:53], v[96:99]\n"
    "ds_read_b64_tr_b16 a[228:229], v86 offset:6144\n"
    "s_add_u32 m0, s86, 143360\n"
    "v_mfma_f32_16x16x32_bf16 v[96:99], a[194:197], v[80:83], v[96:99]\n"
    "buffer_store_dwordx4 v[176:179], v28, s[28:31], s83 offen offset:0 sc1\n"
    "ds_read_b64_tr_b16 a[230:231], v87 offset:62464\n"
    "v_mfma_f32_16x16x32_bf16 v[96:99], a[198:201], v[20:23], v[96:99]\n"
    "ds_read_b64_tr_b16 a[232:233], v86 offset:7168\n"
    "v_mfma_f32_16x16x32_bf16 v[96:99], a[202:205], v[224:227], v[96:99]\n"
    "ds_read_b64_tr_b16 a[234:235], v87 offset:63488\n"
    "v_mfma_f32_16x16x32_bf16 v[112:115], a[250:253], v[46:49], v[112:115]\n"
    "ds_read_b64_tr_b16 a[236:237], v86 offset:8192\n"
    "v_mfma_f32_16x16x32_bf16 v[112:115], a[190:193], v[76:79], v[112:115]\n"
    "ds_read_b64_tr_b16 a[238:239], v87 offset:64512\n"
    "buffer_load_dwordx4 v56, s[20:23], s84 offen offset:0 lds\n"
    "v_mfma_f32_16x16x32_bf16 v[112:115], a[194:197], v[10:13], v[112:115]\n"
    "ds_read_b64_tr_b16 a[240:241], v86 offset:9216\n"
    "v_mfma_f32_16x16x32_bf16 v[112:115], a[198:201], v[192:195], v[112:115]\n"
    "ds_read_b64_tr_b16 a[242:243], v86 offset:0\n"
    "s_waitcnt vmcnt(11) lgkmcnt(7)\n"
    "v_mfma_f32_16x16x32_bf16 v[112:115], a[202:205], v[228:231], v[112:115]\n"
    "s_barrier\n"
    "ds_read_b64_tr_b16 a[244:245], v86 offset:10240\n"
    "v_mfma_f32_16x16x32_bf16 v[92:95], a[206:209], v[196:199], v[92:95]\n"
    "ds_read_b64_tr_b16 a[246:247], v91 offset:16384\n"
    "buffer_load_dwordx4 v57, s[20:23], s84 offen offset:1024 lds\n"
    "v_mfma_f32_16x16x32_bf16 v[92:95], a[210:213], v[204:207], v[92:95]\n"
    "ds_read_b64_tr_b16 a[248:249], v91 offset:26624\n"
    "v_mfma_f32_16x16x32_bf16 v[92:95], a[214:217], v[252:255], v[92:95]\n"
    "ds_read_b64_tr_b16 a[250:251], v91 offset:17408\n"
    "v_mfma_f32_16x16x32_bf16 v[92:95], a[218:221], v[6:9], v[92:95]\n"
    "ds_read_b64_tr_b16 a[252:253], v91 offset:27648\n"
    "v_mfma_f32_16x16x32_bf16 v[92:95], a[222:225], v[160:163], v[92:95]\n"
    "ds_read_b64_tr_b16 a[190:191], v91 offset:18432\n"
    "v_mfma_f32_16x16x32_bf16 v[108:111], a[206:209], v[200:203], v[108:111]\n"
    "ds_read_b64_tr_b16 a[192:193], v91 offset:28672\n"
    "v_mfma_f32_16x16x32_bf16 v[108:111], a[210:213], v[208:211], v[108:111]\n"
    "ds_read_b64_tr_b16 a[194:195], v91 offset:19456\n"
    "buffer_load_dwordx4 v24, s[20:23], s84 offen offset:2048 lds\n"
    "v_mfma_f32_16x16x32_bf16 v[108:111], a[214:217], v[2:5], v[108:111]\n"
    "ds_read_b64_tr_b16 a[196:197], v91 offset:29696\n"
    "v_mfma_f32_16x16x32_bf16 v[108:111], a[218:221], v[38:41], v[108:111]\n"
    "ds_read_b64_tr_b16 a[198:199], v91 offset:20480\n"
    "v_mfma_f32_16x16x32_bf16 v[108:111], a[222:225], v[66:69], v[108:111]\n"
    "buffer_store_dwordx4 v[180:183], v28, s[28:31], s83 offen offset:1024 sc1\n"
    "ds_read_b64_tr_b16 a[200:201], v91 offset:30720\n"
    "v_mfma_f32_16x16x32_bf16 v[96:99], a[226:229], v[196:199], v[96:99]\n"
    "ds_read_b64_tr_b16 a[202:203], v86 offset:16384\n"
    ".p2align 12\n"
    "L_pg10_%=:\n"
    "s_cbranch_execz L_tramp_%=\n"
    "s_waitcnt lgkmcnt(6)\n"
    "v_mfma_f32_16x16x32_bf16 v[96:99], a[230:233], v[204:207], v[96:99]\n"
    "ds_read_b64_tr_b16 a[204:205], v86 offset:26624\n"
    "s_add_u32 s83, s50, s36\n"
    "v_mfma_f32_16x16x32_bf16 v[96:99], a[234:237], v[252:255], v[96:99]\n"
    "ds_read_b64_tr_b16 a[206:207], v86 offset:17408\n"
    "v_mfma_f32_16x16x32_bf16 v[96:99], a[238:241], v[6:9], v[96:99]\n"
    "ds_read_b64_tr_b16 a[208:209], v86 offset:27648\n"
    "v_mfma_f32_16x16x32_bf16 v[96:99], a[242:245], v[160:163], v[96:99]\n"
    "ds_read_b64_tr_b16 a[210:211], v86 offset:18432\n"
    "buffer_load_dwordx4 v75, s[20:23], s84 offen offset:3072 lds\n"
    "v_mfma_f32_16x16x32_bf16 v[112:115], a[226:229], v[200:203], v[112:115]\n"
    "ds_read_b64_tr_b16 a[212:213], v86 offset:28672\n"
    "s_add_u32 m0, s86, 147456\n"
    "v_mfma_f32_16x16x32_bf16 v[112:115], a[230:233], v[208:211], v[112:115]\n"
    "ds_read_b64_tr_b16 a[214:215], v86 offset:19456\n"
    "s_add_u32 s84, s38, s35\n"
    "v_mfma_f32_16x16x32_bf16 v[112:115], a[234:237], v[2:5], v[112:115]\n"
    "ds_read_b64_tr_b16 a[216:217], v86 offset:29696\n"
    "v_mfma_f32_16x16x32_bf16 v[112:115], a[238:241], v[38:41], v[112:115]\n"
    "ds_read_b64_tr_b16 a[218:219], v86 offset:20480\n"
    "v_mfma_f32_16x16x32_bf16 v[112:115], a[242:245], v[66:69], v[112:115]\n"
    "ds_read_b64_tr_b16 a[220:221], v86 offset:30720\n"
    "v_mfma_f32_16x16x32_bf16 v[92:95], a[246:249], a[2:5], v[92:95]\n"
    "ds_read_b64_tr_b16 a[222:223], v91 offset:36864\n"
    "v_mfma_f32_16x16x32_bf16 v[92:95], a[250:253], a[132:135], v[92:95]\n"
    "ds_read_b64_tr_b16 a[224:225], v91 offset:47104\n"
    "s_waitcnt lgkmcnt(7)\n"
    "v_mfma_f32_16x16x32_bf16 v[92:95], a[190:193], a[140:143], v[92:95]\n"
    "ds_read_b64_tr_b16 a[226:227], v91 offset:37888\n"
    "buffer_load_dwordx4 v56, s[12:15], s89 offen offset:0 lds\n"
    "v_mfma_f32_16x16x32_bf16 v[92:95], a[194:197], a[148:151], v[92:95]\n"
    "ds_read_b64_tr_b16 a[228:229], v91 offset:48128\n"
    "s_add_u32 s89, s40, s35\n"
    "v_mfma_f32_16x16x32_bf16 v[92:95], a[198:201], v[240:243], v[92:95]\n"
    "ds_read_b64_tr_b16 a[230:231], v91 offset:38912\n"
    "v_mfma_f32_16x16x32_bf16 v[108:111], a[246:249], a[20:23], v[108:111]\n"
    "ds_read_b64_tr_b16 a[232:233], v91 offset:49152\n"
    "v_mfma_f32_16x16x32_bf16 v[108:111], a[250:253], a[136:139], v[108:111]\n"
    "ds_read_b64_tr_b16 a[234:235], v91 offset:39936\n"
    "v_mfma_f32_16x16x32_bf16 v[108:111], a[190:193], a[144:147], v[108:111]\n"
    "ds_read_b64_tr_b16 a[236:237], v91 offset:50176\n"
    "s_waitcnt vmcnt(12) lgkmcnt(7)\n"
    "v_cmp_eq_u32 vcc, 0, v62\n"
    "v_mfma_f32_16x16x32_bf16 v[108:111], a[194:197], a[152:155], v[108:111]\n"
    "ds_read_b64_tr_b16 a[238:239], v91 offset:40960\n"
    "v_mfma_f32_16x16x32_bf16 v[108:111], a[198:201], v[244:247], v[108:111]\n"
    "ds_read_b64_tr_b16 a[240:241], v91 offset:51200\n"
    "v_mfma_f32_16x16x32_bf16 v[96:99], a[202:205], a[2:5], v[96:99]\n"
    "ds_read_b64_tr_b16 a[242:243], v86 offset:36864\n"
    "s_cbranch_vccnz L_m5_%=\n"
    "L_m5_ret_%=:\n"
    "v_mfma_f32_16x16x32_bf16 v[96:99], a[206:209], a[132:135], v[96:99]\n"
    "ds_read_b64_tr_b16 a[244:245], v86 offset:47104\n"
    "buffer_load_dwordx4 a[74:77], v85, s[28:31], s54 offen offset:0 sc1\n"
    "v_mfma_f32_16x16x32_bf16 v[96:99], a[210:213], a[140:143], v[96:99]\n"
    "ds_read_b64_tr_b16 a[246:247], v86 offset:37888\n"
    "s_add_u32 m0, s86, 0\n"
    "v_mfma_f32_16x16x32_bf16 v[96:99], a[214:217], a[148:151], v[96:99]\n"
    "ds_read_b64_tr_b16 a[248:249], v86 offset:48128\n"
    "v_mfma_f32_16x16x32_bf16 v[96:99], a[218:221], v[240:243], v[96:99]\n"
    "ds_read_b64_tr_b16 a[250:251], v86 offset:38912\n"
    "buffer_load_dwordx4 v56, s[20:23], s84 offen offset:0 lds\n"
    "v_mfma_f32_16x16x32_bf16 v[112:115], a[202:205], a[20:23], v[112:115]\n"
    "ds_read_b64_tr_b16 a[252:253], v86 offset:49152\n"
    "v_mfma_f32_16x16x32_bf16 v[112:115], a[206:209], a[136:139], v[112:115]\n"
    "ds_read_b64_tr_b16 a[190:191], v86 offset:39936\n"
    "v_mfma_f32_16x16x32_bf16 v[112:115], a[210:213], a[144:147], v[112:115]\n"
    "ds_read_b64_tr_b16 a[192:193], v86 offset:50176\n"
    "v_mfma_f32_16x16x32_bf16 v[112:115], a[214:217], a[152:155], v[112:115]\n"
    "ds_read_b64_tr_b16 a[194:195], v86 offset:40960\n"
    "buffer_load_dwordx4 v57, s[20:23], s84 offen offset:1024 lds\n"
    "v_mfma_f32_16x16x32_bf16 v[112:115], a[218:221], v[244:247], v[112:115]\n"
    "ds_read_b64_tr_b16 a[196:197], v86 offset:51200\n"
    "s_waitcnt lgkmcnt(7)\n"
    "v_mfma_f32_16x16x32_bf16 v[92:95], a[222:225], a[156:159], v[92:95]\n"
    "ds_read_b64_tr_b16 a[198:199], v89 offset:40960\n"
    "v_mfma_f32_16x16x32_bf16 v[92:95], a[226:229], a[164:167], v[92:95]\n"
    "ds_read_b64_tr_b16 a[200:201], v89 offset:51200\n"
    "v_mfma_f32_16x16x32_bf16 v[92:95], a[230:233], a[172:175], v[92:95]\n"
    "ds_read_b64_tr_b16 a[202:203], v89 offset:41984\n"
    "v_mfma_f32_16x16x32_bf16 v[92:95], a[234:237], a[180:183], v[92:95]\n"
    "ds_read_b64_tr_b16 a[204:205], v89 offset:52224\n"
    "v_mfma_f32_16x16x32_bf16 v[92:95], a[238:241], a[34:37], v[92:95]\n"
    "ds_read_b64_tr_b16 a[206:207], v89 offset:43008\n"
    "v_mfma_f32_16x16x32_bf16 v[108:111], a[222:225], a[160:163], v[108:111]\n"
    "ds_read_b64_tr_b16 a[208:209], v89 offset:53248\n"
    "buffer_load_dwordx4 a[78:81], v85, s[28:31], s80 offen offset:0 sc1\n"
    "v_mfma_f32_16x16x32_bf16 v[108:111], a[226:229], a[168:171], v[108:111]\n"
    "ds_read_b64_tr_b16 a[210:211], v89 offset:44032\n"
    "s_add_u32 s80, s54, 8192\n"
    "v_mfma_f32_16x16x32_bf16 v[108:111], a[230:233], a[176:179], v[108:111]\n"
    "ds_read_b64_tr_b16 a[212:213], v89 offset:54272\n"
    "v_cvt_pk_bf16_f32 v188, v92, v93\n"
    "v_mfma_f32_16x16x32_bf16 v[108:111], a[234:237], a[184:187], v[108:111]\n"
    "ds_read_b64_tr_b16 a[214:215], v89 offset:45056\n"
    "buffer_load_dwordx4 v24, s[20:23], s84 offen offset:2048 lds\n"
    "v_mfma_f32_16x16x32_bf16 v[108:111], a[238:241], a[70:73], v[108:111]\n"
    "ds_read_b64_tr_b16 a[216:217], v89 offset:55296\n"
    "v_cvt_pk_bf16_f32 v189, v94, v95\n"
    "v_mfma_f32_16x16x32_bf16 v[96:99], a[242:245], a[156:159], v[96:99]\n"
    "ds_read_b64_tr_b16 a[218:219], v90 offset:40960\n"
    "s_waitcnt lgkmcnt(7)\n"
    "v_mfma_f32_16x16x32_bf16 v[96:99], a[246:249], a[164:167], v[96:99]\n"
    "ds_read_b64_tr_b16 a[220:221], v90 offset:51200\n"
    "v_mfma_f32_16x16x32_bf16 v[96:99], a[250:253], a[172:175], v[96:99]\n"
    "ds_read_b64_tr_b16 a[222:223], v90 offset:41984\n"
    "v_cvt_pk_bf16_f32 v168, v108, v109\n"
    "v_mfma_f32_16x16x32_bf16 v[96:99], a[190:193], a[180:183], v[96:99]\n"
    "ds_read_b64_tr_b16 a[224:225], v90 offset:52224\n"
    "v_cvt_pk_bf16_f32 v169, v110, v111\n"
    "v_mfma_f32_16x16x32_bf16 v[96:99], a[194:197], a[34:37], v[96:99]\n"
    "ds_read_b64_tr_b16 a[226:227], v90 offset:43008\n"
    "buffer_load_dwordx4 v75, s[20:23], s84 offen offset:3072 lds\n"
    "v_mfma_f32_16x16x32_bf16 v[112:115], a[242:245], a[160:163], v[112:115]\n"
    "ds_read_b64_tr_b16 a[228:229], v90 offset:53248\n"
    "s_add_u32 m0, s86, 4096\n"
    "v_mfma_f32_16x16x32_bf16 v[112:115], a[246:249], a[168:171], v[112:115]\n"
    "ds_read_b64_tr_b16 a[230:231], v90 offset:44032\n"
    "s_add_u32 s84, s38, s35\n"
    "v_mfma_f32_16x16x32_bf16 v[112:115], a[250:253], a[176:179], v[112:115]\n"
    "ds_read_b64_tr_b16 a[232:233], v90 offset:54272\n"
    "v_cvt_pk_bf16_f32 v190, v96, v97\n"
    "v_mfma_f32_16x16x32_bf16 v[112:115], a[190:193], a[184:187], v[112:115]\n"
    "ds_read_b64_tr_b16 a[234:235], v90 offset:45056\n"
    "v_cvt_pk_bf16_f32 v191, v98, v99\n"
    "v_mfma_f32_16x16x32_bf16 v[112:115], a[194:197], a[70:73], v[112:115]\n"
    "ds_read_b64_tr_b16 a[236:237], v90 offset:55296\n"
    "s_add_u32 s84, s84, 147456\n"
    "v_mfma_f32_16x16x32_bf16 v[100:103], a[198:201], v[34:37], v[100:103]\n"
    "ds_read_b64_tr_b16 a[238:239], v89 offset:61440\n"
    "v_mfma_f32_16x16x32_bf16 v[100:103], a[202:205], v[50:53], v[100:103]\n"
    "ds_read_b64_tr_b16 a[240:241], v27 offset:6144\n"
    "s_waitcnt lgkmcnt(7)\n"
    "v_mfma_f32_16x16x32_bf16 v[100:103], a[206:209], v[80:83], v[100:103]\n"
    "ds_read_b64_tr_b16 a[242:243], v89 offset:62464\n"
    "buffer_load_dwordx4 a[82:85], v85, s[28:31], s80 offen offset:0 sc1\n"
    "v_mfma_f32_16x16x32_bf16 v[100:103], a[210:213], v[20:23], v[100:103]\n"
    "ds_read_b64_tr_b16 a[244:245], v27 offset:7168\n"
    "v_cvt_pk_bf16_f32 v170, v112, v113\n"
    "v_mfma_f32_16x16x32_bf16 v[100:103], a[214:217], v[224:227], v[100:103]\n"
    "ds_read_b64_tr_b16 a[246:247], v89 offset:63488\n"
    "v_cvt_pk_bf16_f32 v171, v114, v115\n"
    "v_mfma_f32_16x16x32_bf16 v[116:119], a[198:201], v[46:49], v[116:119]\n"
    "buffer_store_dwordx4 v[188:191], v28, s[28:31], s50 offen offset:0 sc1\n"
    "ds_read_b64_tr_b16 a[248:249], v27 offset:8192\n"
    "v_mfma_f32_16x16x32_bf16 v[116:119], a[202:205], v[76:79], v[116:119]\n"
    "ds_read_b64_tr_b16 a[250:251], v89 offset:64512\n"
    "s_add_u32 s80, s54, 12288\n"
    "v_mfma_f32_16x16x32_bf16 v[116:119], a[206:209], v[10:13], v[116:119]\n"
    "ds_read_b64_tr_b16 a[252:253], v27 offset:9216\n"
    "v_mfma_f32_16x16x32_bf16 v[116:119], a[210:213], v[192:195], v[116:119]\n"
    "ds_read_b64_tr_b16 a[190:191], v27 offset:0\n"
    "v_mfma_f32_16x16x32_bf16 v[116:119], a[214:217], v[228:231], v[116:119]\n"
    "ds_read_b64_tr_b16 a[192:193], v27 offset:10240\n"
    "buffer_load_dwordx4 v56, s[20:23], s89 offen offset:0 lds\n"
    "v_mfma_f32_16x16x32_bf16 v[104:107], a[218:221], v[34:37], v[104:107]\n"
    "ds_read_b64_tr_b16 a[194:195], v90 offset:61440\n"
    "s_add_u32 m0, s86, 20480\n"
    "v_mfma_f32_16x16x32_bf16 v[104:107], a[222:225], v[50:53], v[104:107]\n"
    "ds_read_b64_tr_b16 a[196:197], v64 offset:6144\n"
    "s_waitcnt lgkmcnt(7)\n"
    "v_mfma_f32_16x16x32_bf16 v[104:107], a[226:229], v[80:83], v[104:107]\n"
    "buffer_store_dwordx4 v[168:171], v28, s[28:31], s50 offen offset:1024 sc1\n"
    "ds_read_b64_tr_b16 a[198:199], v90 offset:62464\n"
    "v_mfma_f32_16x16x32_bf16 v[104:107], a[230:233], v[20:23], v[104:107]\n"
    "ds_read_b64_tr_b16 a[200:201], v64 offset:7168\n"
    "s_add_u32 s89, s42, s35\n"
    "v_mfma_f32_16x16x32_bf16 v[104:107], a[234:237], v[224:227], v[104:107]\n"
    "ds_read_b64_tr_b16 a[202:203], v90 offset:63488\n"
    "v_mfma_f32_16x16x32_bf16 v[120:123], a[218:221], v[46:49], v[120:123]\n"
    "ds_read_b64_tr_b16 a[204:205], v64 offset:8192\n"
    "v_mfma_f32_16x16x32_bf16 v[120:123], a[222:225], v[76:79], v[120:123]\n"
    "ds_read_b64_tr_b16 a[206:207], v90 offset:64512\n"
    "buffer_load_dwordx4 v56, s[20:23], s84 offen offset:0 lds\n"
    "v_mfma_f32_16x16x32_bf16 v[120:123], a[226:229], v[10:13], v[120:123]\n"
    "ds_read_b64_tr_b16 a[208:209], v64 offset:9216\n"
    "v_mfma_f32_16x16x32_bf16 v[120:123], a[230:233], v[192:195], v[120:123]\n"
    "ds_read_b64_tr_b16 a[210:211], v64 offset:0\n"
    "v_mfma_f32_16x16x32_bf16 v[120:123], a[234:237], v[228:231], v[120:123]\n"
    "ds_read_b64_tr_b16 a[212:213], v64 offset:10240\n"
    "buffer_load_dwordx4 a[86:89], v85, s[28:31], s80 offen offset:0 sc1\n"
    "v_mfma_f32_16x16x32_bf16 v[100:103], a[238:241], v[196:199], v[100:103]\n"
    "ds_read_b64_tr_b16 a[214:215], v27 offset:16384\n"
    "v_mfma_f32_16x16x32_bf16 v[100:103], a[242:245], v[204:207], v[100:103]\n"
    "ds_read_b64_tr_b16 a[216:217], v27 offset:26624\n"
    "s_waitcnt lgkmcnt(7)\n"
    "v_mfma_f32_16x16x32_bf16 v[100:103], a[246:249], v[252:255], v[100:103]\n"
    "ds_read_b64_tr_b16 a[218:219], v27 offset:17408\n"
    "buffer_load_dwordx4 v57, s[20:23], s84 offen offset:1024 lds\n"
    "v_mfma_f32_16x16x32_bf16 v[100:103], a[250:253], v[6:9], v[100:103]\n"
    "ds_read_b64_tr_b16 a[220:221], v27 offset:27648\n"
    "v_mfma_f32_16x16x32_bf16 v[100:103], a[190:193], v[160:163], v[100:103]\n"
    "ds_read_b64_tr_b16 a[222:223], v27 offset:18432\n"
    "v_mfma_f32_16x16x32_bf16 v[116:119], a[238:241], v[200:203], v[116:119]\n"
    "ds_read_b64_tr_b16 a[224:225], v27 offset:28672\n"
    "v_mfma_f32_16x16x32_bf16 v[116:119], a[242:245], v[208:211], v[116:119]\n"
    "ds_read_b64_tr_b16 a[226:227], v27 offset:19456\n"
    "buffer_load_dwordx4 v24, s[20:23], s84 offen offset:2048 lds\n"
    "v_mfma_f32_16x16x32_bf16 v[116:119], a[246:249], v[2:5], v[116:119]\n"
    "ds_read_b64_tr_b16 a[228:229], v27 offset:29696\n"
    "v_mfma_f32_16x16x32_bf16 v[116:119], a[250:253], v[38:41], v[116:119]\n"
    "ds_read_b64_tr_b16 a[230:231], v27 offset:20480\n"
    "v_mfma_f32_16x16x32_bf16 v[116:119], a[190:193], v[66:69], v[116:119]\n"
    "ds_read_b64_tr_b16 a[232:233], v27 offset:30720\n"
    "v_mfma_f32_16x16x32_bf16 v[104:107], a[194:197], v[196:199], v[104:107]\n"
    "ds_read_b64_tr_b16 a[234:235], v64 offset:16384\n"
    "v_mfma_f32_16x16x32_bf16 v[104:107], a[198:201], v[204:207], v[104:107]\n"
    "ds_read_b64_tr_b16 a[236:237], v64 offset:26624\n"
    "s_waitcnt lgkmcnt(7)\n"
    "v_mfma_f32_16x16x32_bf16 v[104:107], a[202:205], v[252:255], v[104:107]\n"
    "ds_read_b64_tr_b16 a[238:239], v64 offset:17408\n"
    "v_mfma_f32_16x16x32_bf16 v[104:107], a[206:209], v[6:9], v[104:107]\n"
    "ds_read_b64_tr_b16 a[240:241], v64 offset:27648\n"
    "v_mfma_f32_16x16x32_bf16 v[104:107], a[210:213], v[160:163], v[104:107]\n"
    "ds_read_b64_tr_b16 a[242:243], v64 offset:18432\n"
    "buffer_load_dwordx4 v75, s[20:23], s84 offen offset:3072 lds\n"
    "v_mfma_f32_16x16x32_bf16 v[120:123], a[194:197], v[200:203], v[120:123]\n"
    "ds_read_b64_tr_b16 a[244:245], v64 offset:28672\n"
    "s_add_u32 m0, s86, 24576\n"
    "v_mfma_f32_16x16x32_bf16 v[120:123], a[198:201], v[208:211], v[120:123]\n"
    "ds_read_b64_tr_b16 a[246:247], v64 offset:19456\n"
    "v_mfma_f32_16x16x32_bf16 v[120:123], a[202:205], v[2:5], v[120:123]\n"
    "ds_read_b64_tr_b16 a[248:249], v64 offset:29696\n"
    "v_mfma_f32_16x16x32_bf16 v[120:123], a[206:209], v[38:41], v[120:123]\n"
    "ds_read_b64_tr_b16 a[250:251], v64 offset:20480\n"
    "v_mfma_f32_16x16x32_bf16 v[120:123], a[210:213], v[66:69], v[120:123]\n"
    "ds_read_b64_tr_b16 a[252:253], v64 offset:30720\n"
    "buffer_load_dword v43, v55, s[28:31], 0 offen offset:640 sc1\n"
    "v_mfma_f32_16x16x32_bf16 v[100:103], a[214:217], a[2:5], v[100:103]\n"
    "ds_read_b64_tr_b16 a[190:191], v27 offset:36864\n"
    "v_mfma_f32_16x16x32_bf16 v[100:103], a[218:221], a[132:135], v[100:103]\n"
    "ds_read_b64_tr_b16 a[192:193], v27 offset:47104\n"
    "s_waitcnt lgkmcnt(7)\n"
    "v_mfma_f32_16x16x32_bf16 v[100:103], a[222:225], a[140:143], v[100:103]\n"
    "ds_read_b64_tr_b16 a[194:195], v27 offset:37888\n"
    "buffer_load_dwordx4 v56, s[12:15], s89 offen offset:0 lds\n"
    "v_mfma_f32_16x16x32_bf16 v[100:103], a[226:229], a[148:151], v[100:103]\n"
    "ds_read_b64_tr_b16 a[196:197], v27 offset:48128\n"
    "v_mfma_f32_16x16x32_bf16 v[100:103], a[230:233], v[240:243], v[100:103]\n"
    "ds_read_b64_tr_b16 a[198:199], v27 offset:38912\n"
    "v_mfma_f32_16x16x32_bf16 v[116:119], a[214:217], a[20:23], v[116:119]\n"
    "ds_read_b64_tr_b16 a[200:201], v27 offset:49152\n"
    "v_mfma_f32_16x16x32_bf16 v[116:119], a[218:221], a[136:139], v[116:119]\n"
    "ds_read_b64_tr_b16 a[202:203], v27 offset:39936\n"
    "v_mfma_f32_16x16x32_bf16 v[116:119], a[222:225], a[144:147], v[116:119]\n"
    "ds_read_b64_tr_b16 a[204:205], v27 offset:50176\n"
    "v_mfma_f32_16x16x32_bf16 v[116:119], a[226:229], a[152:155], v[116:119]\n"
    "ds_read_b64_tr_b16 a[206:207], v27 offset:40960\n"
    "v_mfma_f32_16x16x32_bf16 v[116:119], a[230:233], v[244:247], v[116:119]\n"
    "ds_read_b64_tr_b16 a[208:209], v27 offset:51200\n"
    "v_mfma_f32_16x16x32_bf16 v[104:107], a[234:237], a[2:5], v[104:107]\n"
    "ds_read_b64_tr_b16 a[210:211], v64 offset:36864\n"
    "v_mfma_f32_16x16x32_bf16 v[104:107], a[238:241], a[132:135], v[104:107]\n"
    "ds_read_b64_tr_b16 a[212:213], v64 offset:47104\n"
    "s_waitcnt lgkmcnt(7)\n"
    "v_mfma_f32_16x16x32_bf16 v[104:107], a[242:245], a[140:143], v[104:107]\n"
    "ds_read_b64_tr_b16 a[214:215], v64 offset:37888\n"
    "v_mfma_f32_16x16x32_bf16 v[104:107], a[246:249], a[148:151], v[104:107]\n"
    "ds_read_b64_tr_b16 a[216:217], v64 offset:48128\n"
    "v_mfma_f32_16x16x32_bf16 v[104:107], a[250:253], v[240:243], v[104:107]\n"
    "ds_read_b64_tr_b16 a[218:219], v64 offset:38912\n"
    "v_mfma_f32_16x16x32_bf16 v[120:123], a[234:237], a[20:23], v[120:123]\n"
    "ds_read_b64_tr_b16 a[220:221], v64 offset:49152\n"
    "v_mfma_f32_16x16x32_bf16 v[120:123], a[238:241], a[136:139], v[120:123]\n"
    "ds_read_b64_tr_b16 a[222:223], v64 offset:39936\n"
    "v_mfma_f32_16x16x32_bf16 v[120:123], a[242:245], a[144:147], v[120:123]\n"
    "ds_read_b64_tr_b16 a[224:225], v64 offset:50176\n"
    "v_mfma_f32_16x16x32_bf16 v[120:123], a[246:249], a[152:155], v[120:123]\n"
    "ds_read_b64_tr_b16 a[226:227], v64 offset:40960\n"
    "s_waitcnt vmcnt(17) lgkmcnt(7)\n"
    "v_mfma_f32_16x16x32_bf16 v[120:123], a[250:253], v[244:247], v[120:123]\n"
    "s_barrier\n"
    "ds_read_b64_tr_b16 a[228:229], v64 offset:51200\n"
    "v_mfma_f32_16x16x32_bf16 v[100:103], a[190:193], a[156:159], v[100:103]\n"
    "v_mfma_f32_16x16x32_bf16 v[100:103], a[194:197], a[164:167], v[100:103]\n"
    "v_mfma_f32_16x16x32_bf16 v[100:103], a[198:201], a[172:175], v[100:103]\n"
    "v_mfma_f32_16x16x32_bf16 v[100:103], a[202:205], a[180:183], v[100:103]\n"
    "v_mfma_f32_16x16x32_bf16 v[100:103], a[206:209], a[34:37], v[100:103]\n"
    "ds_read_b64_tr_b16 a[230:231], v91 offset:57344\n"
    "v_mfma_f32_16x16x32_bf16 v[116:119], a[190:193], a[160:163], v[116:119]\n"
    "ds_read_b64_tr_b16 a[232:233], v29 offset:2048\n"
    "v_mfma_f32_16x16x32_bf16 v[116:119], a[194:197], a[168:171], v[116:119]\n"
    "ds_read_b64_tr_b16 a[234:235], v91 offset:58368\n"
    "v_mfma_f32_16x16x32_bf16 v[116:119], a[198:201], a[176:179], v[116:119]\n"
    "ds_read_b64_tr_b16 a[236:237], v29 offset:3072\n"
    "v_mfma_f32_16x16x32_bf16 v[116:119], a[202:205], a[184:187], v[116:119]\n"
    "ds_read_b64_tr_b16 a[238:239], v91 offset:59392\n"
    "v_cvt_pk_bf16_f32 v172, v100, v101\n"
    "v_mfma_f32_16x16x32_bf16 v[116:119], a[206:209], a[70:73], v[116:119]\n"
    "ds_read_b64_tr_b16 a[240:241], v29 offset:4096\n"
    "v_cvt_pk_bf16_f32 v173, v102, v103\n"
    "v_mfma_f32_16x16x32_bf16 v[104:107], a[210:213], a[156:159], v[104:107]\n"
    "ds_read_b64_tr_b16 a[242:243], v91 offset:60416\n"
    "s_waitcnt lgkmcnt(7)\n"
    "v_mfma_f32_16x16x32_bf16 v[104:107], a[214:217], a[164:167], v[104:107]\n"
    "ds_read_b64_tr_b16 a[244:245], v29 offset:5120\n"
    "v_mfma_f32_16x16x32_bf16 v[104:107], a[218:221], a[172:175], v[104:107]\n"
    "ds_read_b64_tr_b16 a[246:247], v91 offset:61440\n"
    "v_cvt_pk_bf16_f32 v58, v116, v117\n"
    "v_mfma_f32_16x16x32_bf16 v[104:107], a[222:225], a[180:183], v[104:107]\n"
    "ds_read_b64_tr_b16 a[248:249], v29 offset:6144\n"
    "v_cvt_pk_bf16_f32 v59, v118, v119\n"
    "v_mfma_f32_16x16x32_bf16 v[104:107], a[226:229], a[34:37], v[104:107]\n"
    "ds_read_b64_tr_b16 a[250:251], v86 offset:57344\n"
    "v_mfma_f32_16x16x32_bf16 v[120:123], a[210:213], a[160:163], v[120:123]\n"
    "ds_read_b64_tr_b16 a[252:253], v26 offset:2048\n"
    "v_mfma_f32_16x16x32_bf16 v[120:123], a[214:217], a[168:171], v[120:123]\n"
    "ds_read_b64_tr_b16 a[190:191], v86 offset:58368\n"
    "v_mfma_f32_16x16x32_bf16 v[120:123], a[218:221], a[176:179], v[120:123]\n"
    "ds_read_b64_tr_b16 a[192:193], v26 offset:3072\n"
    "s_waitcnt vmcnt(7) lgkmcnt(7)\n"
    "v_mfma_f32_16x16x32_bf16 v[120:123], a[222:225], a[184:187], v[120:123]\n"
    "ds_read_b64_tr_b16 a[194:195], v86 offset:59392\n"
    "buffer_store_dword a42, v19, s[28:31], s90 offen offset:768 sc1\n"
    "v_mfma_f32_16x16x32_bf16 v[120:123], a[226:229], a[70:73], v[120:123]\n"
    "ds_read_b64_tr_b16 a[196:197], v26 offset:4096\n"
    "v_cvt_pk_bf16_f32 v174, v104, v105\n"
    "v_mfma_f32_16x16x4_4b_bf16 v[124:139], a[66:67], v[70:71], 0\n"
    "ds_read_b64_tr_b16 a[198:199], v86 offset:60416\n"
    "v_cvt_pk_bf16_f32 v175, v106, v107\n"
    "v_mfma_f32_16x16x4_4b_bf16 v[140:155], a[66:67], v[72:73], 0\n"
    "ds_read_b64_tr_b16 a[200:201], v26 offset:5120\n"
    "v_mfma_f32_16x16x4_4b_bf16 v[124:139], a[68:69], v[164:165], v[124:139]\n"
    "ds_read_b64_tr_b16 a[202:203], v86 offset:61440\n"
    "v_cvt_pk_bf16_f32 v60, v120, v121\n"
    "v_cvt_pk_bf16_f32 v61, v122, v123\n"
    "v_mfma_f32_16x16x4_4b_bf16 v[140:155], a[68:69], v[166:167], v[140:155]\n"
    "ds_read_b64_tr_b16 a[204:205], v26 offset:6144\n"
    "s_nop 4\n"
    "v_mfma_f32_16x16x32_bf16 v[124:127], a[230:233], v[34:37], v[124:127]\n"
    "buffer_store_dwordx4 v[172:175], v28, s[28:31], s83 offen offset:0 sc1\n"
    "ds_read_b64_tr_b16 a[206:207], v29 offset:12288\n"
    "v_mfma_f32_16x16x32_bf16 v[124:127], a[234:237], v[50:53], v[124:127]\n"
    "ds_read_b64_tr_b16 a[208:209], v29 offset:22528\n"
    "v_mfma_f32_16x16x32_bf16 v[124:127], a[238:241], v[80:83], v[124:127]\n"
    "ds_read_b64_tr_b16 a[210:211], v29 offset:13312\n"
    "s_waitcnt lgkmcnt(4)\n"
    "v_mfma_f32_16x16x32_bf16 v[124:127], a[242:245], v[20:23], v[124:127]\n"
    "ds_read_b64_tr_b16 a[212:213], v29 offset:23552\n"
    "v_mfma_f32_16x16x32_bf16 v[124:127], a[246:249], v[224:227], v[124:127]\n"
    "ds_read_b64_tr_b16 a[214:215], v29 offset:14336\n"
    "v_mfma_f32_16x16x32_bf16 v[140:143], a[230:233], v[46:49], v[140:143]\n"
    "ds_read_b64_tr_b16 a[216:217], v29 offset:24576\n"
    "v_mfma_f32_16x16x32_bf16 v[140:143], a[234:237], v[76:79], v[140:143]\n"
    "ds_read_b64_tr_b16 a[218:219], v29 offset:15360\n"
    "v_mfma_f32_16x16x32_bf16 v[140:143], a[238:241], v[10:13], v[140:143]\n"
    "ds_read_b64_tr_b16 a[220:221], v29 offset:25600\n"
    "v_mfma_f32_16x16x32_bf16 v[140:143], a[242:245], v[192:195], v[140:143]\n"
    "buffer_store_dwordx4 v[58:61], v28, s[28:31], s83 offen offset:1024 sc1\n"
    "ds_read_b64_tr_b16 a[222:223], v29 offset:16384\n"
    "v_mfma_f32_16x16x32_bf16 v[140:143], a[246:249], v[228:231], v[140:143]\n"
    "ds_read_b64_tr_b16 a[224:225], v29 offset:26624\n"
    "v_mfma_f32_16x16x32_bf16 v[128:131], a[250:253], v[34:37], v[128:131]\n"
    "ds_read_b64_tr_b16 a[226:227], v26 offset:12288\n"
    "v_mfma_f32_16x16x32_bf16 v[128:131], a[190:193], v[50:53], v[128:131]\n"
    "ds_read_b64_tr_b16 a[228:229], v26 offset:22528\n"
    "v_mfma_f32_16x16x32_bf16 v[128:131], a[194:197], v[80:83], v[128:131]\n"
    "ds_read_b64_tr_b16 a[230:231], v26 offset:13312\n"
    "v_mfma_f32_16x16x32_bf16 v[128:131], a[198:201], v[20:23], v[128:131]\n"
    "ds_read_b64_tr_b16 a[232:233], v26 offset:23552\n"
    "s_waitcnt lgkmcnt(6)\n"
    "v_mfma_f32_16x16x32_bf16 v[128:131], a[202:205], v[224:227], v[128:131]\n"
    "ds_read_b64_tr_b16 a[234:235], v26 offset:14336\n"
    "v_mfma_f32_16x16x32_bf16 v[144:147], a[250:253], v[46:49], v[144:147]\n"
    "ds_read_b64_tr_b16 a[236:237], v26 offset:24576\n"
    "s_waitcnt vmcnt(4) lgkmcnt(7)\n"
    "v_cmp_eq_u32 vcc, 0, v43\n"
    "v_mfma_f32_16x16x32_bf16 v[144:147], a[190:193], v[76:79], v[144:147]\n"
    "ds_read_b64_tr_b16 a[238:239], v26 offset:15360\n"
    "v_mfma_f32_16x16x32_bf16 v[144:147], a[194:197], v[10:13], v[144:147]\n"
    "ds_read_b64_tr_b16 a[240:241], v26 offset:25600\n"
    "v_mfma_f32_16x16x32_bf16 v[144:147], a[198:201], v[192:195], v[144:147]\n"
    "ds_read_b64_tr_b16 a[242:243], v26 offset:16384\n"
    "s_waitcnt vmcnt(3) lgkmcnt(7)\n"
    "s_cbranch_vccnz L_m6_%=\n"
    "L_m6_ret_%=:\n"
    "v_mfma_f32_16x16x32_bf16 v[144:147], a[202:205], v[228:231], v[144:147]\n"
    "s_barrier\n"
    "ds_read_b64_tr_b16 a[244:245], v26 offset:26624\n"
    "v_mfma_f32_16x16x32_bf16 v[124:127], a[206:209], v[196:199], v[124:127]\n"
    "ds_read_b64_tr_b16 a[246:247], v42 offset:0\n"
    "s_add_u32 s81, s53, 0\n"
    "v_mfma_f32_16x16x32_bf16 v[124:127], a[210:213], v[204:207], v[124:127]\n"
    "ds_read_b64_tr_b16 a[248:249], v42 offset:10240\n"
    "buffer_load_dwordx4 a[90:93], v28, s[28:31], s81 offen offset:0 sc1\n"
    "v_mfma_f32_16x16x32_bf16 v[124:127], a[214:217], v[252:255], v[124:127]\n"
    "ds_read_b64_tr_b16 a[250:251], v42 offset:1024\n"
    "s_add_u32 s82, s53, 40960\n"
    "v_mfma_f32_16x16x32_bf16 v[124:127], a[218:221], v[6:9], v[124:127]\n"
    "ds_read_b64_tr_b16 a[252:253], v42 offset:11264\n"
    "v_mfma_f32_16x16x32_bf16 v[124:127], a[222:225], v[160:163], v[124:127]\n"
    "ds_read_b64_tr_b16 a[190:191], v42 offset:2048\n"
    "v_mfma_f32_16x16x32_bf16 v[140:143], a[206:209], v[200:203], v[140:143]\n"
    "ds_read_b64_tr_b16 a[192:193], v42 offset:12288\n"
    "v_mfma_f32_16x16x32_bf16 v[140:143], a[210:213], v[208:211], v[140:143]\n"
    "ds_read_b64_tr_b16 a[194:195], v42 offset:3072\n"
    "v_mfma_f32_16x16x32_bf16 v[140:143], a[214:217], v[2:5], v[140:143]\n"
    "ds_read_b64_tr_b16 a[196:197], v42 offset:13312\n"
    "v_mfma_f32_16x16x32_bf16 v[140:143], a[218:221], v[38:41], v[140:143]\n"
    "ds_read_b64_tr_b16 a[198:199], v42 offset:4096\n"
    "v_mfma_f32_16x16x32_bf16 v[140:143], a[222:225], v[66:69], v[140:143]\n"
    "ds_read_b64_tr_b16 a[200:201], v42 offset:14336\n"
    "buffer_load_dwordx4 a[110:113], v28, s[28:31], s81 offen offset:1024 sc1\n"
    "v_mfma_f32_16x16x32_bf16 v[128:131], a[226:229], v[196:199], v[128:131]\n"
    "ds_read_b64_tr_b16 a[202:203], v87 offset:0\n"
    "s_waitcnt lgkmcnt(7)\n"
    "v_mfma_f32_16x16x32_bf16 v[128:131], a[230:233], v[204:207], v[128:131]\n"
    "ds_read_b64_tr_b16 a[204:205], v87 offset:10240\n"
    "s_add_u32 s81, s53, 8192\n"
    "v_mfma_f32_16x16x32_bf16 v[128:131], a[234:237], v[252:255], v[128:131]\n"
    "ds_read_b64_tr_b16 a[206:207], v87 offset:1024\n"
    "v_mfma_f32_16x16x32_bf16 v[128:131], a[238:241], v[6:9], v[128:131]\n"
    "ds_read_b64_tr_b16 a[208:209], v87 offset:11264\n"
    "v_mfma_f32_16x16x32_bf16 v[128:131], a[242:245], v[160:163], v[128:131]\n"
    "ds_read_b64_tr_b16 a[210:211], v87 offset:2048\n"
    "v_mfma_f32_16x16x32_bf16 v[144:147], a[226:229], v[200:203], v[144:147]\n"
    "ds_read_b64_tr_b16 a[212:213], v87 offset:12288\n"
    "v_mfma_f32_16x16x32_bf16 v[144:147], a[230:233], v[208:211], v[144:147]\n"
    "ds_read_b64_tr_b16 a[214:215], v87 offset:3072\n"
    "v_mfma_f32_16x16x32_bf16 v[144:147], a[234:237], v[2:5], v[144:147]\n"
    "ds_read_b64_tr_b16 a[216:217], v87 offset:13312\n"
    "v_mfma_f32_16x16x32_bf16 v[144:147], a[238:241], v[38:41], v[144:147]\n"
    "ds_read_b64_tr_b16 a[218:219], v87 offset:4096\n"
    "v_mfma_f32_16x16x32_bf16 v[144:147], a[242:245], v[66:69], v[144:147]\n"
    "ds_read_b64_tr_b16 a[220:221], v87 offset:14336\n"
    "v_mfma_f32_16x16x32_bf16 v[124:127], a[246:249], a[2:5], v[124:127]\n"
    "ds_read_b64_tr_b16 a[222:223], v42 offset:20480\n"
    "v_mfma_f32_16x16x32_bf16 v[124:127], a[250:253], a[132:135], v[124:127]\n"
    "ds_read_b64_tr_b16 a[224:225], v42 offset:30720\n"
    "s_waitcnt lgkmcnt(7)\n"
    "v_mfma_f32_16x16x32_bf16 v[124:127], a[190:193], a[140:143], v[124:127]\n"
    "ds_read_b64_tr_b16 a[226:227], v42 offset:21504\n"
    "buffer_load_dwordx4 a[94:97], v28, s[28:31], s81 offen offset:0 sc1\n"
    "v_mfma_f32_16x16x32_bf16 v[124:127], a[194:197], a[148:151], v[124:127]\n"
    "ds_read_b64_tr_b16 a[228:229], v42 offset:31744\n"
    "v_mfma_f32_16x16x32_bf16 v[124:127], a[198:201], v[240:243], v[124:127]\n"
    "ds_read_b64_tr_b16 a[230:231], v42 offset:22528\n"
    "v_mfma_f32_16x16x32_bf16 v[140:143], a[246:249], a[20:23], v[140:143]\n"
    "ds_read_b64_tr_b16 a[232:233], v42 offset:32768\n"
    "v_mfma_f32_16x16x32_bf16 v[140:143], a[250:253], a[136:139], v[140:143]\n"
    "ds_read_b64_tr_b16 a[234:235], v42 offset:23552\n"
    "v_mfma_f32_16x16x32_bf16 v[140:143], a[190:193], a[144:147], v[140:143]\n"
    "ds_read_b64_tr_b16 a[236:237], v42 offset:33792\n"
    "v_mfma_f32_16x16x32_bf16 v[140:143], a[194:197], a[152:155], v[140:143]\n"
    "ds_read_b64_tr_b16 a[238:239], v42 offset:24576\n"
    "v_mfma_f32_16x16x32_bf16 v[140:143], a[198:201], v[244:247], v[140:143]\n"
    "ds_read_b64_tr_b16 a[240:241], v42 offset:34816\n"
    "v_mfma_f32_16x16x32_bf16 v[128:131], a[202:205], a[2:5], v[128:131]\n"
    "ds_read_b64_tr_b16 a[242:243], v87 offset:20480\n"
    "v_mfma_f32_16x16x32_bf16 v[128:131], a[206:209], a[132:135], v[128:131]\n"
    "ds_read_b64_tr_b16 a[244:245], v87 offset:30720\n"
    "s_waitcnt lgkmcnt(7)\n"
    "v_mfma_f32_16x16x32_bf16 v[128:131], a[210:213], a[140:143], v[128:131]\n"
    "ds_read_b64_tr_b16 a[246:247], v87 offset:21504\n"
    "v_mfma_f32_16x16x32_bf16 v[128:131], a[214:217], a[148:151], v[128:131]\n"
    "ds_read_b64_tr_b16 a[248:249], v87 offset:31744\n"
    "buffer_load_dwordx4 a[98:101], v28, s[28:31], s81 offen offset:1024 sc1\n"
    "v_mfma_f32_16x16x32_bf16 v[128:131], a[218:221], v[240:243], v[128:131]\n"
    "ds_read_b64_tr_b16 a[250:251], v87 offset:22528\n"
    "v_mfma_f32_16x16x32_bf16 v[144:147], a[202:205], a[20:23], v[144:147]\n"
    "ds_read_b64_tr_b16 a[252:253], v87 offset:32768\n"
    "v_mfma_f32_16x16x32_bf16 v[144:147], a[206:209], a[136:139], v[144:147]\n"
    "ds_read_b64_tr_b16 a[190:191], v87 offset:23552\n"
    "v_mfma_f32_16x16x32_bf16 v[144:147], a[210:213], a[144:147], v[144:147]\n"
    "ds_read_b64_tr_b16 a[192:193], v87 offset:33792\n"
    "v_mfma_f32_16x16x32_bf16 v[144:147], a[214:217], a[152:155], v[144:147]\n"
    "ds_read_b64_tr_b16 a[194:195], v87 offset:24576\n"
    "v_mfma_f32_16x16x32_bf16 v[144:147], a[218:221], v[244:247], v[144:147]\n"
    "ds_read_b64_tr_b16 a[196:197], v87 offset:34816\n"
    "v_mfma_f32_16x16x32_bf16 v[124:127], a[222:225], a[156:159], v[124:127]\n"
    "ds_read_b64_tr_b16 a[198:199], v27 offset:57344\n"
    "v_mfma_f32_16x16x32_bf16 v[124:127], a[226:229], a[164:167], v[124:127]\n"
    "ds_read_b64_tr_b16 a[200:201], v63 offset:2048\n"
    "s_waitcnt lgkmcnt(7)\n"
    "v_mfma_f32_16x16x32_bf16 v[124:127], a[230:233], a[172:175], v[124:127]\n"
    "ds_read_b64_tr_b16 a[202:203], v27 offset:58368\n"
    "buffer_load_dword v62, v55, s[28:31], 0 offen offset:768 sc1\n"
    "v_mfma_f32_16x16x32_bf16 v[124:127], a[234:237], a[180:183], v[124:127]\n"
    "ds_read_b64_tr_b16 a[204:205], v63 offset:3072\n"
    "v_mfma_f32_16x16x32_bf16 v[124:127], a[238:241], a[34:37], v[124:127]\n"
    "ds_read_b64_tr_b16 a[206:207], v27 offset:59392\n"
    "v_mfma_f32_16x16x32_bf16 v[140:143], a[222:225], a[160:163], v[140:143]\n"
    "ds_read_b64_tr_b16 a[208:209], v63 offset:4096\n"
    "v_mfma_f32_16x16x32_bf16 v[140:143], a[226:229], a[168:171], v[140:143]\n"
    "ds_read_b64_tr_b16 a[210:211], v27 offset:60416\n"
    "v_mfma_f32_16x16x32_bf16 v[140:143], a[230:233], a[176:179], v[140:143]\n"
    "ds_read_b64_tr_b16 a[212:213], v63 offset:5120\n"
    "v_mfma_f32_16x16x32_bf16 v[140:143], a[234:237], a[184:187], v[140:143]\n"
    "ds_read_b64_tr_b16 a[214:215], v27 offset:61440\n"
    "v_cvt_pk_bf16_f32 v176, v124, v125\n"
    "v_mfma_f32_16x16x32_bf16 v[140:143], a[238:241], a[70:73], v[140:143]\n"
    "ds_read_b64_tr_b16 a[216:217], v63 offset:6144\n"
    "v_cvt_pk_bf16_f32 v177, v126, v127\n"
    "v_mfma_f32_16x16x32_bf16 v[128:131], a[242:245], a[156:159], v[128:131]\n"
    "ds_read_b64_tr_b16 a[218:219], v64 offset:57344\n"
    "v_mfma_f32_16x16x32_bf16 v[128:131], a[246:249], a[164:167], v[128:131]\n"
    "ds_read_b64_tr_b16 a[220:221], v65 offset:2048\n"
    "s_waitcnt lgkmcnt(7)\n"
    "v_mfma_f32_16x16x32_bf16 v[128:131], a[250:253], a[172:175], v[128:131]\n"
    "ds_read_b64_tr_b16 a[222:223], v64 offset:58368\n"
    "v_cvt_pk_bf16_f32 v180, v140, v141\n"
    "v_mfma_f32_16x16x32_bf16 v[128:131], a[190:193], a[180:183], v[128:131]\n"
    "ds_read_b64_tr_b16 a[224:225], v65 offset:3072\n"
    "v_cvt_pk_bf16_f32 v181, v142, v143\n"
    "v_mfma_f32_16x16x32_bf16 v[128:131], a[194:197], a[34:37], v[128:131]\n"
    "ds_read_b64_tr_b16 a[226:227], v64 offset:59392\n"
    "v_mfma_f32_16x16x32_bf16 v[144:147], a[242:245], a[160:163], v[144:147]\n"
    "ds_read_b64_tr_b16 a[228:229], v65 offset:4096\n"
    "v_mfma_f32_16x16x32_bf16 v[144:147], a[246:249], a[168:171], v[144:147]\n"
    "ds_read_b64_tr_b16 a[230:231], v64 offset:60416\n"
    "v_mfma_f32_16x16x32_bf16 v[144:147], a[250:253], a[176:179], v[144:147]\n"
    "ds_read_b64_tr_b16 a[232:233], v65 offset:5120\n"
    "v_mfma_f32_16x16x32_bf16 v[144:147], a[190:193], a[184:187], v[144:147]\n"
    "ds_read_b64_tr_b16 a[234:235], v64 offset:61440\n"
    "v_cvt_pk_bf16_f32 v178, v128, v129\n"
    "v_mfma_f32_16x16x32_bf16 v[144:147], a[194:197], a[70:73], v[144:147]\n"
    "ds_read_b64_tr_b16 a[236:237], v65 offset:6144\n"
    "v_cvt_pk_bf16_f32 v179, v130, v131\n"
    "v_mfma_f32_16x16x32_bf16 v[132:135], a[198:201], v[34:37], v[132:135]\n"
    "ds_read_b64_tr_b16 a[238:239], v63 offset:12288\n"
    "v_mfma_f32_16x16x32_bf16 v[132:135], a[202:205], v[50:53], v[132:135]\n"
    "ds_read_b64_tr_b16 a[240:241], v63 offset:22528\n"
    "s_waitcnt lgkmcnt(7)\n"
    "v_mfma_f32_16x16x32_bf16 v[132:135], a[206:209], v[80:83], v[132:135]\n"
    "ds_read_b64_tr_b16 a[242:243], v63 offset:13312\n"
    "v_cvt_pk_bf16_f32 v182, v144, v145\n"
    "v_mfma_f32_16x16x32_bf16 v[132:135], a[210:213], v[20:23], v[132:135]\n"
    "ds_read_b64_tr_b16 a[244:245], v63 offset:23552\n"
    "v_cvt_pk_bf16_f32 v183, v146, v147\n"
    "v_mfma_f32_16x16x32_bf16 v[132:135], a[214:217], v[224:227], v[132:135]\n"
    "buffer_store_dwordx4 v[176:179], v28, s[28:31], s51 offen offset:0 sc1\n"
    ".p2align 12\n"
    "L_pg11_%=:\n"
    "s_cbranch_execz L_tramp_%=\n"
    "ds_read_b64_tr_b16 a[246:247], v63 offset:14336\n"
    "v_mfma_f32_16x16x32_bf16 v[148:151], a[198:201], v[46:49], v[148:151]\n"
    "ds_read_b64_tr_b16 a[248:249], v63 offset:24576\n"
    "v_mfma_f32_16x16x32_bf16 v[148:151], a[202:205], v[76:79], v[148:151]\n"
    "ds_read_b64_tr_b16 a[250:251], v63 offset:15360\n"
    "v_mfma_f32_16x16x32_bf16 v[148:151], a[206:209], v[10:13], v[148:151]\n"
    "ds_read_b64_tr_b16 a[252:253], v63 offset:25600\n"
    "v_mfma_f32_16x16x32_bf16 v[148:151], a[210:213], v[192:195], v[148:151]\n"
    "ds_read_b64_tr_b16 a[190:191], v63 offset:16384\n"
    "v_mfma_f32_16x16x32_bf16 v[148:151], a[214:217], v[228:231], v[148:151]\n"
    "ds_read_b64_tr_b16 a[192:193], v63 offset:26624\n"
    "v_mfma_f32_16x16x32_bf16 v[136:139], a[218:221], v[34:37], v[136:139]\n"
    "ds_read_b64_tr_b16 a[194:195], v65 offset:12288\n"
    "v_mfma_f32_16x16x32_bf16 v[136:139], a[222:225], v[50:53], v[136:139]\n"
    "ds_read_b64_tr_b16 a[196:197], v65 offset:22528\n"
    "s_waitcnt lgkmcnt(7)\n"
    "v_mfma_f32_16x16x32_bf16 v[136:139], a[226:229], v[80:83], v[136:139]\n"
    "buffer_store_dwordx4 v[180:183], v28, s[28:31], s51 offen offset:1024 sc1\n"
    "ds_read_b64_tr_b16 a[198:199], v65 offset:13312\n"
    "v_mfma_f32_16x16x32_bf16 v[136:139], a[230:233], v[20:23], v[136:139]\n"
    "ds_read_b64_tr_b16 a[200:201], v65 offset:23552\n"
    "v_mfma_f32_16x16x32_bf16 v[136:139], a[234:237], v[224:227], v[136:139]\n"
    "ds_read_b64_tr_b16 a[202:203], v65 offset:14336\n"
    "v_mfma_f32_16x16x32_bf16 v[152:155], a[218:221], v[46:49], v[152:155]\n"
    "ds_read_b64_tr_b16 a[204:205], v65 offset:24576\n"
    "v_mfma_f32_16x16x32_bf16 v[152:155], a[222:225], v[76:79], v[152:155]\n"
    "ds_read_b64_tr_b16 a[206:207], v65 offset:15360\n"
    "v_mfma_f32_16x16x32_bf16 v[152:155], a[226:229], v[10:13], v[152:155]\n"
    "ds_read_b64_tr_b16 a[208:209], v65 offset:25600\n"
    "v_mfma_f32_16x16x32_bf16 v[152:155], a[230:233], v[192:195], v[152:155]\n"
    "ds_read_b64_tr_b16 a[210:211], v65 offset:16384\n"
    "v_mfma_f32_16x16x32_bf16 v[152:155], a[234:237], v[228:231], v[152:155]\n"
    "ds_read_b64_tr_b16 a[212:213], v65 offset:26624\n"
    "v_mfma_f32_16x16x32_bf16 v[132:135], a[238:241], v[196:199], v[132:135]\n"
    "ds_read_b64_tr_b16 a[214:215], v89 offset:0\n"
    "v_mfma_f32_16x16x32_bf16 v[132:135], a[242:245], v[204:207], v[132:135]\n"
    "ds_read_b64_tr_b16 a[216:217], v89 offset:10240\n"
    "s_waitcnt lgkmcnt(7)\n"
    "v_mfma_f32_16x16x32_bf16 v[132:135], a[246:249], v[252:255], v[132:135]\n"
    "ds_read_b64_tr_b16 a[218:219], v89 offset:1024\n"
    "v_mfma_f32_16x16x32_bf16 v[132:135], a[250:253], v[6:9], v[132:135]\n"
    "ds_read_b64_tr_b16 a[220:221], v89 offset:11264\n"
    "v_mfma_f32_16x16x32_bf16 v[132:135], a[190:193], v[160:163], v[132:135]\n"
    "ds_read_b64_tr_b16 a[222:223], v89 offset:2048\n"
    "v_mfma_f32_16x16x32_bf16 v[148:151], a[238:241], v[200:203], v[148:151]\n"
    "ds_read_b64_tr_b16 a[224:225], v89 offset:12288\n"
    "v_mfma_f32_16x16x32_bf16 v[148:151], a[242:245], v[208:211], v[148:151]\n"
    "ds_read_b64_tr_b16 a[226:227], v89 offset:3072\n"
    "v_mfma_f32_16x16x32_bf16 v[148:151], a[246:249], v[2:5], v[148:151]\n"
    "ds_read_b64_tr_b16 a[228:229], v89 offset:13312\n"
    "v_mfma_f32_16x16x32_bf16 v[148:151], a[250:253], v[38:41], v[148:151]\n"
    "ds_read_b64_tr_b16 a[230:231], v89 offset:4096\n"
    "v_mfma_f32_16x16x32_bf16 v[148:151], a[190:193], v[66:69], v[148:151]\n"
    "ds_read_b64_tr_b16 a[232:233], v89 offset:14336\n"
    "v_mfma_f32_16x16x32_bf16 v[136:139], a[194:197], v[196:199], v[136:139]\n"
    "ds_read_b64_tr_b16 a[234:235], v90 offset:0\n"
    "v_mfma_f32_16x16x32_bf16 v[136:139], a[198:201], v[204:207], v[136:139]\n"
    "ds_read_b64_tr_b16 a[236:237], v90 offset:10240\n"
    "s_waitcnt lgkmcnt(7)\n"
    "v_mfma_f32_16x16x32_bf16 v[136:139], a[202:205], v[252:255], v[136:139]\n"
    "ds_read_b64_tr_b16 a[238:239], v90 offset:1024\n"
    "v_mfma_f32_16x16x32_bf16 v[136:139], a[206:209], v[6:9], v[136:139]\n"
    "ds_read_b64_tr_b16 a[240:241], v90 offset:11264\n"
    "v_mfma_f32_16x16x32_bf16 v[136:139], a[210:213], v[160:163], v[136:139]\n"
    "ds_read_b64_tr_b16 a[242:243], v90 offset:2048\n"
    "v_mfma_f32_16x16x32_bf16 v[152:155], a[194:197], v[200:203], v[152:155]\n"
    "ds_read_b64_tr_b16 a[244:245], v90 offset:12288\n"
    "v_mfma_f32_16x16x32_bf16 v[152:155], a[198:201], v[208:211], v[152:155]\n"
    "ds_read_b64_tr_b16 a[246:247], v90 offset:3072\n"
    "v_mfma_f32_16x16x32_bf16 v[152:155], a[202:205], v[2:5], v[152:155]\n"
    "ds_read_b64_tr_b16 a[248:249], v90 offset:13312\n"
    "v_mfma_f32_16x16x32_bf16 v[152:155], a[206:209], v[38:41], v[152:155]\n"
    "ds_read_b64_tr_b16 a[250:251], v90 offset:4096\n"
    "v_mfma_f32_16x16x32_bf16 v[152:155], a[210:213], v[66:69], v[152:155]\n"
    "ds_read_b64_tr_b16 a[252:253], v90 offset:14336\n"
    "v_mfma_f32_16x16x32_bf16 v[132:135], a[214:217], a[2:5], v[132:135]\n"
    "ds_read_b64_tr_b16 a[190:191], v89 offset:20480\n"
    "v_mfma_f32_16x16x32_bf16 v[132:135], a[218:221], a[132:135], v[132:135]\n"
    "ds_read_b64_tr_b16 a[192:193], v89 offset:30720\n"
    "s_waitcnt lgkmcnt(7)\n"
    "v_mfma_f32_16x16x32_bf16 v[132:135], a[222:225], a[140:143], v[132:135]\n"
    "ds_read_b64_tr_b16 a[194:195], v89 offset:21504\n"
    "s_waitcnt vmcnt(2) lgkmcnt(7)\n"
    "v_cmp_eq_u32 vcc, 0, v62\n"
    "v_mfma_f32_16x16x32_bf16 v[132:135], a[226:229], a[148:151], v[132:135]\n"
    "ds_read_b64_tr_b16 a[196:197], v89 offset:31744\n"
    "v_mfma_f32_16x16x32_bf16 v[132:135], a[230:233], v[240:243], v[132:135]\n"
    "ds_read_b64_tr_b16 a[198:199], v89 offset:22528\n"
    "v_mfma_f32_16x16x32_bf16 v[148:151], a[214:217], a[20:23], v[148:151]\n"
    "ds_read_b64_tr_b16 a[200:201], v89 offset:32768\n"
    "s_cbranch_vccnz L_m7_%=\n"
    "L_m7_ret_%=:\n"
    "v_mfma_f32_16x16x32_bf16 v[148:151], a[218:221], a[136:139], v[148:151]\n"
    "ds_read_b64_tr_b16 a[202:203], v89 offset:23552\n"
    "s_add_u32 s81, s53, 16384\n"
    "v_mfma_f32_16x16x32_bf16 v[148:151], a[222:225], a[144:147], v[148:151]\n"
    "ds_read_b64_tr_b16 a[204:205], v89 offset:33792\n"
    "buffer_load_dwordx4 a[102:105], v28, s[28:31], s81 offen offset:0 sc1\n"
    "v_mfma_f32_16x16x32_bf16 v[148:151], a[226:229], a[152:155], v[148:151]\n"
    "ds_read_b64_tr_b16 a[206:207], v89 offset:24576\n"
    "v_mfma_f32_16x16x32_bf16 v[148:151], a[230:233], v[244:247], v[148:151]\n"
    "ds_read_b64_tr_b16 a[208:209], v89 offset:34816\n"
    "v_mfma_f32_16x16x32_bf16 v[136:139], a[234:237], a[2:5], v[136:139]\n"
    "ds_read_b64_tr_b16 a[210:211], v90 offset:20480\n"
    "buffer_load_dwordx4 a[106:109], v28, s[28:31], s81 offen offset:1024 sc1\n"
    "v_mfma_f32_16x16x32_bf16 v[136:139], a[238:241], a[132:135], v[136:139]\n"
    "ds_read_b64_tr_b16 a[212:213], v90 offset:30720\n"
    "s_add_u32 s81, s53, 24576\n"
    "v_mfma_f32_16x16x32_bf16 v[136:139], a[242:245], a[140:143], v[136:139]\n"
    "ds_read_b64_tr_b16 a[214:215], v90 offset:21504\n"
    "s_waitcnt lgkmcnt(7)\n"
    "v_mfma_f32_16x16x32_bf16 v[136:139], a[246:249], a[148:151], v[136:139]\n"
    "ds_read_b64_tr_b16 a[216:217], v90 offset:31744\n"
    "v_mfma_f32_16x16x32_bf16 v[136:139], a[250:253], v[240:243], v[136:139]\n"
    "ds_read_b64_tr_b16 a[218:219], v90 offset:22528\n"
    "buffer_load_dwordx4 a[114:117], v28, s[28:31], s81 offen offset:0 sc1\n"
    "v_mfma_f32_16x16x32_bf16 v[152:155], a[234:237], a[20:23], v[152:155]\n"
    "ds_read_b64_tr_b16 a[220:221], v90 offset:32768\n"
    "v_mfma_f32_16x16x32_bf16 v[152:155], a[238:241], a[136:139], v[152:155]\n"
    "ds_read_b64_tr_b16 a[222:223], v90 offset:23552\n"
    "v_mfma_f32_16x16x32_bf16 v[152:155], a[242:245], a[144:147], v[152:155]\n"
    "ds_read_b64_tr_b16 a[224:225], v90 offset:33792\n"
    "v_mfma_f32_16x16x32_bf16 v[152:155], a[246:249], a[152:155], v[152:155]\n"
    "ds_read_b64_tr_b16 a[226:227], v90 offset:24576\n"
    "buffer_load_dwordx4 a[30:33], v28, s[28:31], s82 offen offset:0 sc1\n"
    "v_mfma_f32_16x16x32_bf16 v[152:155], a[250:253], v[244:247], v[152:155]\n"
    "ds_read_b64_tr_b16 a[228:229], v90 offset:34816\n"
    "v_mfma_f32_16x16x32_bf16 v[132:135], a[190:193], a[156:159], v[132:135]\n"
    "v_mfma_f32_16x16x32_bf16 v[132:135], a[194:197], a[164:167], v[132:135]\n"
    "buffer_load_dwordx4 a[118:121], v28, s[28:31], s81 offen offset:1024 sc1\n"
    "s_add_u32 s81, s53, 32768\n"
    "v_mfma_f32_16x16x32_bf16 v[132:135], a[198:201], a[172:175], v[132:135]\n"
    "s_waitcnt lgkmcnt(4)\n"
    "v_mfma_f32_16x16x32_bf16 v[132:135], a[202:205], a[180:183], v[132:135]\n"
    "v_mfma_f32_16x16x32_bf16 v[132:135], a[206:209], a[34:37], v[132:135]\n"
    "buffer_load_dwordx4 a[122:125], v28, s[28:31], s81 offen offset:0 sc1\n"
    "v_mfma_f32_16x16x32_bf16 v[148:151], a[190:193], a[160:163], v[148:151]\n"
    "v_mfma_f32_16x16x32_bf16 v[148:151], a[194:197], a[168:171], v[148:151]\n"
    "v_mfma_f32_16x16x32_bf16 v[148:151], a[198:201], a[176:179], v[148:151]\n"
    "buffer_load_dwordx4 a[38:41], v28, s[28:31], s82 offen offset:1024 sc1\n"
    "v_mfma_f32_16x16x32_bf16 v[148:151], a[202:205], a[184:187], v[148:151]\n"
    "v_mfma_f32_16x16x32_bf16 v[148:151], a[206:209], a[70:73], v[148:151]\n"
    "v_mfma_f32_16x16x32_bf16 v[136:139], a[210:213], a[156:159], v[136:139]\n"
    "buffer_load_dwordx4 a[126:129], v28, s[28:31], s81 offen offset:1024 sc1\n"
    "v_mfma_f32_16x16x32_bf16 v[136:139], a[214:217], a[164:167], v[136:139]\n"
    "v_mfma_f32_16x16x32_bf16 v[136:139], a[218:221], a[172:175], v[136:139]\n"
    "s_waitcnt lgkmcnt(0)\n"
    "v_mfma_f32_16x16x32_bf16 v[136:139], a[222:225], a[180:183], v[136:139]\n"
    "v_mfma_f32_16x16x32_bf16 v[136:139], a[226:229], a[34:37], v[136:139]\n"
    "v_mfma_f32_16x16x32_bf16 v[152:155], a[210:213], a[160:163], v[152:155]\n"
    "v_mfma_f32_16x16x32_bf16 v[152:155], a[214:217], a[168:171], v[152:155]\n"
    "v_mfma_f32_16x16x32_bf16 v[152:155], a[218:221], a[176:179], v[152:155]\n"
    "v_mfma_f32_16x16x32_bf16 v[152:155], a[222:225], a[184:187], v[152:155]\n"
    "v_mfma_f32_16x16x32_bf16 v[152:155], a[226:229], a[70:73], v[152:155]\n"
    "s_add_u32 s82, s53, 49152\n"
    "buffer_load_dwordx4 a[24:27], v28, s[28:31], s82 offen offset:0 sc1\n"
    "buffer_load_dwordx4 a[6:9], v28, s[28:31], s82 offen offset:1024 sc1\n"
    "v_mfma_f32_16x16x32_bf16 v[220:223], a[16:19], a[90:93], v[132:135]\n"
    "v_mfma_f32_16x16x32_bf16 v[220:223], a[16:19], a[94:97], v[220:223]\n"
    "v_mfma_f32_16x16x32_bf16 v[232:235], a[14:17], a[90:93], v[136:139]\n"
    "v_mfma_f32_16x16x32_bf16 v[232:235], a[14:17], a[94:97], v[232:235]\n"
    "v_mfma_f32_16x16x32_bf16 v[236:239], a[16:19], a[110:113], v[148:151]\n"
    "v_mfma_f32_16x16x32_bf16 v[236:239], a[16:19], a[98:101], v[236:239]\n"
    "v_mfma_f32_16x16x32_bf16 v[248:251], a[14:17], a[110:113], v[152:155]\n"
    "v_mfma_f32_16x16x32_bf16 v[248:251], a[14:17], a[98:101], v[248:251]\n"
    "v_mfma_f32_16x16x32_bf16 v[50:53], a[16:19], a[74:77], 0\n"
    "v_mfma_f32_16x16x32_bf16 v[50:53], a[16:19], a[78:81], v[50:53]\n"
    "v_mfma_f32_16x16x32_bf16 v[50:53], a[16:19], a[82:85], v[50:53]\n"
    "v_mfma_f32_16x16x32_bf16 v[50:53], a[16:19], a[86:89], v[50:53]\n"
    "v_mfma_f32_16x16x32_bf16 v[76:79], a[14:17], a[74:77], 0\n"
    "v_mfma_f32_16x16x32_bf16 v[76:79], a[14:17], a[78:81], v[76:79]\n"
    "v_mfma_f32_16x16x32_bf16 v[76:79], a[14:17], a[82:85], v[76:79]\n"
    "v_mfma_f32_16x16x32_bf16 v[76:79], a[14:17], a[86:89], v[76:79]\n"
    "s_nop 3\n"
    "v_add_f32_dpp v50, v50, v50 row_ror:4 row_mask:0xf bank_mask:0xf\n"
    "v_add_f32_dpp v51, v51, v51 row_ror:4 row_mask:0xf bank_mask:0xf\n"
    "v_add_f32_dpp v52, v52, v52 row_ror:4 row_mask:0xf bank_mask:0xf\n"
    "v_add_f32_dpp v53, v53, v53 row_ror:4 row_mask:0xf bank_mask:0xf\n"
    "v_add_f32_dpp v76, v76, v76 row_ror:4 row_mask:0xf bank_mask:0xf\n"
    "v_add_f32_dpp v77, v77, v77 row_ror:4 row_mask:0xf bank_mask:0xf\n"
    "v_add_f32_dpp v78, v78, v78 row_ror:4 row_mask:0xf bank_mask:0xf\n"
    "v_add_f32_dpp v79, v79, v79 row_ror:4 row_mask:0xf bank_mask:0xf\n"
    "v_add_f32_dpp v50, v50, v50 row_ror:8 row_mask:0xf bank_mask:0xf\n"
    "v_add_f32_dpp v51, v51, v51 row_ror:8 row_mask:0xf bank_mask:0xf\n"
    "v_add_f32_dpp v52, v52, v52 row_ror:8 row_mask:0xf bank_mask:0xf\n"
    "v_add_f32_dpp v53, v53, v53 row_ror:8 row_mask:0xf bank_mask:0xf\n"
    "v_add_f32_dpp v76, v76, v76 row_ror:8 row_mask:0xf bank_mask:0xf\n"
    "v_add_f32_dpp v77, v77, v77 row_ror:8 row_mask:0xf bank_mask:0xf\n"
    "v_add_f32_dpp v78, v78, v78 row_ror:8 row_mask:0xf bank_mask:0xf\n"
    "v_add_f32_dpp v79, v79, v79 row_ror:8 row_mask:0xf bank_mask:0xf\n"
    "v_lshlrev_b32 v16, 16, v216\n"
    "v_and_b32 v17, 4294901760, v216\n"
    "v_fma_f32 v50, v50, 1.0, v16\n"
    "v_fma_f32 v51, v51, 1.0, v17\n"
    "v_lshlrev_b32 v16, 16, v217\n"
    "v_and_b32 v17, 4294901760, v217\n"
    "v_fma_f32 v52, v52, 1.0, v16\n"
    "v_fma_f32 v53, v53, 1.0, v17\n"
    "v_lshlrev_b32 v16, 16, v218\n"
    "v_and_b32 v17, 4294901760, v218\n"
    "v_fma_f32 v76, v76, 1.0, v16\n"
    "v_fma_f32 v77, v77, 1.0, v17\n"
    "v_lshlrev_b32 v16, 16, v219\n"
    "v_and_b32 v17, 4294901760, v219\n"
    "v_fma_f32 v78, v78, 1.0, v16\n"
    "v_fma_f32 v79, v79, 1.0, v17\n"
    "v_cvt_pk_bf16_f32 v216, v50, v51\n"
    "v_cvt_pk_bf16_f32 v217, v52, v53\n"
    "v_cvt_pk_bf16_f32 v218, v76, v77\n"
    "v_cvt_pk_bf16_f32 v219, v78, v79\n"
    "s_nop 0\n"
    "v_permlane16_swap_b32 v216, v218\n"
    "v_permlane16_swap_b32 v217, v219\n"
    "buffer_store_dwordx4 v[216:219], v45, s[24:27], 0 offen offset:0\n"
    "v_lshlrev_b32 v16, 16, v156\n"
    "v_and_b32 v17, 4294901760, v156\n"
    "v_fma_f32 v220, v220, v84, v16\n"
    "v_fma_f32 v221, v221, v84, v17\n"
    "v_lshlrev_b32 v16, 16, v157\n"
    "v_and_b32 v17, 4294901760, v157\n"
    "v_fma_f32 v222, v222, v84, v16\n"
    "v_fma_f32 v223, v223, v84, v17\n"
    "v_lshlrev_b32 v16, 16, v158\n"
    "v_and_b32 v17, 4294901760, v158\n"
    "v_fma_f32 v232, v232, v84, v16\n"
    "v_fma_f32 v233, v233, v84, v17\n"
    "v_lshlrev_b32 v16, 16, v159\n"
    "v_and_b32 v17, 4294901760, v159\n"
    "v_fma_f32 v234, v234, v84, v16\n"
    "v_fma_f32 v235, v235, v84, v17\n"
    "v_lshlrev_b32 v16, 16, v212\n"
    "v_and_b32 v17, 4294901760, v212\n"
    "v_fma_f32 v236, v236, v88, v16\n"
    "v_fma_f32 v237, v237, v88, v17\n"
    "v_lshlrev_b32 v16, 16, v213\n"
    "v_and_b32 v17, 4294901760, v213\n"
    "v_fma_f32 v238, v238, v88, v16\n"
    "v_fma_f32 v239, v239, v88, v17\n"
    "v_lshlrev_b32 v16, 16, v214\n"
    "v_and_b32 v17, 4294901760, v214\n"
    "v_fma_f32 v248, v248, v88, v16\n"
    "v_fma_f32 v249, v249, v88, v17\n"
    "v_lshlrev_b32 v16, 16, v215\n"
    "v_and_b32 v17, 4294901760, v215\n"
    "v_fma_f32 v250, v250, v88, v16\n"
    "v_fma_f32 v251, v251, v88, v17\n"
    "s_waitcnt vmcnt(10)\n"
    "v_mfma_f32_16x16x32_bf16 v[10:13], a[16:19], a[102:105], 0\n"
    "s_waitcnt vmcnt(8)\n"
    "v_mfma_f32_16x16x32_bf16 v[10:13], a[16:19], a[114:117], v[10:13]\n"
    "s_waitcnt vmcnt(5)\n"
    "v_mfma_f32_16x16x32_bf16 v[10:13], a[16:19], a[122:125], v[10:13]\n"
    "v_mfma_f32_16x16x32_bf16 v[80:83], a[14:17], a[102:105], 0\n"
    "v_mfma_f32_16x16x32_bf16 v[80:83], a[14:17], a[114:117], v[80:83]\n"
    "v_mfma_f32_16x16x32_bf16 v[80:83], a[14:17], a[122:125], v[80:83]\n"
    "v_mfma_f32_16x16x32_bf16 v[20:23], a[16:19], a[106:109], 0\n"
    "v_mfma_f32_16x16x32_bf16 v[20:23], a[16:19], a[118:121], v[20:23]\n"
    "s_waitcnt vmcnt(3)\n"
    "v_mfma_f32_16x16x32_bf16 v[20:23], a[16:19], a[126:129], v[20:23]\n"
    "v_mfma_f32_16x16x32_bf16 v[192:195], a[14:17], a[106:109], 0\n"
    "v_mfma_f32_16x16x32_bf16 v[192:195], a[14:17], a[118:121], v[192:195]\n"
    "v_mfma_f32_16x16x32_bf16 v[192:195], a[14:17], a[126:129], v[192:195]\n"
    "s_waitcnt vmcnt(1)\n"
    "s_nop 3\n"
    "L_rb_%=:\n"
    "v_mfma_f32_16x16x32_bf16 v[30:33], a[16:19], a[30:33], v[10:13]\n"
    "v_mfma_f32_16x16x32_bf16 v[30:33], a[16:19], a[24:27], v[30:33]\n"
    "v_mfma_f32_16x16x32_bf16 v[184:187], a[14:17], a[30:33], v[80:83]\n"
    "v_mfma_f32_16x16x32_bf16 v[184:187], a[14:17], a[24:27], v[184:187]\n"
    "v_mfma_f32_16x16x32_bf16 v[34:37], a[16:19], a[38:41], v[20:23]\n"
    "v_mfma_f32_16x16x32_bf16 v[34:37], a[16:19], a[6:9], v[34:37]\n"
    "v_mfma_f32_16x16x32_bf16 v[46:49], a[14:17], a[38:41], v[192:195]\n"
    "v_mfma_f32_16x16x32_bf16 v[46:49], a[14:17], a[6:9], v[46:49]\n"
    "s_nop 5\n"
    "v_cmp_u_f32 vcc, v30, v34\n"
    "s_cbranch_vccnz L_rb_slow_%=\n"
    "L_rb_ret_%=:\n"
    "v_fma_f32 v30, v30, v84, v220\n"
    "v_fma_f32 v31, v31, v84, v221\n"
    "v_fma_f32 v32, v32, v84, v222\n"
    "v_fma_f32 v33, v33, v84, v223\n"
    "v_fma_f32 v184, v184, v84, v232\n"
    "v_fma_f32 v185, v185, v84, v233\n"
    "v_fma_f32 v186, v186, v84, v234\n"
    "v_fma_f32 v187, v187, v84, v235\n"
    "v_cvt_pk_bf16_f32 v156, v30, v31\n"
    "v_cvt_pk_bf16_f32 v157, v32, v33\n"
    "v_cvt_pk_bf16_f32 v158, v184, v185\n"
    "v_cvt_pk_bf16_f32 v159, v186, v187\n"
    "s_nop 0\n"
    "v_permlane16_swap_b32 v156, v158\n"
    "v_permlane16_swap_b32 v157, v159\n"
    "buffer_store_dwordx4 v[156:159], v44, s[24:27], 0 offen offset:0\n"
    "v_fma_f32 v34, v34, v88, v236\n"
    "v_fma_f32 v35, v35, v88, v237\n"
    "v_fma_f32 v36, v36, v88, v238\n"
    "v_fma_f32 v37, v37, v88, v239\n"
    "v_fma_f32 v46, v46, v88, v248\n"
    "v_fma_f32 v47, v47, v88, v249\n"
    "v_fma_f32 v48, v48, v88, v250\n"
    "v_fma_f32 v49, v49, v88, v251\n"
    "v_cvt_pk_bf16_f32 v212, v34, v35\n"
    "v_cvt_pk_bf16_f32 v213, v36, v37\n"
    "v_cvt_pk_bf16_f32 v214, v46, v47\n"
    "v_cvt_pk_bf16_f32 v215, v48, v49\n"
    "s_nop 0\n"
    "v_permlane16_swap_b32 v212, v214\n"
    "v_permlane16_swap_b32 v213, v215\n"
    "buffer_store_dwordx4 v[212:215], v44, s[24:27], s2 offen offset:0\n"
    "s_endpgm\n"
    "L_ld0_%=:\n"
    "s_nop 7\n"
    "v_lshlrev_b32 v38, 1, v28\n"
    "s_lshl_b32 s68, s78, 11\n"
    "s_add_u32 s68, s68, 16384\n"
    "buffer_load_dwordx4 v[30:33], v38, s[28:31], s68 offen sc1\n"
    "buffer_load_dwordx4 v[34:37], v38, s[28:31], s68 offen offset:16 sc1\n"
    "buffer_load_dwordx4 v[38:41], v28, s[28:31], s91 offen sc1\n"
    "s_nop 7\n"
    "s_branch L_ld0_ret_%=\n"
    "L_ld1_%=:\n"
    "s_nop 7\n"
    "s_waitcnt vmcnt(22)\n"
    "s_mov_b64 s[68:69], vcc\n"
    "v_add_u32 v30, 1, v30\n"
    "v_cmp_eq_u32 vcc, v30, v38\n"
    "s_nop 1\n"
    "v_addc_co_u32 v38, vcc, 0, v30, vcc\n"
    "v_accvgpr_write_b32 a2, v38\n"
    "v_add_u32 v32, 1, v32\n"
    "v_cmp_eq_u32 vcc, v32, v39\n"
    "s_nop 1\n"
    "v_addc_co_u32 v39, vcc, 0, v32, vcc\n"
    "v_accvgpr_write_b32 a3, v39\n"
    "v_add_u32 v34, 1, v34\n"
    "v_cmp_eq_u32 vcc, v34, v40\n"
    "s_nop 1\n"
    "v_addc_co_u32 v40, vcc, 0, v34, vcc\n"
    "v_accvgpr_write_b32 a4, v40\n"
    "v_add_u32 v36, 1, v36\n"
    "v_cmp_eq_u32 vcc, v36, v41\n"
    "s_nop 1\n"
    "v_addc_co_u32 v41, vcc, 0, v36, vcc\n"
    "v_accvgpr_write_b32 a5, v41\n"
    "buffer_store_dwordx4 v[38:41], v28, s[28:31], s91 offen sc1\n"
    "s_nop 4\n"
    "s_mov_b64 vcc, s[68:69]\n"
    "s_nop 7\n"
    "s_branch L_ld1_ret_%=\n"
    "L_ld2_%=:\n"
    "s_nop 7\n"
    "v_lshlrev_b32 v38, 1, v28\n"
    "s_lshl_b32 s68, s78, 11\n"
    "s_add_u32 s68, s68, 16384\n"
    "buffer_load_dwordx4 v[30:33], v38, s[28:31], s68 offen sc1\n"
    "buffer_load_dwordx4 v[34:37], v38, s[28:31], s68 offen offset:16 sc1\n"
    "s_nop 7\n"
    "s_branch L_ld2_ret_%=\n"
    "L_ld3_%=:\n"
    "s_nop 7\n"
    "s_waitcnt vmcnt(8)\n"
    "L_ld3_loop_%=:\n"
    "v_accvgpr_read_b32 v38, a2\n"
    "v_xor_b32 v38, v38, v30\n"
    "v_accvgpr_read_b32 v39, a3\n"
    "v_xor_b32 v39, v39, v32\n"
    "v_accvgpr_read_b32 v40, a4\n"
    "v_xor_b32 v40, v40, v34\n"
    "v_accvgpr_read_b32 v41, a5\n"
    "v_xor_b32 v41, v41, v36\n"
    "v_or3_b32 v38, v38, v39, v40\n"
    "v_or_b32 v38, v38, v41\n"
    "v_cmp_ne_u32 vcc, 0, v38\n"
    "s_cbranch_vccz L_ld3_ok_%=\n"
    "s_add_u32 s0, s0, 1\n"
    "s_cmp_gt_u32 s0, 2000000\n"
    "s_cbranch_scc1 L_ld3_ok_%=\n"
    "s_sleep 1\n"
    "v_lshlrev_b32 v38, 1, v28\n"
    "s_lshl_b32 s68, s78, 11\n"
    "s_add_u32 s68, s68, 16384\n"
    "buffer_load_dwordx4 v[30:33], v38, s[28:31], s68 offen sc1\n"
    "buffer_load_dwordx4 v[34:37], v38, s[28:31], s68 offen offset:16 sc1\n"
    "s_waitcnt vmcnt(0)\n"
    "s_branch L_ld3_loop_%=\n"
    "L_ld3_ok_%=:\n"
    "v_mov_b32 v38, v31\n"
    "v_mov_b32 v39, v33\n"
    "v_mov_b32 v40, v35\n"
    "v_mov_b32 v41, v37\n"
    "s_nop 7\n"
    "s_branch L_ld3_ret_%=\n"
    "L_ld4_%=:\n"
    "s_nop 7\n"
    "s_add_u32 s68, s91, 4096\n"
    "buffer_store_dwordx4 v[38:41], v28, s[28:31], s68 offen sc1\n"
    "s_nop 7\n"
    "s_branch L_ld4_ret_%=\n"
    "L_rel_%=:\n"
    "s_sleep 4\n"
    "buffer_load_dword v26, v25, s[28:31], s91 offen sc1\n"
    "s_waitcnt vmcnt(0)\n"
    "buffer_store_dwordx2 v[26:27], v19, s[28:31], s97 offen sc1\n"
    "buffer_load_dword v43, v25, s[28:31], s82 offen offset:0 sc1\n"
    "s_add_u32 s0, s0, 1\n"
    "s_waitcnt vmcnt(0)\n"
    "v_cmp_ne_u32 vcc, v43, v27\n"
    "s_cmp_gt_u32 s0, 2000000\n"
    "s_cbranch_scc1 L_rel_ret_%=\n"
    "s_cbranch_vccnz L_rel_%=\n"
    "s_branch L_rel_ret_%=\n"
    "L_kv_%=:\n"
    "s_sleep 4\n"
    "buffer_load_dword v43, v55, s[28:31], 0 offen offset:0 sc1\n"
    "s_add_u32 s0, s0, 1\n"
    "s_waitcnt vmcnt(0)\n"
    "v_cmp_eq_u32 vcc, 0, v43\n"
    "s_cmp_gt_u32 s0, 2000000\n"
    "s_cbranch_scc1 L_kv_ret_%=\n"
    "s_cbranch_vccnz L_kv_%=\n"
    "s_branch L_kv_ret_%=\n"
    "L_h_%=:\n"
    "s_sleep 4\n"
    "buffer_load_dword v43, v25, s[28:31], s99 offen offset:128 sc1\n"
    "s_add_u32 s0, s0, 1\n"
    "s_waitcnt vmcnt(0)\n"
    "v_cmp_eq_u32 vcc, 0, v43\n"
    "s_cmp_gt_u32 s0, 2000000\n"
    "s_cbranch_scc1 L_h_ret_%=\n"
    "s_cbranch_vccnz L_h_%=\n"
    "s_branch L_h_ret_%=\n"
    "L_h2_%=:\n"
    "s_sleep 4\n"
    "buffer_load_dword v43, v25, s[28:31], s99 offen offset:256 sc1\n"
    "s_add_u32 s0, s0, 1\n"
    "s_waitcnt vmcnt(0)\n"
    "v_cmp_eq_u32 vcc, 0, v43\n"
    "s_cmp_gt_u32 s0, 2000000\n"
    "s_cbranch_scc1 L_h2_ret_%=\n"
    "s_cbranch_vccnz L_h2_%=\n"
    "s_branch L_h2_ret_%=\n"
    "L_o_%=:\n"
    "s_sleep 4\n"
    "buffer_load_dword v43, v25, s[28:31], s99 offen offset:384 sc1\n"
    "s_add_u32 s0, s0, 1\n"
    "s_waitcnt vmcnt(0)\n"
    "v_cmp_eq_u32 vcc, 0, v43\n"
    "s_cmp_gt_u32 s0, 2000000\n"
    "s_cbranch_scc1 L_o_ret_%=\n"
    "s_cbranch_vccnz L_o_%=\n"
    "s_branch L_o_ret_%=\n"
    "L_m5_%=:\n"
    "s_sleep 4\n"
    "buffer_load_dword v62, v18, s[28:31], 0 offen offset:512 sc1\n"
    "s_add_u32 s0, s0, 1\n"
    "s_waitcnt vmcnt(0)\n"
    "v_cmp_eq_u32 vcc, 0, v62\n"
    "s_cmp_gt_u32 s0, 2000000\n"
    "s_cbranch_scc1 L_m5_ret_%=\n"
    "s_cbranch_vccnz L_m5_%=\n"
    "s_branch L_m5_ret_%=\n"
    "L_m6_%=:\n"
    "s_sleep 4\n"
    "buffer_load_dword v43, v55, s[28:31], 0 offen offset:640 sc1\n"
    "s_add_u32 s0, s0, 1\n"
    "s_waitcnt vmcnt(0)\n"
    "v_cmp_eq_u32 vcc, 0, v43\n"
    "s_cmp_gt_u32 s0, 2000000\n"
    "s_cbranch_scc1 L_m6_ret_%=\n"
    "s_cbranch_vccnz L_m6_%=\n"
    "s_branch L_m6_ret_%=\n"
    "L_m7_%=:\n"
    "s_sleep 4\n"
    "buffer_load_dword v62, v55, s[28:31], 0 offen offset:768 sc1\n"
    "s_add_u32 s0, s0, 1\n"
    "s_waitcnt vmcnt(0)\n"
    "v_cmp_eq_u32 vcc, 0, v62\n"
    "s_cmp_gt_u32 s0, 2000000\n"
    "s_cbranch_scc1 L_m7_ret_%=\n"
    "s_cbranch_vccnz L_m7_%=\n"
    "s_branch L_m7_ret_%=\n"
    "L_rb_slow_%=:\n"
    "s_add_u32 s0, s0, 1\n"
    "s_cmp_gt_u32 s0, 2000000\n"
    "s_cbranch_scc1 L_rb_ret_%=\n"
    "v_mov_b32 v15, -1\n"
    "v_accvgpr_read_b32 v16, a30\n"
    "v_xor_b32 v16, 0x7fa57fa5, v16\n"
    "v_min_u32 v15, v15, v16\n"
    "v_accvgpr_read_b32 v16, a31\n"
    "v_xor_b32 v16, 0x7fa57fa5, v16\n"
    "v_min_u32 v15, v15, v16\n"
    "v_accvgpr_read_b32 v16, a32\n"
    "v_xor_b32 v16, 0x7fa57fa5, v16\n"
    "v_min_u32 v15, v15, v16\n"
    "v_accvgpr_read_b32 v16, a33\n"
    "v_xor_b32 v16, 0x7fa57fa5, v16\n"
    "v_min_u32 v15, v15, v16\n"
    "v_accvgpr_read_b32 v16, a38\n"
    "v_xor_b32 v16, 0x7fa57fa5, v16\n"
    "v_min_u32 v15, v15, v16\n"
    "v_accvgpr_read_b32 v16, a39\n"
    "v_xor_b32 v16, 0x7fa57fa5, v16\n"
    "v_min_u32 v15, v15, v16\n"
    "v_accvgpr_read_b32 v16, a40\n"
    "v_xor_b32 v16, 0x7fa57fa5, v16\n"
    "v_min_u32 v15, v15, v16\n"
    "v_accvgpr_read_b32 v16, a41\n"
    "v_xor_b32 v16, 0x7fa57fa5, v16\n"
    "v_min_u32 v15, v15, v16\n"
    "v_accvgpr_read_b32 v16, a24\n"
    "v_xor_b32 v16, 0x7fa57fa5, v16\n"
    "v_min_u32 v15, v15, v16\n"
    "v_accvgpr_read_b32 v16, a25\n"
    "v_xor_b32 v16, 0x7fa57fa5, v16\n"
    "v_min_u32 v15, v15, v16\n"
    "v_accvgpr_read_b32 v16, a26\n"
    "v_xor_b32 v16, 0x7fa57fa5, v16\n"
    "v_min_u32 v15, v15, v16\n"
    "v_accvgpr_read_b32 v16, a27\n"
    "v_xor_b32 v16, 0x7fa57fa5, v16\n"
    "v_min_u32 v15, v15, v16\n"
    "v_accvgpr_read_b32 v16, a6\n"
    "v_xor_b32 v16, 0x7fa57fa5, v16\n"
    "v_min_u32 v15, v15, v16\n"
    "v_accvgpr_read_b32 v16, a7\n"
    "v_xor_b32 v16, 0x7fa57fa5, v16\n"
    "v_min_u32 v15, v15, v16\n"
    "v_accvgpr_read_b32 v16, a8\n"
    "v_xor_b32 v16, 0x7fa57fa5, v16\n"
    "v_min_u32 v15, v15, v16\n"
    "v_accvgpr_read_b32 v16, a9\n"
    "v_xor_b32 v16, 0x7fa57fa5, v16\n"
    "v_min_u32 v15, v15, v16\n"
    "v_cmp_eq_u32 vcc, 0, v15\n"
    "s_cbranch_vccz L_rb_ret_%=\n"
    "s_add_u32 s81, s53, 40960\n"
    "buffer_load_dwordx4 a[30:33], v28, s[28:31], s81 offen offset:0 sc1\n"
    "buffer_load_dwordx4 a[38:41], v28, s[28:31], s81 offen offset:1024 sc1\n"
    "s_add_u32 s81, s53, 49152\n"
    "buffer_load_dwordx4 a[24:27], v28, s[28:31], s81 offen offset:0 sc1\n"
    "buffer_load_dwordx4 a[6:9], v28, s[28:31], s81 offen offset:1024 sc1\n"
    "s_waitcnt vmcnt(0)\n"
    "s_branch L_rb_%=\n"
    "L_end_%=:\n"
    "L_fb_%=:\n"
    "s_cmp_lg_u32 s1, 0\n"
    "s_cbranch_scc1 L_fb_ret_%=\n"
    "s_mov_b32 s1, 1\n"
    "s_waitcnt vmcnt(0) lgkmcnt(0)\n"
    "s_nop 15\n"
    "v_mov_b32 v116, 0xf149f2ca\n"
    "v_mov_b32 v117, 0xf149f2ca\n"
    "v_mov_b32 v118, 0xf149f2ca\n"
    "v_mov_b32 v119, 0xf149f2ca\n"
    "ds_read_b64 a[70:71], v18 offset:0\n"
    "ds_read_b64 a[72:73], v44 offset:0\n"
    "ds_read_b64 a[74:75], v18 offset:8\n"
    "s_waitcnt lgkmcnt(0)\n"
    "v_mfma_f32_16x16x32_bf16 v[92:95], a[70:73], v[212:215], v[236:239]\n"
    "v_mfma_f32_16x16x32_bf16 v[100:103], a[72:75], v[220:223], v[236:239]\n"
    "s_nop 8\n"
    "v_max3_f32 v116, v116, v92, v93\n"
    "v_max3_f32 v116, v116, v94, v95\n"
    "v_max3_f32 v118, v118, v100, v101\n"
    "v_max3_f32 v118, v118, v102, v103\n"
    "s_nop 1\n"
    "ds_read_b64 a[70:71], v18 offset:1280\n"
    "ds_read_b64 a[72:73], v44 offset:1280\n"
    "ds_read_b64 a[74:75], v18 offset:1288\n"
    "s_waitcnt lgkmcnt(0)\n"
    "v_mfma_f32_16x16x32_bf16 v[92:95], a[70:73], v[212:215], 0\n"
    "v_mfma_f32_16x16x32_bf16 v[96:99], a[70:73], v[216:219], v[236:239]\n"
    "v_mfma_f32_16x16x32_bf16 v[100:103], a[72:75], v[220:223], 0\n"
    "v_mfma_f32_16x16x32_bf16 v[104:107], a[72:75], v[232:235], v[236:239]\n"
    "s_nop 8\n"
    "v_max3_f32 v116, v116, v92, v93\n"
    "v_max3_f32 v116, v116, v94, v95\n"
    "v_max3_f32 v117, v117, v96, v97\n"
    "v_max3_f32 v117, v117, v98, v99\n"
    "v_max3_f32 v118, v118, v100, v101\n"
    "v_max3_f32 v118, v118, v102, v103\n"
    "v_max3_f32 v119, v119, v104, v105\n"
    "v_max3_f32 v119, v119, v106, v107\n"
    "s_nop 1\n"
    "ds_read_b64 a[70:71], v18 offset:2560\n"
    "ds_read_b64 a[72:73], v44 offset:2560\n"
    "ds_read_b64 a[74:75], v18 offset:2568\n"
    "s_waitcnt lgkmcnt(0)\n"
    "v_mfma_f32_16x16x32_bf16 v[92:95], a[70:73], v[212:215], 0\n"
    "v_mfma_f32_16x16x32_bf16 v[96:99], a[70:73], v[216:219], 0\n"
    "v_mfma_f32_16x16x32_bf16 v[100:103], a[72:75], v[220:223], 0\n"
    "v_mfma_f32_16x16x32_bf16 v[104:107], a[72:75], v[232:235], 0\n"
    "s_nop 8\n"
    "v_max3_f32 v116, v116, v92, v93\n"
    "v_max3_f32 v116, v116, v94, v95\n"
    "v_max3_f32 v117, v117, v96, v97\n"
    "v_max3_f32 v117, v117, v98, v99\n"
    "v_max3_f32 v118, v118, v100, v101\n"
    "v_max3_f32 v118, v118, v102, v103\n"
    "v_max3_f32 v119, v119, v104, v105\n"
    "v_max3_f32 v119, v119, v106, v107\n"
    "s_nop 1\n"
    "ds_read_b64 a[70:71], v18 offset:3840\n"
    "ds_read_b64 a[72:73], v44 offset:3840\n"
    "ds_read_b64 a[74:75], v18 offset:3848\n"
    "s_waitcnt lgkmcnt(0)\n"
    "v_mfma_f32_16x16x32_bf16 v[92:95], a[70:73], v[212:215], 0\n"
    "v_mfma_f32_16x16x32_bf16 v[96:99], a[70:73], v[216:219], 0\n"
    "v_mfma_f32_16x16x32_bf16 v[100:103], a[72:75], v[220:223], 0\n"
    "v_mfma_f32_16x16x32_bf16 v[104:107], a[72:75], v[232:235], 0\n"
    "s_nop 8\n"
    "v_max3_f32 v116, v116, v92, v93\n"
    "v_max3_f32 v116, v116, v94, v95\n"
    "v_max3_f32 v117, v117, v96, v97\n"
    "v_max3_f32 v117, v117, v98, v99\n"
    "v_max3_f32 v118, v118, v100, v101\n"
    "v_max3_f32 v118, v118, v102, v103\n"
    "v_max3_f32 v119, v119, v104, v105\n"
    "v_max3_f32 v119, v119, v106, v107\n"
    "s_nop 1\n"
    "ds_read_b64 a[70:71], v18 offset:5120\n"
    "ds_read_b64 a[72:73], v44 offset:5120\n"
    "ds_read_b64 a[74:75], v18 offset:5128\n"
    "s_waitcnt lgkmcnt(0)\n"
    "v_mfma_f32_16x16x32_bf16 v[92:95], a[70:73], v[212:215], 0\n"
    "v_mfma_f32_16x16x32_bf16 v[96:99], a[70:73], v[216:219], 0\n"
    "v_mfma_f32_16x16x32_bf16 v[100:103], a[72:75], v[220:223], 0\n"
    "v_mfma_f32_16x16x32_bf16 v[104:107], a[72:75], v[232:235], 0\n"
    "s_nop 8\n"
    "v_max3_f32 v116, v116, v92, v93\n"
    "v_max3_f32 v116, v116, v94, v95\n"
    ".p2align 12\n"
    "L_pg12_%=:\n"
    "s_cbranch_execz L_tramp_%=\n"
    "v_max3_f32 v117, v117, v96, v97\n"
    "v_max3_f32 v117, v117, v98, v99\n"
    "v_max3_f32 v118, v118, v100, v101\n"
    "v_max3_f32 v118, v118, v102, v103\n"
    "v_max3_f32 v119, v119, v104, v105\n"
    "v_max3_f32 v119, v119, v106, v107\n"
    "s_nop 1\n"
    "ds_read_b64 a[70:71], v18 offset:6400\n"
    "ds_read_b64 a[72:73], v44 offset:6400\n"
    "ds_read_b64 a[74:75], v18 offset:6408\n"
    "s_waitcnt lgkmcnt(0)\n"
    "v_mfma_f32_16x16x32_bf16 v[92:95], a[70:73], v[212:215], 0\n"
    "v_mfma_f32_16x16x32_bf16 v[96:99], a[70:73], v[216:219], 0\n"
    "v_mfma_f32_16x16x32_bf16 v[100:103], a[72:75], v[220:223], 0\n"
    "v_mfma_f32_16x16x32_bf16 v[104:107], a[72:75], v[232:235], 0\n"
    "s_nop 8\n"
    "v_max3_f32 v116, v116, v92, v93\n"
    "v_max3_f32 v116, v116, v94, v95\n"
    "v_max3_f32 v117, v117, v96, v97\n"
    "v_max3_f32 v117, v117, v98, v99\n"
    "v_max3_f32 v118, v118, v100, v101\n"
    "v_max3_f32 v118, v118, v102, v103\n"
    "v_max3_f32 v119, v119, v104, v105\n"
    "v_max3_f32 v119, v119, v106, v107\n"
    "s_nop 1\n"
    "ds_read_b64 a[70:71], v18 offset:7680\n"
    "ds_read_b64 a[72:73], v44 offset:7680\n"
    "ds_read_b64 a[74:75], v18 offset:7688\n"
    "s_waitcnt lgkmcnt(0)\n"
    "v_mfma_f32_16x16x32_bf16 v[92:95], a[70:73], v[212:215], 0\n"
    "v_mfma_f32_16x16x32_bf16 v[96:99], a[70:73], v[216:219], 0\n"
    "v_mfma_f32_16x16x32_bf16 v[100:103], a[72:75], v[220:223], 0\n"
    "v_mfma_f32_16x16x32_bf16 v[104:107], a[72:75], v[232:235], 0\n"
    "s_nop 8\n"
    "v_max3_f32 v116, v116, v92, v93\n"
    "v_max3_f32 v116, v116, v94, v95\n"
    "v_max3_f32 v117, v117, v96, v97\n"
    "v_max3_f32 v117, v117, v98, v99\n"
    "v_max3_f32 v118, v118, v100, v101\n"
    "v_max3_f32 v118, v118, v102, v103\n"
    "v_max3_f32 v119, v119, v104, v105\n"
    "v_max3_f32 v119, v119, v106, v107\n"
    "s_nop 1\n"
    "ds_read_b64 a[70:71], v18 offset:8960\n"
    "ds_read_b64 a[72:73], v44 offset:8960\n"
    "ds_read_b64 a[74:75], v18 offset:8968\n"
    "s_waitcnt lgkmcnt(0)\n"
    "v_mfma_f32_16x16x32_bf16 v[92:95], a[70:73], v[212:215], 0\n"
    "v_mfma_f32_16x16x32_bf16 v[96:99], a[70:73], v[216:219], 0\n"
    "v_mfma_f32_16x16x32_bf16 v[100:103], a[72:75], v[220:223], 0\n"
    "v_mfma_f32_16x16x32_bf16 v[104:107], a[72:75], v[232:235], 0\n"
    "s_nop 8\n"
    "v_max3_f32 v116, v116, v92, v93\n"
    "v_max3_f32 v116, v116, v94, v95\n"
    "v_max3_f32 v117, v117, v96, v97\n"
    "v_max3_f32 v117, v117, v98, v99\n"
    "v_max3_f32 v118, v118, v100, v101\n"
    "v_max3_f32 v118, v118, v102, v103\n"
    "v_max3_f32 v119, v119, v104, v105\n"
    "v_max3_f32 v119, v119, v106, v107\n"
    "s_nop 1\n"
    "ds_read_b64 a[70:71], v18 offset:10240\n"
    "ds_read_b64 a[72:73], v44 offset:10240\n"
    "ds_read_b64 a[74:75], v18 offset:10248\n"
    "s_waitcnt lgkmcnt(0)\n"
    "v_mfma_f32_16x16x32_bf16 v[92:95], a[70:73], v[212:215], 0\n"
    "v_mfma_f32_16x16x32_bf16 v[96:99], a[70:73], v[216:219], 0\n"
    "v_mfma_f32_16x16x32_bf16 v[100:103], a[72:75], v[220:223], 0\n"
    "v_mfma_f32_16x16x32_bf16 v[104:107], a[72:75], v[232:235], 0\n"
    "s_nop 8\n"
    "v_max3_f32 v116, v116, v92, v93\n"
    "v_max3_f32 v116, v116, v94, v95\n"
    "v_max3_f32 v117, v117, v96, v97\n"
    "v_max3_f32 v117, v117, v98, v99\n"
    "v_max3_f32 v118, v118, v100, v101\n"
    "v_max3_f32 v118, v118, v102, v103\n"
    "v_max3_f32 v119, v119, v104, v105\n"
    "v_max3_f32 v119, v119, v106, v107\n"
    "s_nop 1\n"
    "ds_read_b64 a[70:71], v18 offset:11520\n"
    "ds_read_b64 a[72:73], v44 offset:11520\n"
    "ds_read_b64 a[74:75], v18 offset:11528\n"
    "s_waitcnt lgkmcnt(0)\n"
    "v_mfma_f32_16x16x32_bf16 v[92:95], a[70:73], v[212:215], 0\n"
    "v_mfma_f32_16x16x32_bf16 v[96:99], a[70:73], v[216:219], 0\n"
    "v_mfma_f32_16x16x32_bf16 v[100:103], a[72:75], v[220:223], 0\n"
    "v_mfma_f32_16x16x32_bf16 v[104:107], a[72:75], v[232:235], 0\n"
    "s_nop 8\n"
    "v_max3_f32 v116, v116, v92, v93\n"
    "v_max3_f32 v116, v116, v94, v95\n"
    "v_max3_f32 v117, v117, v96, v97\n"
    "v_max3_f32 v117, v117, v98, v99\n"
    "v_max3_f32 v118, v118, v100, v101\n"
    "v_max3_f32 v118, v118, v102, v103\n"
    "v_max3_f32 v119, v119, v104, v105\n"
    "v_max3_f32 v119, v119, v106, v107\n"
    "s_nop 1\n"
    "ds_read_b64 a[70:71], v18 offset:12800\n"
    "ds_read_b64 a[72:73], v44 offset:12800\n"
    "ds_read_b64 a[74:75], v18 offset:12808\n"
    "s_waitcnt lgkmcnt(0)\n"
    "v_mfma_f32_16x16x32_bf16 v[92:95], a[70:73], v[212:215], 0\n"
    "v_mfma_f32_16x16x32_bf16 v[96:99], a[70:73], v[216:219], 0\n"
    "v_mfma_f32_16x16x32_bf16 v[100:103], a[72:75], v[220:223], 0\n"
    "v_mfma_f32_16x16x32_bf16 v[104:107], a[72:75], v[232:235], 0\n"
    "s_nop 8\n"
    "v_max3_f32 v116, v116, v92, v93\n"
    "v_max3_f32 v116, v116, v94, v95\n"
    "v_max3_f32 v117, v117, v96, v97\n"
    "v_max3_f32 v117, v117, v98, v99\n"
    "v_max3_f32 v118, v118, v100, v101\n"
    "v_max3_f32 v118, v118, v102, v103\n"
    "v_max3_f32 v119, v119, v104, v105\n"
    "v_max3_f32 v119, v119, v106, v107\n"
    "s_nop 1\n"
    "ds_read_b64 a[70:71], v18 offset:14080\n"
    "ds_read_b64 a[72:73], v44 offset:14080\n"
    "ds_read_b64 a[74:75], v18 offset:14088\n"
    "s_waitcnt lgkmcnt(0)\n"
    "v_mfma_f32_16x16x32_bf16 v[92:95], a[70:73], v[212:215], 0\n"
    "v_mfma_f32_16x16x32_bf16 v[96:99], a[70:73], v[216:219], 0\n"
    "v_mfma_f32_16x16x32_bf16 v[100:103], a[72:75], v[220:223], 0\n"
    "v_mfma_f32_16x16x32_bf16 v[104:107], a[72:75], v[232:235], 0\n"
    "s_nop 8\n"
    "v_max3_f32 v116, v116, v92, v93\n"
    "v_max3_f32 v116, v116, v94, v95\n"
    "v_max3_f32 v117, v117, v96, v97\n"
    "v_max3_f32 v117, v117, v98, v99\n"
    "v_max3_f32 v118, v118, v100, v101\n"
    "v_max3_f32 v118, v118, v102, v103\n"
    "v_max3_f32 v119, v119, v104, v105\n"
    "v_max3_f32 v119, v119, v106, v107\n"
    "s_nop 1\n"
    "ds_read_b64 a[70:71], v18 offset:15360\n"
    "ds_read_b64 a[72:73], v44 offset:15360\n"
    "ds_read_b64 a[74:75], v18 offset:15368\n"
    "s_waitcnt lgkmcnt(0)\n"
    "v_mfma_f32_16x16x32_bf16 v[92:95], a[70:73], v[212:215], 0\n"
    "v_mfma_f32_16x16x32_bf16 v[96:99], a[70:73], v[216:219], 0\n"
    "v_mfma_f32_16x16x32_bf16 v[100:103], a[72:75], v[220:223], 0\n"
    "v_mfma_f32_16x16x32_bf16 v[104:107], a[72:75], v[232:235], 0\n"
    "s_nop 8\n"
    "v_max3_f32 v116, v116, v92, v93\n"
    "v_max3_f32 v116, v116, v94, v95\n"
    "v_max3_f32 v117, v117, v96, v97\n"
    "v_max3_f32 v117, v117, v98, v99\n"
    "v_max3_f32 v118, v118, v100, v101\n"
    "v_max3_f32 v118, v118, v102, v103\n"
    "v_max3_f32 v119, v119, v104, v105\n"
    "v_max3_f32 v119, v119, v106, v107\n"
    "s_nop 1\n"
    "ds_read_b64 a[70:71], v18 offset:16640\n"
    "ds_read_b64 a[72:73], v44 offset:16640\n"
    "ds_read_b64 a[74:75], v18 offset:16648\n"
    "s_waitcnt lgkmcnt(0)\n"
    "v_mfma_f32_16x16x32_bf16 v[92:95], a[70:73], v[212:215], 0\n"
    "v_mfma_f32_16x16x32_bf16 v[96:99], a[70:73], v[216:219], 0\n"
    "v_mfma_f32_16x16x32_bf16 v[100:103], a[72:75], v[220:223], 0\n"
    "v_mfma_f32_16x16x32_bf16 v[104:107], a[72:75], v[232:235], 0\n"
    "s_nop 8\n"
    "v_max3_f32 v116, v116, v92, v93\n"
    "v_max3_f32 v116, v116, v94, v95\n"
    "v_max3_f32 v117, v117, v96, v97\n"
    "v_max3_f32 v117, v117, v98, v99\n"
    "v_max3_f32 v118, v118, v100, v101\n"
    "v_max3_f32 v118, v118, v102, v103\n"
    "v_max3_f32 v119, v119, v104, v105\n"
    "v_max3_f32 v119, v119, v106, v107\n"
    "s_nop 1\n"
    "ds_read_b64 a[70:71], v18 offset:17920\n"
    "ds_read_b64 a[72:73], v44 offset:17920\n"
    "ds_read_b64 a[74:75], v18 offset:17928\n"
    "s_waitcnt lgkmcnt(0)\n"
    "v_mfma_f32_16x16x32_bf16 v[92:95], a[70:73], v[212:215], 0\n"
    "v_mfma_f32_16x16x32_bf16 v[96:99], a[70:73], v[216:219], 0\n"
    "v_mfma_f32_16x16x32_bf16 v[100:103], a[72:75], v[220:223], 0\n"
    "v_mfma_f32_16x16x32_bf16 v[104:107], a[72:75], v[232:235], 0\n"
    "s_nop 8\n"
    "v_max3_f32 v116, v116, v92, v93\n"
    "v_max3_f32 v116, v116, v94, v95\n"
    "v_max3_f32 v117, v117, v96, v97\n"
    "v_max3_f32 v117, v117, v98, v99\n"
    "v_max3_f32 v118, v118, v100, v101\n"
    "v_max3_f32 v118, v118, v102, v103\n"
    "v_max3_f32 v119, v119, v104, v105\n"
    "v_max3_f32 v119, v119, v106, v107\n"
    "s_nop 1\n"
    "ds_read_b64 a[70:71], v18 offset:19200\n"
    "ds_read_b64 a[72:73], v44 offset:19200\n"
    "ds_read_b64 a[74:75], v18 offset:19208\n"
    "s_waitcnt lgkmcnt(0)\n"
    "v_mfma_f32_16x16x32_bf16 v[92:95], a[70:73], v[212:215], 0\n"
    "v_mfma_f32_16x16x32_bf16 v[96:99], a[70:73], v[216:219], 0\n"
    "v_mfma_f32_16x16x32_bf16 v[100:103], a[72:75], v[220:223], 0\n"
    "v_mfma_f32_16x16x32_bf16 v[104:107], a[72:75], v[232:235], 0\n"
    "s_nop 8\n"
    "v_max3_f32 v116, v116, v92, v93\n"
    "v_max3_f32 v116, v116, v94, v95\n"
    "v_max3_f32 v117, v117, v96, v97\n"
    "v_max3_f32 v117, v117, v98, v99\n"
    "v_max3_f32 v118, v118, v100, v101\n"
    "v_max3_f32 v118, v118, v102, v103\n"
    "v_max3_f32 v119, v119, v104, v105\n"
    "v_max3_f32 v119, v119, v106, v107\n"
    "s_nop 1\n"
    "ds_read_b64 a[70:71], v18 offset:20480\n"
    "ds_read_b64 a[72:73], v44 offset:20480\n"
    "ds_read_b64 a[74:75], v18 offset:20488\n"
    "s_waitcnt lgkmcnt(0)\n"
    "v_mfma_f32_16x16x32_bf16 v[92:95], a[70:73], v[212:215], v[248:251]\n"
    "v_mfma_f32_16x16x32_bf16 v[96:99], a[70:73], v[216:219], 0\n"
    "v_mfma_f32_16x16x32_bf16 v[100:103], a[72:75], v[220:223], v[248:251]\n"
    "v_mfma_f32_16x16x32_bf16 v[104:107], a[72:75], v[232:235], 0\n"
    "s_nop 8\n"
    "v_max3_f32 v116, v116, v92, v93\n"
    "v_max3_f32 v116, v116, v94, v95\n"
    "v_max3_f32 v117, v117, v96, v97\n"
    "v_max3_f32 v117, v117, v98, v99\n"
    "v_max3_f32 v118, v118, v100, v101\n"
    "v_max3_f32 v118, v118, v102, v103\n"
    "v_max3_f32 v119, v119, v104, v105\n"
    "v_max3_f32 v119, v119, v106, v107\n"
    "s_nop 1\n"
    "ds_read_b64 a[70:71], v18 offset:21760\n"
    "ds_read_b64 a[72:73], v44 offset:21760\n"
    "ds_read_b64 a[74:75], v18 offset:21768\n"
    "s_waitcnt lgkmcnt(0)\n"
    "v_mfma_f32_16x16x32_bf16 v[96:99], a[70:73], v[216:219], v[248:251]\n"
    "v_mfma_f32_16x16x32_bf16 v[104:107], a[72:75], v[232:235], v[248:251]\n"
    "s_nop 8\n"
    "v_max3_f32 v117, v117, v96, v97\n"
    "v_max3_f32 v117, v117, v98, v99\n"
    "v_max3_f32 v119, v119, v104, v105\n"
    "v_max3_f32 v119, v119, v106, v107\n"
    "s_nop 1\n"
    "v_xor_b32 v120, 16, v1\n"
    "v_lshlrev_b32 v120, 2, v120\n"
    "ds_bpermute_b32 v121, v120, v116\n"
    "s_waitcnt lgkmcnt(0)\n"
    "v_max_f32 v116, v116, v121\n"
    "ds_bpermute_b32 v121, v120, v117\n"
    "s_waitcnt lgkmcnt(0)\n"
    "v_max_f32 v117, v117, v121\n"
    "ds_bpermute_b32 v121, v120, v118\n"
    "s_waitcnt lgkmcnt(0)\n"
    "v_max_f32 v118, v118, v121\n"
    "ds_bpermute_b32 v121, v120, v119\n"
    "s_waitcnt lgkmcnt(0)\n"
    "v_max_f32 v119, v119, v121\n"
    "v_xor_b32 v120, 32, v1\n"
    "v_lshlrev_b32 v120, 2, v120\n"
    "ds_bpermute_b32 v121, v120, v116\n"
    "s_waitcnt lgkmcnt(0)\n"
    "v_max_f32 v116, v116, v121\n"
    "ds_bpermute_b32 v121, v120, v117\n"
    "s_waitcnt lgkmcnt(0)\n"
    "v_max_f32 v117, v117, v121\n"
    "ds_bpermute_b32 v121, v120, v118\n"
    "s_waitcnt lgkmcnt(0)\n"
    "v_max_f32 v118, v118, v121\n"
    "ds_bpermute_b32 v121, v120, v119\n"
    "s_waitcnt lgkmcnt(0)\n"
    "v_max_f32 v119, v119, v121\n"
    "v_sub_f32 v116, 0, v116\n"
    "v_and_b32 v120, 0xffff0000, v116\n"
    "v_sub_f32 v116, v116, v120\n"
    "v_and_b32 v121, 0xffff0000, v116\n"
    "v_sub_f32 v116, v116, v121\n"
    "v_lshrrev_b32 v120, 16, v120\n"
    "v_or_b32 v120, v120, v121\n"
    "v_lshrrev_b32 v116, 16, v116\n"
    "v_or_b32 v116, 1.0, v116\n"
    "v_cndmask_b32 v214, v214, v120, s[72:73]\n"
    "v_cndmask_b32 v215, v215, v116, s[72:73]\n"
    "v_sub_f32 v117, 0, v117\n"
    "v_and_b32 v120, 0xffff0000, v117\n"
    "v_sub_f32 v117, v117, v120\n"
    "v_and_b32 v121, 0xffff0000, v117\n"
    "v_sub_f32 v117, v117, v121\n"
    "v_lshrrev_b32 v120, 16, v120\n"
    "v_or_b32 v120, v120, v121\n"
    "v_lshrrev_b32 v117, 16, v117\n"
    "v_or_b32 v117, 1.0, v117\n"
    "v_cndmask_b32 v218, v218, v120, s[72:73]\n"
    "v_cndmask_b32 v219, v219, v117, s[72:73]\n"
    "v_sub_f32 v118, 0, v118\n"
    "v_and_b32 v120, 0xffff0000, v118\n"
    "v_sub_f32 v118, v118, v120\n"
    "v_and_b32 v121, 0xffff0000, v118\n"
    "v_sub_f32 v118, v118, v121\n"
    "v_lshrrev_b32 v120, 16, v120\n"
    "v_or_b32 v120, v120, v121\n"
    "v_lshrrev_b32 v118, 16, v118\n"
    "v_or_b32 v118, 1.0, v118\n"
    "v_cndmask_b32 v220, v220, v120, s[72:73]\n"
    "v_cndmask_b32 v221, v221, v118, s[72:73]\n"
    "v_sub_f32 v119, 0, v119\n"
    "v_and_b32 v120, 0xffff0000, v119\n"
    "v_sub_f32 v119, v119, v120\n"
    "v_and_b32 v121, 0xffff0000, v119\n"
    "v_sub_f32 v119, v119, v121\n"
    "v_lshrrev_b32 v120, 16, v120\n"
    "v_or_b32 v120, v120, v121\n"
    "v_lshrrev_b32 v119, 16, v119\n"
    "v_or_b32 v119, 1.0, v119\n"
    "v_cndmask_b32 v232, v232, v120, s[72:73]\n"
    "v_cndmask_b32 v233, v233, v119, s[72:73]\n"
    "ds_read_b64 a[70:71], v18 offset:0\n"
    "ds_read_b64 a[72:73], v44 offset:0\n"
    "ds_read_b64 a[74:75], v18 offset:8\n"
    "ds_read_b64 a[76:77], v18 offset:1280\n"
    "ds_read_b64 a[78:79], v44 offset:1280\n"
    "ds_read_b64 a[80:81], v18 offset:1288\n"
    "ds_read_b64_tr_b16 a[94:95], v45 offset:0\n"
    "ds_read_b64_tr_b16 a[96:97], v45 offset:1280\n"
    "ds_read_b64_tr_b16 a[98:99], v85 offset:0\n"
    "ds_read_b64_tr_b16 a[100:101], v85 offset:1280\n"
    "s_waitcnt lgkmcnt(0)\n"
    "s_nop 15\n"
    "s_branch L_att_%=\n"
    :: "s"(__builtin_amdgcn_kernarg_segment_ptr()), "s"(blockIdx.x), "v"(threadIdx.x), "s"((unsigned)(size_t)lds)
    : "v1","v2","v3","v4","v5","v6","v7","v8","v9","v10","v11","v12","v13","v14","v15","v16","v17","v18","v19","v20","v21","v22","v23","v24","v25","v26","v27","v28","v29","v30","v31","v32","v33","v34","v35","v36","v37","v38","v39","v40","v41","v42","v43","v44","v45","v46","v47","v48","v49","v50","v51","v52","v53","v54","v55","v56","v57","v58","v59","v60","v61","v62","v63","v64","v65","v66","v67","v68","v69","v70","v71","v72","v73","v74","v75","v76","v77","v78","v79","v80","v81","v82","v83","v84","v85","v86","v87","v88","v89","v90","v91","v92","v93","v94","v95","v96","v97","v98","v99","v100","v101","v102","v103","v104","v105","v106","v107","v108","v109","v110","v111","v112","v113","v114","v115","v116","v117","v118","v119","v120","v121","v122","v123","v124","v125","v126","v127","v128","v129","v130","v131","v132","v133","v134","v135","v136","v137","v138","v139","v140","v141","v142","v143","v144","v145","v146","v147","v148","v149","v150","v151","v152","v153","v154","v155","v156","v157","v158","v159","v160","v161","v162","v163","v164","v165","v166","v167","v168","v169","v170","v171","v172","v173","v174","v175","v176","v177","v178","v179","v180","v181","v182","v183","v184","v185","v186","v187","v188","v189","v190","v191","v192","v193","v194","v195","v196","v197","v198","v199","v200","v201","v202","v203","v204","v205","v206","v207","v208","v209","v210","v211","v212","v213","v214","v215","v216","v217","v218","v219","v220","v221","v222","v223","v224","v225","v226","v227","v228","v229","v230","v231","v232","v233","v234","v235","v236","v237","v238","v239","v240","v241","v242","v243","v244","v245","v246","v247","v248","v249","v250","v251","v252","v253","v254","v255","a0","a1","a2","a3","a4","a5","a6","a7","a8","a9","a10","a11","a12","a13","a14","a15","a16","a17","a18","a19","a20","a21","a22","a23","a24","a25","a26","a27","a28","a29","a30","a31","a32","a33","a34","a35","a36","a37","a38","a39","a40","a41","a42","a43","a44","a45","a46","a47","a48","a49","a50","a51","a52","a53","a54","a55","a56","a57","a58","a59","a60","a61","a62","a63","a64","a65","a66","a67","a68","a69","a70","a71","a72","a73","a74","a75","a76","a77","a78","a79","a80","a81","a82","a83","a84","a85","a86","a87","a88","a89","a90","a91","a92","a93","a94","a95","a96","a97","a98","a99","a100","a101","a102","a103","a104","a105","a106","a107","a108","a109","a110","a111","a112","a113","a114","a115","a116","a117","a118","a119","a120","a121","a122","a123","a124","a125","a126","a127","a128","a129","a130","a131","a132","a133","a134","a135","a136","a137","a138","a139","a140","a141","a142","a143","a144","a145","a146","a147","a148","a149","a150","a151","a152","a153","a154","a155","a156","a157","a158","a159","a160","a161","a162","a163","a164","a165","a166","a167","a168","a169","a170","a171","a172","a173","a174","a175","a176","a177","a178","a179","a180","a181","a182","a183","a184","a185","a186","a187","a188","a189","a190","a191","a192","a193","a194","a195","a196","a197","a198","a199","a200","a201","a202","a203","a204","a205","a206","a207","a208","a209","a210","a211","a212","a213","a214","a215","a216","a217","a218","a219","a220","a221","a222","a223","a224","a225","a226","a227","a228","a229","a230","a231","a232","a233","a234","a235","a236","a237","a238","a239","a240","a241","a242","a243","a244","a245","a246","a247","a248","a249","a250","a251","a252","a253","a254","a255","s4","s5","s6","s7","s8","s9","s10","s11","s12","s13","s14","s15","s16","s17","s18","s19","s20","s21","s22","s23","s24","s25","s26","s27","s28","s29","s30","s31","s32","s33","s34","s35","s36","s37","s38","s39","s40","s41","s42","s43","s44","s45","s46","s47","s48","s49","s50","s51","s52","s53","s54","s55","s56","s57","s58","s59","s60","s61","s62","s63","s64","s65","s66","s67","s68","s69","s70","s71","s72","s73","s74","s75","s76","s77","s78","s79","s80","s81","s82","s83","s84","s85","s86","s87","s88","s89","s90","s91","s92","s93","s94","s95","s96","s97","s98","s99","scc","vcc","memory");
}

extern "C" void kernel_launch(void* const* d_in, const int* in_sizes, int n_in, void* d_out, int out_size, void* d_ws, size_t ws_size,
                              hipStream_t stream) {
    parallel_sum__sliding_block__dense_activation_relu__prompt_kernel<<<256, 256, 0, stream>>>(d_in[0], d_in[1], d_in[2], d_in[4], d_in[5], d_out, d_ws);
}
```
